```python
import torch

def _softmax(x, axis=-1):
    return torch.softmax(x, dim=axis)

def _gelu(x):
    return torch.nn.functional.gelu(x, approximate='tanh')

def _silu(x):
    return torch.nn.functional.silu(x)

def _sigmoid(x):
    return torch.sigmoid(x)

def _layernorm(x, axis=-1, eps=1e-05):
    m = x.mean(dim=axis, keepdim=True)
    v = x.var(dim=axis, keepdim=True, unbiased=False)
    return (x - m) / torch.sqrt(v + eps)

def _scatter_add(tgt, idx, src, axis=0):
    return tgt.clone().index_add_(axis, idx, src)

def _segment_sum(src, seg, n):
    out = torch.zeros((n,) + src.shape[1:], dtype=src.dtype)
    return out.index_add_(0, seg, src)

def _pad(x, pw):
    flat = tuple((v for pair in reversed(pw) for v in pair))
    return torch.nn.functional.pad(x, flat)
kernel_name = 'adaLN_cond__sliding_block__swiglu_ffn__prompt'

def setup_inputs():

    _w = lambda a: (lambda t: t.to(torch.bfloat16) if t.is_floating_point() else t)(torch.as_tensor(a))

    def _A_setup():
        B, S, D = (1, 2048, 640)
        return {'x': _w((lambda *s: torch.randn(*s, dtype=torch.bfloat16) * 0.05)(B, S, D)), 'wqkv': _w((lambda *s: torch.randn(*s, dtype=torch.bfloat16) * 0.05)(D, 3 * D)), 'wo': _w((lambda *s: torch.randn(*s, dtype=torch.bfloat16) * 0.05)(D, D))}
    _A_in = _A_setup()

    def _B_setup():
        B, S, D, F = (1, 2048, 640, 1536)
        return {'x': _w((lambda *s: torch.randn(*s, dtype=torch.bfloat16) * 0.05)(B, S, D)), 'w_gate': _w((lambda *s: torch.randn(*s, dtype=torch.bfloat16) * 0.05)(D, F)), 'w_up': _w((lambda *s: torch.randn(*s, dtype=torch.bfloat16) * 0.05)(D, F)), 'w_down': _w((lambda *s: torch.randn(*s, dtype=torch.bfloat16) * 0.05)(F, D))}
    _B_in = _B_setup()
    return {'A_x': _A_in['x'], 'A_wqkv': _A_in['wqkv'], 'A_wo': _A_in['wo'], 'B_x': _B_in['x'], 'B_w_gate': _B_in['w_gate'], 'B_w_up': _B_in['w_up'], 'B_w_down': _B_in['w_down'], 'c': _w((lambda *s: torch.randn(*s, dtype=torch.bfloat16) * 0.1)(1, 640) * 8.0), 'w_mod': _w((lambda *s: torch.randn(*s, dtype=torch.bfloat16) * 0.1)(640, 6 * 640) * 640 ** (-0.5) * 8.0)}

def reference(A_x, A_wqkv, A_wo, B_x, B_w_gate, B_w_up, B_w_down, c, w_mod):

    def _A_ref(x, wqkv, wo):
        B, S, D = x.shape
        H = 32
        Dh = D // H
        W = 256
        qkv = (lambda x, *d: x.permute(*d))((x @ wqkv).reshape(B, S, 3, H, Dh), 2, 0, 3, 1, 4)
        q, k, v = (qkv[0], qkv[1], qkv[2])
        scores = torch.einsum('bhqd,bhkd->bhqk', q, k) * Dh ** (-0.5)
        idx = torch.arange(S)
        mask = (idx[:, None] - idx[None, :] >= 0) & (idx[:, None] - idx[None, :] < W)
        scores = torch.where(mask[None, None], scores, -1000000000.0)
        p = _softmax(scores, axis=-1)
        o = torch.einsum('bhqk,bhkd->bhqd', p, v)
        return (lambda x, *d: x.permute(*d))(o, 0, 2, 1, 3).reshape(B, S, D) @ wo

    def _B_ref(x, w_gate, w_up, w_down):
        g = _silu(x @ w_gate)
        u = x @ w_up
        return g * u @ w_down
    x = A_x
    mod = (_silu(c) @ w_mod).reshape(c.shape[0], 6, 1, -1)
    sa, ca, ga, sb, cb, gb = (mod[:, 0].clone(), mod[:, 1].clone(), mod[:, 2].clone(), mod[:, 3].clone(), mod[:, 4].clone(), mod[:, 5].clone())
    h = _layernorm(x, axis=-1) * (1 + ca) + sa
    x = x + ga * _A_ref(h, A_wqkv, A_wo)
    h = _layernorm(x, axis=-1) * (1 + cb) + sb
    x = x + gb * _B_ref(h, B_w_gate, B_w_up, B_w_down)
    return x
```

```cpp
#include <hip/hip_runtime.h>
#include <hip/hip_bf16.h>
#include <stdint.h>
typedef unsigned short u16;
__device__ __forceinline__ float b2f(u16 v){ return __uint_as_float(((unsigned)v)<<16); }
__device__ __forceinline__ u16 f2b(float f){ __hip_bfloat16 h = __float2bfloat16(f); return *(u16*)&h; }
__device__ __forceinline__ float rb(float f){ return b2f(f2b(f)); }
constexpr int S=2048, D=640, F=1536, H=32, DH=20, W=256;

__global__ void k_mod(const u16* c, const u16* wmod, float* mod){
  int j = blockIdx.x*blockDim.x+threadIdx.x; if(j>=6*D) return;
  float acc=0; for(int k=0;k<D;k++){ float cv=b2f(c[k]); float s=rb(cv/(1.f+__expf(-cv))); acc+=s*b2f(wmod[(size_t)k*6*D+j]); }
  mod[j]=rb(acc);
}
__global__ void k_ln(const u16* x, const float* mod, int sidx, int cidx, u16* h){
  int row=blockIdx.x; __shared__ float red[256];
  float v[3]; float s=0; for(int i=0;i<3;i++){int c=threadIdx.x+i*256; v[i]= c<D? b2f(x[row*D+c]):0.f; s+=v[i];}
  red[threadIdx.x]=s; __syncthreads(); for(int o=128;o>0;o>>=1){ if(threadIdx.x<o) red[threadIdx.x]+=red[threadIdx.x+o]; __syncthreads(); }
  float m=red[0]/D; __syncthreads();
  s=0; for(int i=0;i<3;i++){int c=threadIdx.x+i*256; if(c<D){float d=v[i]-m; s+=d*d;}}
  red[threadIdx.x]=s; __syncthreads(); for(int o=128;o>0;o>>=1){ if(threadIdx.x<o) red[threadIdx.x]+=red[threadIdx.x+o]; __syncthreads(); }
  float r=rsqrtf(red[0]/D+1e-5f);
  for(int i=0;i<3;i++){int c=threadIdx.x+i*256; if(c<D){ float n=(v[i]-m)*r; h[row*D+c]=f2b(n*(1.f+mod[cidx*D+c])+mod[sidx*D+c]); }}
}
__global__ void k_gemm(const u16* A, const u16* B, u16* C, int M,int N,int K){
  int n=blockIdx.x*blockDim.x+threadIdx.x, m=blockIdx.y; if(n>=N) return;
  float acc=0; for(int k=0;k<K;k++) acc+=b2f(A[(size_t)m*K+k])*b2f(B[(size_t)k*N+n]);
  C[(size_t)m*N+n]=f2b(acc);
}
__global__ void k_attn(const u16* qkv, u16* o){
  int q=blockIdx.x, h=blockIdx.y, t=threadIdx.x; __shared__ float p[256]; __shared__ float red[256];
  int key=q-t; float s=-1e30f;
  if(key>=0){ float a=0; for(int d=0;d<DH;d++) a+=b2f(qkv[(size_t)q*3*D+h*DH+d])*b2f(qkv[(size_t)key*3*D+D+h*DH+d]); s=a*0.22360679775f; }
  red[t]=s; __syncthreads(); for(int o2=128;o2>0;o2>>=1){ if(t<o2) red[t]=fmaxf(red[t],red[t+o2]); __syncthreads(); }
  float m=red[0]; __syncthreads(); float e= key>=0? __expf(s-m):0.f; p[t]=e; red[t]=e; __syncthreads();
  for(int o2=128;o2>0;o2>>=1){ if(t<o2) red[t]+=red[t+o2]; __syncthreads(); }
  float l=red[0];
  if(t<DH){ float a=0; for(int j=0;j<256;j++){ int kk=q-j; if(kk<0) break; a+=p[j]*b2f(qkv[(size_t)kk*3*D+2*D+h*DH+t]); } o[(size_t)q*D+h*DH+t]=f2b(a/l); }
}
__global__ void k_res(const u16* x, const u16* y, const float* mod, int gidx, u16* out){
  int i=blockIdx.x*blockDim.x+threadIdx.x; if(i>=S*D) return; int c=i%D;
  out[i]=f2b(b2f(x[i])+mod[gidx*D+c]*b2f(y[i]));
}
__global__ void k_act(const u16* g, const u16* u, u16* a){
  int i=blockIdx.x*blockDim.x+threadIdx.x; if(i>=S*F) return; float gv=b2f(g[i]);
  a[i]=f2b(gv/(1.f+__expf(-gv))*b2f(u[i]));
}
extern "C" void kernel_launch(void* const* d_in, const int* in_sizes, int n_in, void* d_out, int out_size, void* d_ws, size_t ws_size, hipStream_t stream){
  const u16* x=(const u16*)d_in[0]; const u16* wqkv=(const u16*)d_in[1]; const u16* wo=(const u16*)d_in[2];
  const u16* wg=(const u16*)d_in[4]; const u16* wu=(const u16*)d_in[5]; const u16* wd=(const u16*)d_in[6];
  const u16* c=(const u16*)d_in[7]; const u16* wm=(const u16*)d_in[8];
  char* ws=(char*)d_ws; float* mod=(float*)ws; u16* h=(u16*)(ws+(1<<20)); u16* qkv=(u16*)(ws+(8<<20)); u16* o=(u16*)(ws+(24<<20));
  u16* y=(u16*)(ws+(32<<20)); u16* x1=(u16*)(ws+(40<<20)); u16* g=(u16*)(ws+(48<<20)); u16* u=(u16*)(ws+(64<<20)); u16* a=(u16*)(ws+(80<<20));
  k_mod<<<15,256,0,stream>>>(c,wm,mod);
  k_ln<<<S,256,0,stream>>>(x,mod,0,1,h);
  k_gemm<<<dim3(8,S),256,0,stream>>>(h,wqkv,qkv,S,3*D,D);
  k_attn<<<dim3(S,H),256,0,stream>>>(qkv,o);
  k_gemm<<<dim3(3,S),256,0,stream>>>(o,wo,y,S,D,D);
  k_res<<<S*D/256,256,0,stream>>>(x,y,mod,2,x1);
  k_ln<<<S,256,0,stream>>>(x1,mod,3,4,h);
  k_gemm<<<dim3(6,S),256,0,stream>>>(h,wg,g,S,F,D);
  k_gemm<<<dim3(6,S),256,0,stream>>>(h,wu,u,S,F,D);
  k_act<<<S*F/256,256,0,stream>>>(g,u,a);
  k_gemm<<<dim3(3,S),256,0,stream>>>(a,wd,y,S,D,F);
  k_res<<<S*D/256,256,0,stream>>>(x1,y,mod,5,(u16*)d_out);
}
```

```cpp
#include <hip/hip_runtime.h>
#include <hip/hip_bf16.h>
#include <stdint.h>
#define VAR 1
typedef unsigned short u16;
__device__ __forceinline__ float b2f(u16 v){ return __uint_as_float(((unsigned)v)<<16); }
__device__ __forceinline__ u16 f2b(float f){ __hip_bfloat16 h = __float2bfloat16(f); return *(u16*)&h; }
__device__ __forceinline__ float rb(float f){ return b2f(f2b(f)); }
constexpr int S=2048, D=640, F=1536, H=32, DH=20, W=256;

__global__ void k_mod(const u16* c, const u16* wmod, float* mod){
  int j = blockIdx.x*blockDim.x+threadIdx.x; if(j>=6*D) return;
  float acc=0; for(int k=0;k<D;k++){ float cv=b2f(c[k]); float s=rb(cv/(1.f+__expf(-cv))); acc+=s*b2f(wmod[(size_t)k*6*D+j]); }
  mod[j]=rb(acc);
}
__global__ void k_ln(const u16* x, const float* mod, int sidx, int cidx, u16* h, float* stats){
  int row=blockIdx.x; __shared__ float red[256];
  float v[3]; float s=0; for(int i=0;i<3;i++){int c=threadIdx.x+i*256; v[i]= c<D? b2f(x[row*D+c]):0.f; s+=v[i];}
  red[threadIdx.x]=s; __syncthreads(); for(int o=128;o>0;o>>=1){ if(threadIdx.x<o) red[threadIdx.x]+=red[threadIdx.x+o]; __syncthreads(); }
  float m=red[0]/D; __syncthreads();
  s=0; for(int i=0;i<3;i++){int c=threadIdx.x+i*256; if(c<D){float d=v[i]-m; s+=d*d;}}
  red[threadIdx.x]=s; __syncthreads(); for(int o=128;o>0;o>>=1){ if(threadIdx.x<o) red[threadIdx.x]+=red[threadIdx.x+o]; __syncthreads(); }
  float var=rb(red[0]/D); float den=rb(sqrtf(rb(var+1e-5f))); float mb=rb(m);
  if(threadIdx.x==0){ stats[row*2]=mb; stats[row*2+1]=1.f/den; }
  for(int i=0;i<3;i++){int c=threadIdx.x+i*256; if(c<D){
    float n=(v[i]-mb)/den; h[row*D+c]=f2b(n*rb(1.f+mod[cidx*D+c])+mod[sidx*D+c]);
  }}
}
__global__ void k_gemm(const u16* A, const u16* B, u16* C, int M,int N,int K){
  int n=blockIdx.x*blockDim.x+threadIdx.x, m=blockIdx.y; if(n>=N) return;
  float acc=0; for(int k=0;k<K;k++) acc+=b2f(A[(size_t)m*K+k])*b2f(B[(size_t)k*N+n]);
  C[(size_t)m*N+n]=f2b(acc);
}
__global__ void k_gemm_fold(const u16* A, const u16* B, u16* C, int M,int N,int K, const float* mod, int sidx, int cidx, const float* stats){
  int n=blockIdx.x*blockDim.x+threadIdx.x, m=blockIdx.y; if(n>=N) return;
  float acc=0, cs=0, bias=0; for(int k=0;k<K;k++){ float w=b2f(B[(size_t)k*N+n]); float wp=rb(w*rb(1.f+mod[cidx*D+k])); acc+=b2f(A[(size_t)m*K+k])*wp; cs+=wp; bias+=mod[sidx*D+k]*w; }
  C[(size_t)m*N+n]=f2b(stats[m*2+1]*(acc-stats[m*2]*cs)+bias);
}
__global__ void k_attn(const u16* qkv, u16* o){
  int q=blockIdx.x, h=blockIdx.y, t=threadIdx.x; __shared__ float p[256]; __shared__ float red[256];
  int key=q-t; float s=-1e30f;
  if(key>=0){ float a=0; for(int d=0;d<DH;d++) a+=b2f(qkv[(size_t)q*3*D+h*DH+d])*b2f(qkv[(size_t)key*3*D+D+h*DH+d]);
    s=a*0.22360679775f;
  }
  red[t]=s; __syncthreads(); for(int o2=128;o2>0;o2>>=1){ if(t<o2) red[t]=fmaxf(red[t],red[t+o2]); __syncthreads(); }
  float m=red[0]; __syncthreads(); float e= key>=0? __expf(s-m):0.f; p[t]=e; red[t]=e; __syncthreads();
  for(int o2=128;o2>0;o2>>=1){ if(t<o2) red[t]+=red[t+o2]; __syncthreads(); }
  float l=red[0];
  if(t<DH){ float a=0; float l2=0; for(int j=0;j<256;j++){ int kk=q-j; if(kk<0) break; a+=rb(p[j])*b2f(qkv[(size_t)kk*3*D+2*D+h*DH+t]); l2+=rb(p[j]); } o[(size_t)q*D+h*DH+t]=f2b(a/l2); }
}
__global__ void k_res(const u16* x, const u16* y, const float* mod, int gidx, u16* out){
  int i=blockIdx.x*blockDim.x+threadIdx.x; if(i>=S*D) return; int c=i%D;
  out[i]=f2b(b2f(x[i])+rb(mod[gidx*D+c]*b2f(y[i])));
}
__global__ void k_act(const u16* g, const u16* u, u16* a){
  int i=blockIdx.x*blockDim.x+threadIdx.x; if(i>=S*F) return; float gv=b2f(g[i]);
  a[i]=f2b(rb(gv/(1.f+__expf(-gv)))*b2f(u[i]));
}
extern "C" void kernel_launch(void* const* d_in, const int* in_sizes, int n_in, void* d_out, int out_size, void* d_ws, size_t ws_size, hipStream_t stream){
  const u16* x=(const u16*)d_in[0]; const u16* wqkv=(const u16*)d_in[1]; const u16* wo=(const u16*)d_in[2];
  const u16* wg=(const u16*)d_in[4]; const u16* wu=(const u16*)d_in[5]; const u16* wd=(const u16*)d_in[6];
  const u16* c=(const u16*)d_in[7]; const u16* wm=(const u16*)d_in[8];
  char* ws=(char*)d_ws; float* mod=(float*)ws; float* stats=(float*)(ws+(512<<10)); u16* h=(u16*)(ws+(1<<20)); u16* qkv=(u16*)(ws+(8<<20)); u16* o=(u16*)(ws+(24<<20));
  u16* y=(u16*)(ws+(32<<20)); u16* x1=(u16*)(ws+(40<<20)); u16* g=(u16*)(ws+(48<<20)); u16* u=(u16*)(ws+(64<<20)); u16* a=(u16*)(ws+(80<<20));
  k_mod<<<15,256,0,stream>>>(c,wm,mod);
  k_ln<<<S,256,0,stream>>>(x,mod,0,1,h,stats);
  k_gemm<<<dim3(8,S),256,0,stream>>>(h,wqkv,qkv,S,3*D,D);
  k_attn<<<dim3(S,H),256,0,stream>>>(qkv,o);
  k_gemm<<<dim3(3,S),256,0,stream>>>(o,wo,y,S,D,D);
  k_res<<<S*D/256,256,0,stream>>>(x,y,mod,2,x1);
  k_ln<<<S,256,0,stream>>>(x1,mod,3,4,h,stats);
  k_gemm<<<dim3(6,S),256,0,stream>>>(h,wg,g,S,F,D);
  k_gemm<<<dim3(6,S),256,0,stream>>>(h,wu,u,S,F,D);
  k_act<<<S*F/256,256,0,stream>>>(g,u,a);
  k_gemm<<<dim3(3,S),256,0,stream>>>(a,wd,y,S,D,F);
  k_res<<<S*D/256,256,0,stream>>>(x1,y,mod,5,(u16*)d_out);
}
```

```cpp
#include <hip/hip_runtime.h>
#include <hip/hip_bf16.h>
#include <stdint.h>

#define GAS __attribute__((address_space(1)))
#define LAS __attribute__((address_space(3)))
#if defined(__HIP_DEVICE_COMPILE__)
#define NOPK __attribute__((target("no-packed-fp32-ops")))
#else
#define NOPK
#endif
#define DI __device__ __forceinline__ NOPK
#define LAM __attribute__((always_inline)) NOPK
typedef unsigned int u32;
typedef unsigned short u16;
typedef u32 u32x4 __attribute__((ext_vector_type(4)));
typedef u32 u32x2 __attribute__((ext_vector_type(2)));
typedef float f32x2 __attribute__((ext_vector_type(2)));
typedef float f32x4 __attribute__((ext_vector_type(4)));
typedef float f32x16 __attribute__((ext_vector_type(16)));
typedef __bf16 bf16x2 __attribute__((ext_vector_type(2)));
typedef __bf16 bf16x4 __attribute__((ext_vector_type(4)));
typedef __bf16 bf16x8 __attribute__((ext_vector_type(8)));
typedef GAS u32 gu32;

#define STAMPS 0

namespace {
constexpr int S = 2048, D = 640, F = 1536;
constexpr u32 WS_KVF = 0;
constexpr u32 WS_CNT = 32768;
constexpr u32 WS_MODF = 65536;
constexpr u32 WS_GS1 = WS_MODF + 16384;
constexpr u32 WS_CSP = WS_GS1 + 8192;
constexpr u32 WS_STAT = WS_CSP + 16 * 16 * 1536;
constexpr u32 WS_N1 = WS_STAT + 64 * 8 * 32 * 16;
constexpr u32 WS_OAF = WS_N1 + 64 * 40 * 1024;
constexpr u32 WS_X1 = WS_OAF + 64 * 40 * 1024;
constexpr u32 WS_ACT = WS_X1 + 64 * 40 * 1024;
constexpr u32 KVB = 22528;
constexpr u32 WS_KVX = WS_ACT + 64 * 96 * 1024;
constexpr u32 WQB = 153600;
constexpr u32 WS_WQF = WS_KVX + 256 * KVB;
constexpr u32 WS_DBG = WS_WQF + 16 * WQB;
constexpr u32 WS_END = WS_DBG + 256 * 32 * 8;
constexpr u32 L_MISC = 153600;
constexpr u32 L_GS = L_MISC;
constexpr u32 L_MODS = L_MISC + 6144;
constexpr u32 L_MODS2 = L_MISC + 7168;
constexpr u32 L_T2 = L_MISC + 8192;
constexpr u32 L_FLAG = L_MISC + 9728;
constexpr u32 LDS_BYTES = 163840;
constexpr u32 L_K = 0, L_V = 30720;
constexpr float QSCALE = 0.22360679774997896f * 1.4426950408889634f;
constexpr u32 SPIN_CAP = 1u << 20;

DI float u2f(u32 v) { return __builtin_bit_cast(float, v); }
DI u32 f2u(float v) { return __builtin_bit_cast(u32, v); }
DI int tidx() { return (int)__builtin_amdgcn_workitem_id_x(); }
DI int bidx() { return (int)__builtin_amdgcn_workgroup_id_x(); }
DI float bf2f(u32 v) { return u2f(v << 16); }
DI float lo16(u32 v) { return u2f(v << 16); }
DI float hi16(u32 v) { return u2f(v & 0xffff0000u); }
DI u32 pk2(float a, float b) { f32x2 v = {a, b}; return __builtin_bit_cast(u32, __builtin_convertvector(v, bf16x2)); }
DI float rbf(float a) { return lo16(pk2(a, a)); }
DI float fma1(float a, float b, float c) { return __builtin_fmaf(a, b, c); }
DI float mul1(float a, float b) { return a * b; }
DI float add1(float a, float b) { return a + b; }
DI float max3(float a, float b, float c) { return __builtin_fmaxf(__builtin_fmaxf(a, b), c); }
DI float xor32f(float v) {
  auto r = __builtin_amdgcn_permlane32_swap(f2u(v), f2u(v), false, false);
  return u2f((tidx() & 32) ? r[0] : r[1]);
}
DI float sum32(float v) { auto r = __builtin_amdgcn_permlane32_swap(f2u(v), f2u(v), false, false); return u2f(r[0]) + u2f(r[1]); }
DI float sum16(float v) { auto r = __builtin_amdgcn_permlane16_swap(f2u(v), f2u(v), false, false); return u2f(r[0]) + u2f(r[1]); }
DI float max32(float v) { auto r = __builtin_amdgcn_permlane32_swap(f2u(v), f2u(v), false, false); return __builtin_fmaxf(u2f(r[0]), u2f(r[1])); }
template <int CTRL> DI float dppf(float v) { return u2f(__builtin_amdgcn_update_dpp(0u, f2u(v), CTRL, 0xf, 0xf, true)); }

#define MFMA32(a, b, c) __builtin_amdgcn_mfma_f32_32x32x16_bf16(__builtin_bit_cast(bf16x8, a), __builtin_bit_cast(bf16x8, b), c, 0, 0, 0)
#define MFMA16(a, b, c) __builtin_amdgcn_mfma_f32_16x16x32_bf16(__builtin_bit_cast(bf16x8, a), __builtin_bit_cast(bf16x8, b), c, 0, 0, 0)
#define PIN(x) asm volatile("" : "+v"(x))
#define WAITV(n) asm volatile("s_waitcnt vmcnt(%0)" ::"i"(n) : "memory")
#define BAR() asm volatile("s_waitcnt lgkmcnt(0)\n\ts_barrier" ::: "memory")
#define RLX_AGENT __ATOMIC_RELAXED, __HIP_MEMORY_SCOPE_AGENT

DI u32x4 lds128(u32 a) { return *(const LAS u32x4*)(size_t)a; }
DI f32x4 ldsf4(u32 a) { return *(const LAS f32x4*)(size_t)a; }
DI u32x2 lds64(u32 a) { return *(const LAS u32x2*)(size_t)a; }
DI float ldsf(u32 a) { return *(const LAS float*)(size_t)a; }
DI void st128(u32 a, u32x4 v) { *(LAS u32x4*)(size_t)a = v; }
DI void stf4(u32 a, f32x4 v) { *(LAS f32x4*)(size_t)a = v; }
DI void st64(u32 a, u32x2 v) { *(LAS u32x2*)(size_t)a = v; }
DI u32x2 ldstr(u32 a) { return __builtin_bit_cast(u32x2, __builtin_amdgcn_ds_read_tr16_b64_v4bf16((LAS bf16x4*)(size_t)a)); }
DI u32x4 ldstr2(u32 a, u32 b) { u32x2 lo = ldstr(a), hi = ldstr(b); return u32x4{lo.x, lo.y, hi.x, hi.y}; }

DI void dma(const char* sbase, u32 voff, u32 lds) {
  asm volatile("s_mov_b32 m0, %2\n\ts_nop 0\n\tglobal_load_lds_dwordx4 %0, %1" ::"v"(voff), "s"(sbase), "s"(lds) : "memory");
}
template <int IMM> DI void gld_sc1(u32x4& r, const char* sbase, u32 voff) {
  asm volatile("global_load_dwordx4 %0, %1, %2 offset:%3 sc1" : "=v"(r) : "v"(voff), "s"(sbase), "i"(IMM) : "memory");
}
template <int IMM> DI void gld(u32x4& r, const char* sbase, u32 voff) {
  asm volatile("global_load_dwordx4 %0, %1, %2 offset:%3" : "=v"(r) : "v"(voff), "s"(sbase), "i"(IMM) : "memory");
}
DI void gld64(u32x2& r, const char* sbase, u32 voff) {
  asm volatile("global_load_dwordx2 %0, %1, %2" : "=v"(r) : "v"(voff), "s"(sbase) : "memory");
}
DI void poll_ge(const char* ws, u32 off, u32 want) {
  gu32* p = (gu32*)(ws + off);
  for (u32 spins = 0; spins < SPIN_CAP; ++spins) {
    if ((u32)__builtin_amdgcn_readfirstlane(__hip_atomic_load(p, RLX_AGENT)) >= want) break;
    __builtin_amdgcn_s_sleep(1);
  }
  __builtin_amdgcn_fence(__ATOMIC_ACQUIRE, "wavefront");
}
DI void signal(const char* ws, u32 off) { __hip_atomic_fetch_add((gu32*)(ws + off), 1u, RLX_AGENT); }

__global__ void __launch_bounds__(256) NOPK k1(const u16* __restrict__ x, const u16* __restrict__ wqkv, const u16* __restrict__ cvec,
                                          const u16* __restrict__ wmod, char* __restrict__ ws) {
  __shared__ float red[64 * 32 + 8 * 32];
  const int b = bidx(), t = tidx();
  if (b < 120) {
    const int p = t & 3, r = t >> 2, col0 = 32 * b;
    u32x4 wv[10]; u16 cv[10];
#pragma unroll
    for (int i = 0; i < 10; ++i) { wv[i] = *(const u32x4*)(wmod + (size_t)(r + 64 * i) * 3840 + col0 + 8 * p); cv[i] = cvec[r + 64 * i]; }
    float acc[8] = {0, 0, 0, 0, 0, 0, 0, 0};
#pragma unroll
    for (int i = 0; i < 10; ++i) {
      const float c = bf2f(cv[i]);
      const float s = rbf(c * __builtin_amdgcn_rcpf(1.f + __builtin_amdgcn_exp2f(-1.4426950408889634f * c)));
#pragma unroll
      for (int e = 0; e < 4; ++e) { acc[2 * e] = __builtin_fmaf(s, lo16(wv[i][e]), acc[2 * e]); acc[2 * e + 1] = __builtin_fmaf(s, hi16(wv[i][e]), acc[2 * e + 1]); }
    }
    *(f32x4*)&red[r * 32 + 8 * p] = f32x4{acc[0], acc[1], acc[2], acc[3]};
    *(f32x4*)&red[r * 32 + 8 * p + 4] = f32x4{acc[4], acc[5], acc[6], acc[7]};
    BAR();
    { const int c = t & 31, part = t >> 5; float s = 0;
#pragma unroll
      for (int i = 0; i < 8; ++i) s += red[(part * 8 + i) * 32 + c];
      red[2048 + part * 32 + c] = s; }
    BAR();
    if (t < 32) {
      float s = 0;
#pragma unroll
      for (int i = 0; i < 8; ++i) s += red[2048 + i * 32 + t];
      const int col = col0 + t, m = col / 640, k = col - 640 * m;
      float v = rbf(s);
      if (m == 1 || m == 4) v = rbf(1.f + v);
      ((float*)(ws + WS_MODF))[col] = v;
      if (m < 2) ((float*)(ws + WS_GS1))[(k >> 3) * 16 + (m == 0 ? 8 : 0) + (k & 7)] = v;
    }
    if (b == 0) {
      *(u32*)(ws + WS_KVF + t * 128) = 0u;
      if (t < 64) *(u32*)(ws + WS_CNT + t * 256) = 0u;
    }
  } else if (b < 184) {
    const int tt = b - 120, w = t >> 6, lane = t & 63, r = lane >> 3, c = lane & 7, tok = 8 * w + r;
    const u16* row = x + (size_t)(32 * tt + tok) * D;
    u32x4 v[10];
#pragma unroll
    for (int i = 0; i < 10; ++i) v[i] = *(const u32x4*)(row + 8 * (c + 8 * i));
    float s = 0;
#pragma unroll
    for (int i = 0; i < 10; ++i)
#pragma unroll
      for (int e = 0; e < 4; ++e) s += lo16(v[i][e]) + hi16(v[i][e]);
    s += dppf<0xB1>(s); s += dppf<0x4E>(s); s += dppf<0x141>(s);
    const float mean = s * (1.f / 640.f);
    float q = 0;
#pragma unroll
    for (int i = 0; i < 10; ++i)
#pragma unroll
      for (int e = 0; e < 4; ++e) { const float a = lo16(v[i][e]) - mean, bb = hi16(v[i][e]) - mean; q = __builtin_fmaf(a, a, q); q = __builtin_fmaf(bb, bb, q); }
    q += dppf<0xB1>(q); q += dppf<0x4E>(q); q += dppf<0x141>(q);
    const float mb = rbf(mean), den = rbf(__builtin_amdgcn_sqrtf(rbf(rbf(q * (1.f / 640.f)) + 1e-5f))), rr = __builtin_amdgcn_rcpf(den);
    char* dst = ws + WS_N1 + (size_t)tt * 40960 + tok * 16;
#pragma unroll
    for (int i = 0; i < 10; ++i) {
      u32x4 o;
#pragma unroll
      for (int e = 0; e < 4; ++e) {
        const u32 d = pk2(lo16(v[i][e]) - mb, hi16(v[i][e]) - mb);
        o[e] = pk2(lo16(d) * rr, hi16(d) * rr);
      }
      *(u32x4*)(dst + (c + 8 * i) * 512) = o;
    }
  } else {
    const int idx = b - 184, ks = idx >> 2, jq = idx & 3;
    if (t < 240) {
      const int seg = t / 80, cp = t - 80 * seg;
      const u16* src = wqkv + (size_t)(16 * ks) * 1920 + 640 * seg + 160 * jq + 2 * cp;
      u32 v[16];
#pragma unroll
      for (int i = 0; i < 16; ++i) v[i] = *(const u32*)(src + (size_t)i * 1920);
      const int jj = (2 * cp) / 40, within = 2 * cp - 40 * jj, j = 4 * jq + jj;
      const int n = (seg == 1 ? 0 : seg == 2 ? 40 : 80) + within, nb = n >> 5, r = n & 31;
      char* dst = ws + WS_WQF + (size_t)j * WQB;
#pragma unroll
      for (int h = 0; h < 2; ++h) {
        u32x4 a, bq;
#pragma unroll
        for (int e = 0; e < 4; ++e) {
          a[e] = __builtin_amdgcn_perm(v[8 * h + 2 * e + 1], v[8 * h + 2 * e], 0x05040100u);
          bq[e] = __builtin_amdgcn_perm(v[8 * h + 2 * e + 1], v[8 * h + 2 * e], 0x07060302u);
        }
        const int off = nb < 3 ? ((nb * 40 + ks) * 64 + h * 32 + r) * 16 : 122880 + ks * 768 + (h * 24 + r) * 16;
        *(u32x4*)(dst + off) = a;
        *(u32x4*)(dst + off + 16) = bq;
      }
    }
  }
}

struct P2 {
  const u16 *x, *wo, *wg, *wu, *wd;
  u16* out;
  char* ws;
};

DI u32 kv_piece(int p, int sl) {
  return p < 5 ? sl * 5120 + p * 1024 : p < 10 ? 15360 + sl * 5120 + (p - 5) * 1024 : p < 16 ? L_V + sl * 6144 + (p - 10) * 1024 : L_V + 18432 + sl * 6144 + (p - 16) * 1024;
}

template <bool PERM> struct WStage {
  const char* base;
  const char* basey;
  u32 vx, vy;
  int kh, qq;
  DI void init(const u16* W, int khalf_rows, int n8, int w, int lane) {
    kh = w >> 2; qq = w & 3;
    const int b5 = lane >> 5;
    const int kq_lane = PERM ? 2 * b5 : b5;
    const int kq_wave = PERM ? 4 * (qq >> 1) + (qq & 1) : 2 * qq;
    vx = (u32)((4 * kq_lane + ((lane >> 2) & 3)) * 1280 + ((lane >> 4) & 1) * 64 + (((lane & 3) ^ (2 * (qq & 1))) * 16));
    base = (const char*)W + (size_t)(kh * khalf_rows + 4 * kq_wave) * 1280 + 160 * n8;
    const int g1 = (lane >> 4) & 1, g0 = (lane >> 3) & 1;
    const int kqy = PERM ? 4 * g1 + 2 * b5 + g0 : 4 * g1 + 2 * g0 + b5;
    vy = (u32)((4 * kqy + ((lane >> 1) & 3)) * 1280 + 128 + (lane & 1) * 16);
    basey = (const char*)W + (size_t)(kh * khalf_rows) * 1280 + 160 * n8;
  }
};
DI u32 wo_slot(int s) { return s < 8 ? (66 + 10 * s) * 1024 : (s - 8) * 10240; }
DI u32 wd_slot(int s) { return (s < 15 ? s : s - 15) * 10240; }
template <bool PERM> DI u32 slot(int s) { return PERM ? wd_slot(s) : wo_slot(s); }
template <bool PERM, int S0, int NS> DI void issue_wstages(const WStage<PERM>& ws_) {
#pragma unroll
  for (int s = S0; s < S0 + NS; ++s) dma(ws_.base + (size_t)s * 40960, ws_.vx, slot<PERM>(s) + ws_.kh * 5120 + ws_.qq * 1024);
#pragma unroll
  for (int m = 0; m < (NS + 3) / 4; ++m) {
    int s = S0 + 4 * m + ws_.qq; s = s < S0 + NS ? s : S0 + NS - 1;
    dma(ws_.basey + (size_t)s * 40960, ws_.vy, slot<PERM>(s) + ws_.kh * 5120 + 4096);
  }
}

DI constexpr int gu_unit(int p, int kh, int i) {
  return p == 0 ? (i < 11 ? 10 + 11 * kh + i : 33 + 9 * kh + (i - 11))
       : p == 1 ? (i < 12 ? 51 + 12 * kh + i : i < 17 ? 5 * kh + (i - 12) : 26 + 3 * kh + (i - 17))
                : (i < 9 ? 33 + 9 * kh + i : 51 + 11 * kh + (i - 9));
}
template <int P, int I0, int I1> DI void issue_gu(const char* wgb, const char* wub, u32 vgu, int w) {
  constexpr int NI = I1 - I0, N = 4 * NI, PER = (N + 7) / 8;
#pragma unroll
  for (int m = 0; m < PER; ++m) {
    int id = w + 8 * m; id = id < N ? id : N - 1;
    const int kh = id >= 2 * NI ? 1 : 0, r = id - kh * 2 * NI;
    const int u0 = kh ? gu_unit(P, 1, I0) : gu_unit(P, 0, I0);
    dma(((r & 1) ? wub : wgb) + (size_t)(320 * kh + 16 * I0 + 16 * (r >> 1)) * 3072 + P * 64, vgu, u0 * 2048 + r * 1024);
  }
}

__global__ void __launch_bounds__(512, 2) NOPK k2(P2 p) {
  const int tid = tidx(), lane = tid & 63, w = __builtin_amdgcn_readfirstlane(tid >> 6);
  const int b = bidx(), mt = (b & 7) * 2 + (b >> 7), j = (b >> 3) & 15;
  const int tt = w & 3, kh = w >> 2, c32 = lane & 31, h = lane >> 5;
  const char* ws = p.ws;
  const __amdgpu_buffer_rsrc_t rs = __builtin_amdgcn_make_buffer_rsrc(p.ws, 0, WS_END, 0x00020000);
  const u32 l16 = lane * 16;
  const int th = j >> 3, n8 = j & 7;
#define STAMP(i) do {} while (0)
  STAMP(0);
  u32x4 a[24];
  {
    if (w < 5) dma(ws + WS_GS1 + w * 1024, l16, L_GS + w * 1024);
    if (w == 5) {
      const int m = lane < 20 ? 2 : lane < 40 ? 4 : 5, e = lane < 60 ? lane % 20 : 19;
      dma(ws + WS_MODF, (u32)(m * 2560 + n8 * 320 + e * 16), L_MODS);
    }
    if (w == 6) {
      const int m = lane < 10 ? 4 : 3, e = lane < 20 ? lane % 10 : 9;
      dma(ws + WS_MODF, (u32)(m * 2560 + mt * 160 + e * 16), L_MODS2);
    }
    const char* wq = ws + WS_WQF + (size_t)j * WQB;
    const char* n1 = ws + WS_N1 + (size_t)((mt * 4 + tt) * 40 + kh * 20) * 1024;
#pragma unroll
    for (int s = 0; s < 5; ++s) {
      const int ks = kh * 20 + 4 * s + tt;
#pragma unroll
      for (int nb = 0; nb < 3; ++nb) dma(wq + (nb * 40 + ks) * 1024, l16, (nb * 40 + ks) * 1024);
      gld<0>(a[4 * s], n1 + s * 4096, l16); gld<1024>(a[4 * s + 1], n1 + s * 4096, l16);
      gld<2048>(a[4 * s + 2], n1 + s * 4096, l16); gld<3072>(a[4 * s + 3], n1 + s * 4096, l16);
    }
#pragma unroll
    for (int i = 0; i < 4; ++i) { int pc = w + 8 * i; pc = pc < 30 ? pc : 29; dma(wq + 122880 + pc * 1024, l16, 122880 + pc * 1024); }
  }
  const int rsw = c32 ^ (16 * kh);
  f32x16 acc[3];
#pragma unroll
  for (int i = 0; i < 3; ++i)
#pragma unroll
    for (int e = 0; e < 16; ++e) acc[i][e] = 0.f;
  {
    const u32 wb = kh * 20480 + (h * 32 + rsw) * 16, gb_ = L_GS + kh * 2560 + h * 64;
#pragma unroll
    for (int s = 0; s < 5; ++s) {
      WAITV(39 - 7 * (s + 1));
      BAR();
#pragma unroll
      for (int i = 0; i < 4; ++i) PIN(a[4 * s + i]);
#pragma unroll
      for (int i = 0; i < 4; ++i) {
        const int k = 4 * s + i;
        const f32x4 g0 = ldsf4(gb_ + k * 128), g1 = ldsf4(gb_ + k * 128 + 16), s0 = ldsf4(gb_ + k * 128 + 32), s1 = ldsf4(gb_ + k * 128 + 48);
        const u32x4 v = a[k];
        u32x4 o;
        o[0] = pk2(fma1(lo16(v[0]), g0[0], s0[0]), fma1(hi16(v[0]), g0[1], s0[1]));
        o[1] = pk2(fma1(lo16(v[1]), g0[2], s0[2]), fma1(hi16(v[1]), g0[3], s0[3]));
        o[2] = pk2(fma1(lo16(v[2]), g1[0], s1[0]), fma1(hi16(v[2]), g1[1], s1[1]));
        o[3] = pk2(fma1(lo16(v[3]), g1[2], s1[2]), fma1(hi16(v[3]), g1[3], s1[3]));
        a[k] = o;
#pragma unroll
        for (int nb = 0; nb < 3; ++nb) acc[nb] = MFMA32(lds128(wb + nb * 40960 + k * 1024), o, acc[nb]);
      }
    }
  }
  STAMP(1);
  WAITV(0);
  BAR();
  const u32 exw = 67584 + w * 6144 + l16, exr = 67584 + (w ^ 4) * 6144 + l16;
#pragma unroll
  for (int nb = 0; nb < 3; ++nb) {
    stf4(exw + nb * 2048, f32x4{acc[nb][8], acc[nb][9], acc[nb][10], acc[nb][11]});
    stf4(exw + nb * 2048 + 1024, f32x4{acc[nb][12], acc[nb][13], acc[nb][14], acc[nb][15]});
  }
  f32x16 acc3;
#pragma unroll
  for (int e = 0; e < 16; ++e) acc3[e] = 0.f;
  {
    const int r3 = rsw < 24 ? rsw : (rsw < 28 ? rsw - 4 : rsw - 12);
    const u32 wb3 = 122880 + kh * 15360 + (h * 24 + r3) * 16;
#pragma unroll
    for (int k = 0; k < 20; ++k) acc3 = MFMA32(lds128(wb3 + k * 768), a[k], acc3);
  }
  BAR();
  const int tokl = 32 * tt + c32;
  auto img_addr = [&](int n0) LAM -> u32 {
    const int part = (n0 * 205) >> 12, d = n0 - 20 * part;
    const u32 base = part < 2 ? part * 15360 + 10240 + tokl * 40 : part < 4 ? L_V + (part - 2) * 18432 + 12288 + tokl * 48 : (part - 4) * 5120 + tokl * 40;
    return base + d * 2;
  };
#pragma unroll
  for (int nb = 0; nb < 3; ++nb)
#pragma unroll
    for (int q = 0; q < 2; ++q) {
      const f32x4 o = ldsf4(exr + nb * 2048 + q * 1024);
      const int n0 = 32 * nb + 16 * kh + 8 * q + 4 * h;
      const float sc = n0 >= 80 ? QSCALE : 1.f;
      float v0 = acc[nb][4 * q] + o[0], v1 = acc[nb][4 * q + 1] + o[1], v2 = acc[nb][4 * q + 2] + o[2], v3 = acc[nb][4 * q + 3] + o[3];
      if (nb == 2) { v0 *= sc; v1 *= sc; v2 *= sc; v3 *= sc; }
      st64(img_addr(n0), u32x2{pk2(v0, v1), pk2(v2, v3)});
    }
  if (tid < 256) st64(L_V + (tid >> 7) * 18432 + 12288 + (tid & 127) * 48 + 40, u32x2{0x3f803f80u, 0x3f803f80u});
  const u32 exw3 = 116736 + w * 2048 + l16, exr3 = 116736 + (w ^ 4) * 2048 + l16;
  stf4(exw3, f32x4{acc3[8], acc3[9], acc3[10], acc3[11]});
  stf4(exw3 + 1024, f32x4{acc3[12], acc3[13], acc3[14], acc3[15]});
  BAR();
  {
    const u32 dst = WS_KVX + (u32)(mt * 16 + j) * KVB + l16;
#pragma unroll
    for (int i = 0; i < 3; ++i) {
      const int pc = w + 8 * i;
      if (pc < 22) __builtin_amdgcn_raw_buffer_store_b128(lds128(kv_piece(pc, 2) + l16), rs, dst + pc * 1024, 0, 16);
    }
#pragma unroll
    for (int q = 0; q < 2; ++q) {
      const f32x4 o = ldsf4(exr3 + q * 1024);
      const int n0 = 96 + 16 * kh + 8 * q + 4 * h;
      const float v0 = (acc3[4 * q] + o[0]) * QSCALE, v1 = (acc3[4 * q + 1] + o[1]) * QSCALE, v2 = (acc3[4 * q + 2] + o[2]) * QSCALE, v3 = (acc3[4 * q + 3] + o[3]) * QSCALE;
      if (n0 < 120) st64(img_addr(n0), u32x2{pk2(v0, v1), pk2(v2, v3)});
    }
  }
  WAITV(0);
  BAR();
  if (tid == 0) __hip_atomic_store((gu32*)(ws + WS_KVF + (mt * 16 + j) * 128), 1u, RLX_AGENT);
  STAMP(2);
  const int hh = w >> 2, qb = w & 3;
  u32x4 q0f, q1f;
  {
    const u32 qa = hh * 5120 + (32 * qb + c32) * 40;
    const u32x2 x0 = lds64(qa + 16 * h), x1 = lds64(qa + 16 * h + 8), x2 = lds64(qa + 32);
    q0f = u32x4{x0.x, x0.y, x1.x, x1.y};
    q1f = u32x4{h ? 0u : x2.x, h ? 0u : x2.y, 0u, 0u};
  }
  if (w == 0) {
    if (mt >= 1) poll_ge(ws, WS_KVF + ((mt - 1) * 16 + j) * 128, 1u);
    if (mt >= 2) poll_ge(ws, WS_KVF + ((mt - 2) * 16 + j) * 128, 1u);
  }
  BAR();
  STAMP(3);
  WStage<false> wos; wos.init(p.wo, 320, n8, w, lane);
  u32x4 csw[5]; u32x2 xr[3];
  const int c16 = lane & 15, g4 = lane >> 4;
  const int tok64 = 128 * mt + 64 * th + 16 * tt + c16;
  {
    u32x4 hl[6];
#pragma unroll
    for (int sl = 0; sl < 2; ++sl) {
      const int sm = mt + sl >= 2 ? mt - 2 + sl : mt;
      const char* src = ws + WS_KVX + (size_t)(sm * 16 + j) * KVB;
#pragma unroll
      for (int i = 0; i < 3; ++i) { int pc = w + 8 * i; pc = pc < 22 ? pc : 21; gld_sc1<0>(hl[3 * sl + i], src + pc * 1024, l16); }
    }
    issue_wstages<false, 0, 8>(wos);
    {
      const int cw = (w - 1) & 3, ch = (cw < 3 ? cw : 0) * 8 + (lane & 7), rg = lane >> 3;
      const char* src = (const char*)(ch < 12 ? p.wg : p.wu);
      const u32 off = (u32)((40 * mt + 5 * rg) * 3072 + (96 * j + 8 * (ch < 12 ? ch : ch - 12)) * 2);
      const char* pl = src + off;
#pragma unroll
      for (int i = 0; i < 5; ++i) asm volatile("global_load_dwordx4 %0, %1, off offset:%2" : "=v"(csw[i]) : "v"(pl + (i >> 1) * 6144), "i"((i & 1) * 3072) : "memory");
    }
#pragma unroll
    for (int m = 0; m < 3; ++m) {
      const int nt = (m + 3 * kh) % 5;
      gld64(xr[m], (const char*)p.x, (u32)(tok64 * 1280 + (80 * n8 + 16 * nt + 4 * g4) * 2));
    }
    WAITV(18);
#pragma unroll
    for (int i = 0; i < 6; ++i) PIN(hl[i]);
#pragma unroll
    for (int sl = 0; sl < 2; ++sl)
#pragma unroll
      for (int i = 0; i < 3; ++i) {
        int pc = w + 8 * i; pc = pc < 22 ? pc : 21;
        if (mt + sl >= 2) st128(kv_piece(pc, sl) + l16, hl[3 * sl + i]); else st128(kv_piece(pc, sl) + l16, u32x4{0u, 0u, 0u, 0u});
      }
  }
  BAR();
  STAMP(4);
  {
    const int tmin = mt >= 2 ? 0 : 8 - 4 * mt - qb;
    const u32 kb = L_K + hh * 15360 + (32 * qb + c32) * 40;
    const u32 vb = L_V + hh * 18432 + (32 * qb + 4 * h + ((lane >> 2) & 3)) * 48 + ((lane >> 4) & 1) * 32 + (lane & 3) * 8;
    const int qlim = c32 - 4 * h;
    auto qk = [&](int t, const f32x16& cin) LAM -> f32x16 {
      const u32x2 k0 = lds64(kb + t * 1280 + 16 * h), k1 = lds64(kb + t * 1280 + 16 * h + 8), k2 = lds64(kb + t * 1280 + 32);
      f32x16 s = MFMA32((u32x4{k0.x, k0.y, k1.x, k1.y}), q0f, cin);
      return MFMA32((u32x4{k2.x, k2.y, k2.x, k2.y}), q1f, s);
    };
    f32x16 cz;
#pragma unroll
    for (int e = 0; e < 16; ++e) cz[e] = 0.f;
    float mx;
    {
      f32x16 s8 = qk(8, cz);
      if (tmin == 0) {
        const f32x16 s0 = qk(0, cz);
#pragma unroll
        for (int e = 0; e < 16; ++e) s8[e] = ((e & 3) + 8 * (e >> 2) > qlim) ? s0[e] : s8[e];
      } else {
#pragma unroll
        for (int e = 0; e < 16; ++e) s8[e] = ((e & 3) + 8 * (e >> 2) > qlim) ? -3.0e38f : s8[e];
      }
      mx = __builtin_fmaxf(s8[0], s8[1]);
#pragma unroll
      for (int e = 2; e < 16; e += 2) mx = max3(mx, s8[e], s8[e + 1]);
    }
#pragma unroll
    for (int t = 1; t < 8; ++t)
      if (t >= tmin) {
        const f32x16 s = qk(t, cz);
#pragma unroll
        for (int e = 0; e < 16; e += 2) mx = max3(mx, s[e], s[e + 1]);
      }
    mx = max32(mx);
    f32x16 cm, oacc;
#pragma unroll
    for (int e = 0; e < 16; ++e) { cm[e] = -mx; oacc[e] = 0.f; }
    auto pv = [&](int t, const float (&pp)[16]) LAM {
#pragma unroll
      for (int s2 = 0; s2 < 2; ++s2) {
        const u32x4 pf = {pk2(pp[8 * s2], pp[8 * s2 + 1]), pk2(pp[8 * s2 + 2], pp[8 * s2 + 3]), pk2(pp[8 * s2 + 4], pp[8 * s2 + 5]), pk2(pp[8 * s2 + 6], pp[8 * s2 + 7])};
        const u32 va = vb + (32 * t + 16 * s2) * 48;
        oacc = MFMA32(ldstr2(va, va + 384), pf, oacc);
      }
    };
    {
      const f32x16 s8 = qk(8, cm);
      float p8[16], p0[16];
      if (tmin == 0) {
        const f32x16 s0 = qk(0, cm);
#pragma unroll
        for (int e = 0; e < 16; ++e) {
          const bool m0 = (e & 3) + 8 * (e >> 2) > qlim;
          const float pe = __builtin_amdgcn_exp2f(m0 ? s0[e] : s8[e]);
          p0[e] = m0 ? pe : 0.f; p8[e] = m0 ? 0.f : pe;
        }
        pv(0, p0);
      } else {
#pragma unroll
        for (int e = 0; e < 16; ++e) { const bool m0 = (e & 3) + 8 * (e >> 2) > qlim; const float pe = __builtin_amdgcn_exp2f(s8[e]); p8[e] = m0 ? 0.f : pe; }
      }
      pv(8, p8);
    }
#pragma unroll
    for (int t = 1; t < 8; ++t)
      if (t >= tmin) {
        const f32x16 s = qk(t, cm);
        float pp[16];
#pragma unroll
        for (int e = 0; e < 16; ++e) pp[e] = __builtin_amdgcn_exp2f(s[e]);
        pv(t, pp);
      }
    const float lx = xor32f(oacc[8]);
    const float inv = __builtin_amdgcn_rcpf(h ? oacc[8] : lx);
    const u32 ob = WS_OAF + (u32)((mt * 4 + qb) * 40) * 1024 + c32 * 16;
#pragma unroll
    for (int g = 0; g < 3; ++g) {
      const int k = 40 * j + 20 * hh + 8 * g + 4 * h;
      const u32x2 o = {pk2(oacc[4 * g] * inv, oacc[4 * g + 1] * inv), pk2(oacc[4 * g + 2] * inv, oacc[4 * g + 3] * inv)};
      if (g < 2 || h == 0) __builtin_amdgcn_raw_buffer_store_b64(o, rs, ob + (k >> 3) * 512 + ((k >> 2) & 1) * 8, 0, 16);
    }
  }
  STAMP(5);
  WAITV(0);
  BAR();
  if (tid == 0) signal(ws, WS_CNT + mt * 256);
  STAMP(6);
  const char* wgb = (const char*)p.wg + 192 * j;
  const char* wub = (const char*)p.wu + 192 * j;
  const u32 vgu = (u32)((lane >> 2) * 3072 + (lane & 3) * 16);
  issue_wstages<false, 8, 2>(wos);
  issue_gu<0, 0, 11>(wgb, wub, vgu, w);
  if (w >= 1 && w <= 3) {
#pragma unroll
    for (int i = 0; i < 5; ++i) PIN(csw[i]);
    const int rg = lane >> 3;
    float cs[16];
#pragma unroll
    for (int e = 0; e < 16; ++e) cs[e] = 0.f;
#pragma unroll
    for (int i = 0; i < 5; ++i) {
      const float g = ldsf(L_MODS2 + (5 * rg + i) * 4), s = ldsf(L_MODS2 + 160 + (5 * rg + i) * 4);
#pragma unroll
      for (int e = 0; e < 4; ++e) {
        const float x0 = lo16(csw[i][e]), x1 = hi16(csw[i][e]);
        cs[2 * e] = __builtin_fmaf(g, x0, cs[2 * e]); cs[2 * e + 1] = __builtin_fmaf(g, x1, cs[2 * e + 1]);
        cs[8 + 2 * e] = __builtin_fmaf(s, x0, cs[8 + 2 * e]); cs[8 + 2 * e + 1] = __builtin_fmaf(s, x1, cs[8 + 2 * e + 1]);
      }
    }
    float r8[8], r4[4];
#pragma unroll
    for (int e = 0; e < 8; ++e) {
      auto r = __builtin_amdgcn_permlane32_swap(f2u(cs[e]), f2u(cs[8 + e]), false, false);
      r8[e] = u2f(r[0]) + u2f(r[1]);
    }
#pragma unroll
    for (int e = 0; e < 4; ++e) {
      auto r = __builtin_amdgcn_permlane16_swap(f2u(r8[e]), f2u(r8[4 + e]), false, false);
      r4[e] = u2f(r[0]) + u2f(r[1]);
    }
#pragma unroll
    for (int e = 0; e < 4; ++e) r4[e] += dppf<0x128>(r4[e]);
    const int ch = (w - 1) * 8 + (lane & 7);
    const u32 off = WS_CSP + (u32)(j * 16 + mt) * 1536 + ch * 64 + (lane >> 5) * 32 + ((lane >> 4) & 1) * 16;
    if ((lane & 8) == 0)
      __builtin_amdgcn_raw_buffer_store_b128(u32x4{f2u(r4[0]), f2u(r4[1]), f2u(r4[2]), f2u(r4[3])}, rs, off, 0, 16);
    WAITV(0);
    if (lane == 0) signal(ws, WS_CNT + (48 + j) * 256);
  }
  if (w == 0) poll_ge(ws, WS_CNT + mt * 256, 16u);
  BAR();
  STAMP(7);
  u32 wbase[5];
#pragma unroll
  for (int m = 0; m < 5; ++m) {
    const int nt = (m + 3 * kh) % 5, q = (lane >> 2) & 3, pp = lane & 3;
    const u32 bx = g4 * 1024 + (nt >> 1) * 256 + q * 64 + (((nt & 1) ^ (g4 & 1)) * 32) + pp * 8;
    const u32 by = 4096 + (g4 >> 1) * 256 + (g4 & 1) * 128 + q * 32 + pp * 8;
    wbase[m] = kh * 5120 + (nt < 4 ? bx : by);
  }
  const u32 avoff = (g4 >> 1) * 1024 + (g4 & 1) * 512 + (16 * (tt & 1) + c16) * 16;
  const int tt64 = mt * 4 + 2 * th + (tt >> 1);
  f32x4 c5[5];
  u32x2 x1k[3];
  {
    const char* ab = ws + WS_OAF + (size_t)(tt64 * 40 + 20 * kh) * 1024;
#pragma unroll
    for (int s = 0; s < 10; s += 2) { gld_sc1<0>(a[s], ab + s * 2048, avoff); gld_sc1<2048>(a[s + 1], ab + s * 2048, avoff); }
#pragma unroll
    for (int m = 0; m < 5; ++m) c5[m] = f32x4{0.f, 0.f, 0.f, 0.f};
#pragma unroll
    for (int s = 0; s < 10; ++s) {
      WAITV(9 - s);
      if (s == 8) BAR();
      PIN(a[s]);
#pragma unroll
      for (int m = 0; m < 5; ++m) c5[m] = MFMA16(ldstr2(wbase[m] + wo_slot(s), wbase[m] + wo_slot(s) + 512), a[s], c5[m]);
    }
    STAMP(8);
    const u32 ew = 67584 + (tt * 5) * 1024 + l16;
    stf4(ew + ((3 + 3 * kh) % 5) * 1024, c5[3]);
    stf4(ew + ((4 + 3 * kh) % 5) * 1024, c5[4]);
    if (kh) stf4(ew, c5[2]);
    BAR();
#pragma unroll
    for (int m = 0; m < 3; ++m) PIN(xr[m]);
    float xs[12];
    const u32 xb = WS_X1 + (u32)(tt64 * 40 + 5 * n8) * 1024 + (g4 >> 1) * 512 + (16 * (tt & 1) + c16) * 16 + (g4 & 1) * 8;
#pragma unroll
    for (int m = 0; m < 3; ++m) {
      if (m < 2 || kh == 0) {
        const int nt = (m + 3 * kh) % 5;
        const f32x4 o = ldsf4(ew + nt * 1024), ga = ldsf4(L_MODS + (16 * nt + 4 * g4) * 4), g2 = ldsf4(L_MODS + 320 + (16 * nt + 4 * g4) * 4);
        const u32 y01 = pk2(c5[m][0] + o[0], c5[m][1] + o[1]), y23 = pk2(c5[m][2] + o[2], c5[m][3] + o[3]);
        const u32 t01 = pk2(ga[0] * lo16(y01), ga[1] * hi16(y01)), t23 = pk2(ga[2] * lo16(y23), ga[3] * hi16(y23));
        x1k[m] = u32x2{pk2(lo16(xr[m].x) + lo16(t01), hi16(xr[m].x) + hi16(t01)), pk2(lo16(xr[m].y) + lo16(t23), hi16(xr[m].y) + hi16(t23))};
        xs[4 * m] = lo16(x1k[m].x); xs[4 * m + 1] = hi16(x1k[m].x); xs[4 * m + 2] = lo16(x1k[m].y); xs[4 * m + 3] = hi16(x1k[m].y);
        __builtin_amdgcn_raw_buffer_store_b64(u32x2{pk2(xs[4 * m] * g2[0], xs[4 * m + 1] * g2[1]), pk2(xs[4 * m + 2] * g2[2], xs[4 * m + 3] * g2[3])}, rs, xb + nt * 1024, 0, 16);
      } else {
        x1k[m] = u32x2{0u, 0u};
        xs[8] = xs[9] = xs[10] = xs[11] = 0.f;
      }
    }
    float s = 0.f;
#pragma unroll
    for (int e = 0; e < 12; ++e) s += xs[e];
    s = sum32(sum16(s));
    const float mean = s * (kh ? 1.f / 32.f : 1.f / 48.f);
    float q = 0.f;
#pragma unroll
    for (int e = 0; e < 8; ++e) { const float d = xs[e] - mean; q = __builtin_fmaf(d, d, q); }
    if (kh == 0) {
#pragma unroll
      for (int e = 8; e < 12; ++e) { const float d = xs[e] - mean; q = __builtin_fmaf(d, d, q); }
    }
    q = sum32(sum16(q));
    if (g4 == 0)
      __builtin_amdgcn_raw_buffer_store_b64(u32x2{f2u(mean), f2u(q)}, rs, WS_STAT + (u32)((tt64 * 8 + n8) * 32 + 16 * (tt & 1) + c16) * 16 + kh * 8, 0, 16);
  }
  WAITV(0);
  BAR();
  if (tid == 0) signal(ws, WS_CNT + (16 + mt) * 256);
  STAMP(9);
  issue_gu<0, 11, 20>(wgb, wub, vgu, w);
  issue_gu<1, 0, 12>(wgb, wub, vgu, w);
  issue_gu<1, 12, 17>(wgb, wub, vgu, w);
  if (w == 0) { poll_ge(ws, WS_CNT + (16 + mt) * 256, 16u); poll_ge(ws, WS_CNT + (48 + j) * 256, 48u); }
  BAR();
  STAMP(10);
  {
    u32x4 stv[4], csv[4];
    {
      const char* sb = ws + WS_STAT + (size_t)(mt * 4 + tt) * 4096;
      const u32 so = h * 2048 + c32 * 16;
      gld_sc1<0>(stv[0], sb, so); gld_sc1<512>(stv[1], sb, so); gld_sc1<1024>(stv[2], sb, so); gld_sc1<1536>(stv[3], sb, so);
      const char* cb = ws + WS_CSP + (size_t)(j * 16 + 4 * (w >> 1)) * 1536;
      const u32 co = ((w & 1) * 48 + (lane < 48 ? lane : 47)) * 16;
      gld_sc1<0>(csv[0], cb, co); gld_sc1<1536>(csv[1], cb, co); gld_sc1<0>(csv[2], cb + 3072, co); gld_sc1<1536>(csv[3], cb + 3072, co);
      const char* ab = ws + WS_X1 + (size_t)((mt * 4 + tt) * 40 + 20 * kh) * 1024;
#pragma unroll
      for (int s = 0; s < 5; ++s) {
        gld_sc1<0>(a[4 * s], ab + s * 4096, l16); gld_sc1<1024>(a[4 * s + 1], ab + s * 4096, l16);
        gld_sc1<2048>(a[4 * s + 2], ab + s * 4096, l16); gld_sc1<3072>(a[4 * s + 3], ab + s * 4096, l16);
      }
    }
    WAITV(20);
#pragma unroll
    for (int i = 0; i < 4; ++i) { PIN(stv[i]); PIN(csv[i]); }
    {
      f32x4 sm;
#pragma unroll
      for (int e = 0; e < 4; ++e) sm[e] = (u2f(csv[0][e]) + u2f(csv[1][e])) + (u2f(csv[2][e]) + u2f(csv[3][e]));
      if (lane < 48) stf4(L_GS + (w >> 1) * 1536 + ((w & 1) * 48 + lane) * 16, sm);
    }
    float rr, rm;
    {
      float s = 0.f;
#pragma unroll
      for (int i = 0; i < 4; ++i) s += 48.f * u2f(stv[i][0]) + 32.f * u2f(stv[i][2]);
      const float mean = sum32(s) * (1.f / 640.f);
      float q = 0.f;
#pragma unroll
      for (int i = 0; i < 4; ++i) {
        const float d0 = u2f(stv[i][0]) - mean, d1 = u2f(stv[i][2]) - mean;
        q += u2f(stv[i][1]) + u2f(stv[i][3]) + 48.f * d0 * d0 + 32.f * d1 * d1;
      }
      q = sum32(q);
      const float den = rbf(__builtin_amdgcn_sqrtf(rbf(rbf(q * (1.f / 640.f)) + 1e-5f)));
      rr = __builtin_amdgcn_rcpf(den); rm = -rr * rbf(mean);
    }
    const u32 gl = h * 512 + ((lane >> 2) & 3) * 64 + ((((lane >> 4) & 1) ^ kh) * 32) + (lane & 3) * 8;
    const u32 sw = 20480 + w * 4096 + l16, sr = 20480 + (w ^ 4) * 4096 + l16;
    const u32 actb = WS_ACT + (u32)((mt * 4 + tt) * 96 + 6 * j + kh) * 1024 + l16;
    f32x16 ag, au;
    float keep[16];
    auto epilogue = [&](int pp) LAM {
      u32x4 o;
#pragma unroll
      for (int q = 0; q < 2; ++q) {
        const u32 tb = L_T2 + (4 * pp + 2 * kh + q) * 64 + h * 16;
        const f32x4 cg = ldsf4(tb), bg = ldsf4(tb + 32), cu = ldsf4(tb + 768), bu = ldsf4(tb + 800);
        float r4[4];
#pragma unroll
        for (int i = 0; i < 4; ++i) {
          const float g = fma1(keep[4 * q + i], rr, fma1(cg[i], rm, bg[i]));
          const float u = fma1(keep[8 + 4 * q + i], rr, fma1(cu[i], rm, bu[i]));
          const float e = __builtin_amdgcn_exp2f(mul1(g, -1.4426950408889634f));
          r4[i] = mul1(mul1(g, u), __builtin_amdgcn_rcpf(add1(e, 1.f)));
        }
        o[2 * q] = pk2(r4[0], r4[1]); o[2 * q + 1] = pk2(r4[2], r4[3]);
      }
      __builtin_amdgcn_raw_buffer_store_b128(o, rs, actb + pp * 2048, 0, 16);
    };
#pragma unroll
    for (int pp = 0; pp < 3; ++pp) {
#pragma unroll
      for (int e = 0; e < 16; ++e) { ag[e] = 0.f; au[e] = 0.f; }
      constexpr int MID[3] = {11, 12, 9};
#pragma unroll
      for (int i = 0; i < 20; ++i) {
        if (i == MID[pp]) {
          if (pp == 1) WAITV(6);
          if (pp == 2) WAITV(1);
          BAR();
          if (pp == 0) {
            issue_gu<1, 17, 20>(wgb, wub, vgu, w);
            if (tid < 96) {
              const u32 t4 = L_GS + tid * 16;
              stf4(L_T2 + tid * 16, (ldsf4(t4) + ldsf4(t4 + 1536)) + (ldsf4(t4 + 3072) + ldsf4(t4 + 4608)));
            }
          }
          if (pp == 1) issue_gu<2, 9, 20>(wgb, wub, vgu, w);
        }
        if (pp == 0) { if (i < 11) WAITV(19 - i); else WAITV(19 - i + 2); PIN(a[i]); }
        const u32 ua = gl + (kh ? gu_unit(pp, 1, i) : gu_unit(pp, 0, i)) * 2048;
        ag = MFMA32(ldstr2(ua, ua + 256), a[i], ag);
        au = MFMA32(ldstr2(ua + 1024, ua + 1280), a[i], au);
        if (pp > 0 && i == 2) epilogue(pp - 1);
      }
      stf4(sw, f32x4{ag[8], ag[9], ag[10], ag[11]}); stf4(sw + 1024, f32x4{ag[12], ag[13], ag[14], ag[15]});
      stf4(sw + 2048, f32x4{au[8], au[9], au[10], au[11]}); stf4(sw + 3072, f32x4{au[12], au[13], au[14], au[15]});
      if (pp == 1) WAITV(7);
      BAR();
      if (pp == 0) issue_gu<2, 0, 9>(wgb, wub, vgu, w);
#pragma unroll
      for (int q = 0; q < 2; ++q) {
        const f32x4 og = ldsf4(sr + q * 1024), ou = ldsf4(sr + 2048 + q * 1024);
#pragma unroll
        for (int i = 0; i < 4; ++i) { keep[4 * q + i] = ag[4 * q + i] + og[i]; keep[8 + 4 * q + i] = au[4 * q + i] + ou[i]; }
      }
    }
    STAMP(11);
    epilogue(2);
  }
  WAITV(0);
  BAR();
  if (tid == 0) signal(ws, WS_CNT + (32 + mt) * 256);
  STAMP(12);
  {
    WStage<true> wds; wds.init(p.wd, 768, n8, w, lane);
    issue_wstages<true, 0, 15>(wds);
    if (w == 0) poll_ge(ws, WS_CNT + (32 + mt) * 256, 16u);
    BAR();
    STAMP(13);
    const char* ab = ws + WS_ACT + (size_t)(tt64 * 96 + 48 * kh) * 1024;
#pragma unroll
    for (int s = 0; s < 24; s += 2) { gld_sc1<0>(a[s], ab + s * 2048, avoff); gld_sc1<2048>(a[s + 1], ab + s * 2048, avoff); }
#pragma unroll
    for (int m = 0; m < 5; ++m) c5[m] = f32x4{0.f, 0.f, 0.f, 0.f};
#pragma unroll
    for (int s = 0; s < 24; ++s) {
      if (s == 5) { BAR(); issue_wstages<true, 15, 5>(wds); }
      if (s == 9) { BAR(); issue_wstages<true, 20, 4>(wds); }
      if (s < 5) WAITV(23 - s); else if (s < 9) WAITV(23 - s + 7); else if (s < 15) WAITV(23 - s + 12);
      if (s == 0) BAR();
      if (s == 15) { WAITV(5); BAR(); }
      if (s == 20) { WAITV(0); BAR(); }
      PIN(a[s]);
#pragma unroll
      for (int m = 0; m < 5; ++m) c5[m] = MFMA16(ldstr2(wbase[m] + wd_slot(s), wbase[m] + wd_slot(s) + 512), a[s], c5[m]);
    }
    STAMP(14);
    const u32 ew = 92160 + (tt * 5) * 1024 + l16;
    stf4(ew + ((3 + 3 * kh) % 5) * 1024, c5[3]);
    stf4(ew + ((4 + 3 * kh) % 5) * 1024, c5[4]);
    if (kh) stf4(ew, c5[2]);
    BAR();
#pragma unroll
    for (int m = 0; m < 3; ++m)
      if (m < 2 || kh == 0) {
        const int nt = (m + 3 * kh) % 5;
        const f32x4 o = ldsf4(ew + nt * 1024), gb = ldsf4(L_MODS + 640 + (16 * nt + 4 * g4) * 4);
        const u32 y01 = pk2(c5[m][0] + o[0], c5[m][1] + o[1]), y23 = pk2(c5[m][2] + o[2], c5[m][3] + o[3]);
        const u32 t01 = pk2(gb[0] * lo16(y01), gb[1] * hi16(y01)), t23 = pk2(gb[2] * lo16(y23), gb[3] * hi16(y23));
        const u32x2 r = {pk2(lo16(x1k[m].x) + lo16(t01), hi16(x1k[m].x) + hi16(t01)), pk2(lo16(x1k[m].y) + lo16(t23), hi16(x1k[m].y) + hi16(t23))};
        *(GAS u32x2*)((GAS char*)p.out + (size_t)tok64 * 1280 + (80 * n8 + 16 * nt + 4 * g4) * 2) = r;
      }
  }
}
}

extern "C" void kernel_launch(void* const* d_in, const int* in_sizes, int n_in, void* d_out, int out_size, void* d_ws, size_t ws_size,
                              hipStream_t stream) {
  (void)in_sizes; (void)n_in; (void)out_size; (void)ws_size;
  hipFuncSetAttribute((const void*)k2, hipFuncAttributeMaxDynamicSharedMemorySize, LDS_BYTES);
  k1<<<344, 256, 0, stream>>>((const u16*)d_in[0], (const u16*)d_in[1], (const u16*)d_in[7], (const u16*)d_in[8], (char*)d_ws);
  P2 p;
  p.x = (const u16*)d_in[0]; p.wo = (const u16*)d_in[2]; p.wg = (const u16*)d_in[4]; p.wu = (const u16*)d_in[5]; p.wd = (const u16*)d_in[6];
  p.out = (u16*)d_out; p.ws = (char*)d_ws;
  k2<<<256, 512, LDS_BYTES, stream>>>(p);
}
```

```cpp
#include <hip/hip_runtime.h>
#include <hip/hip_bf16.h>
#include <stdint.h>

#define GAS __attribute__((address_space(1)))
#define LAS __attribute__((address_space(3)))
#if defined(__HIP_DEVICE_COMPILE__)
#define NOPK __attribute__((target("no-packed-fp32-ops")))
#else
#define NOPK
#endif
#define DI __device__ __forceinline__ NOPK
#define LAM __attribute__((always_inline)) NOPK
typedef unsigned int u32;
typedef unsigned short u16;
typedef u32 u32x4 __attribute__((ext_vector_type(4)));
typedef u32 u32x2 __attribute__((ext_vector_type(2)));
typedef float f32x2 __attribute__((ext_vector_type(2)));
typedef float f32x4 __attribute__((ext_vector_type(4)));
typedef float f32x16 __attribute__((ext_vector_type(16)));
typedef __bf16 bf16x2 __attribute__((ext_vector_type(2)));
typedef __bf16 bf16x4 __attribute__((ext_vector_type(4)));
typedef __bf16 bf16x8 __attribute__((ext_vector_type(8)));
typedef GAS u32 gu32;

#define STAMPS 0

namespace {
constexpr int S = 2048, D = 640, F = 1536;
constexpr u32 WS_KVF = 0;
constexpr u32 WS_CNT = 32768;
constexpr u32 WS_MODF = 65536;
constexpr u32 WS_GS1 = WS_MODF + 16384;
constexpr u32 WS_CSP = WS_GS1 + 8192;
constexpr u32 WS_STAT = WS_CSP + 16 * 16 * 1536;
constexpr u32 WS_N1 = WS_STAT + 64 * 8 * 32 * 16;
constexpr u32 WS_OAF = WS_N1 + 64 * 40 * 1024;
constexpr u32 WS_X1 = WS_OAF + 64 * 40 * 1024;
constexpr u32 WS_ACT = WS_X1 + 64 * 40 * 1024;
constexpr u32 KVB = 22528;
constexpr u32 WS_KVX = WS_ACT + 64 * 96 * 1024;
constexpr u32 WQB = 153600;
constexpr u32 WS_WQF = WS_KVX + 256 * KVB;
constexpr u32 WOB = 102400, WGB = 245760, WDB = 245760;
constexpr u32 WS_WOF = WS_WQF + 16 * WQB;
constexpr u32 WS_WGF = WS_WOF + 8 * WOB;
constexpr u32 WS_WDF = WS_WGF + 16 * WGB;
constexpr u32 WS_DBG = WS_WDF + 8 * WDB;
constexpr u32 WS_DBG1 = WS_DBG + 256 * 32 * 8;
constexpr u32 WS_END = WS_DBG1 + 1024 * 16;
constexpr u32 L_MISC = 153600;
constexpr u32 L_GS = L_MISC;
constexpr u32 L_MODS = L_MISC + 6144;
constexpr u32 L_MODS2 = L_MISC + 7168;
constexpr u32 L_T2 = L_MISC + 8192;
constexpr u32 L_FLAG = L_MISC + 9728;
constexpr u32 LDS_BYTES = 163840;
constexpr u32 L_K = 0, L_V = 30720;
constexpr float QSCALE = 0.22360679774997896f * 1.4426950408889634f;
constexpr int K1_BLOCKS = 796;
constexpr u32 SPIN_CAP = 1u << 20;

DI float u2f(u32 v) { return __builtin_bit_cast(float, v); }
DI u32 f2u(float v) { return __builtin_bit_cast(u32, v); }
DI int tidx() { return (int)__builtin_amdgcn_workitem_id_x(); }
DI int bidx() { return (int)__builtin_amdgcn_workgroup_id_x(); }
DI float bf2f(u32 v) { return u2f(v << 16); }
DI float lo16(u32 v) { return u2f(v << 16); }
DI float hi16(u32 v) { return u2f(v & 0xffff0000u); }
DI u32 pk2(float a, float b) { f32x2 v = {a, b}; return __builtin_bit_cast(u32, __builtin_convertvector(v, bf16x2)); }
DI float rbf(float a) { return lo16(pk2(a, a)); }
DI float fma1(float a, float b, float c) { return __builtin_fmaf(a, b, c); }
DI float mul1(float a, float b) { return a * b; }
DI float add1(float a, float b) { return a + b; }
DI float max3(float a, float b, float c) { return __builtin_fmaxf(__builtin_fmaxf(a, b), c); }
DI float xor32f(float v) {
  auto r = __builtin_amdgcn_permlane32_swap(f2u(v), f2u(v), false, false);
  return u2f((tidx() & 32) ? r[0] : r[1]);
}
DI float sum32(float v) { auto r = __builtin_amdgcn_permlane32_swap(f2u(v), f2u(v), false, false); return u2f(r[0]) + u2f(r[1]); }
DI float sum16(float v) { auto r = __builtin_amdgcn_permlane16_swap(f2u(v), f2u(v), false, false); return u2f(r[0]) + u2f(r[1]); }
DI float max32(float v) { auto r = __builtin_amdgcn_permlane32_swap(f2u(v), f2u(v), false, false); return __builtin_fmaxf(u2f(r[0]), u2f(r[1])); }
template <int CTRL> DI float dppf(float v) { return u2f(__builtin_amdgcn_update_dpp(0u, f2u(v), CTRL, 0xf, 0xf, true)); }

#define MFMA32(a, b, c) __builtin_amdgcn_mfma_f32_32x32x16_bf16(__builtin_bit_cast(bf16x8, a), __builtin_bit_cast(bf16x8, b), c, 0, 0, 0)
#define MFMA16(a, b, c) __builtin_amdgcn_mfma_f32_16x16x32_bf16(__builtin_bit_cast(bf16x8, a), __builtin_bit_cast(bf16x8, b), c, 0, 0, 0)
#define PIN(x) asm volatile("" : "+v"(x))
#define WAITV(n) asm volatile("s_waitcnt vmcnt(%0)" ::"i"(n) : "memory")
#define BAR() asm volatile("s_waitcnt lgkmcnt(0)\n\ts_barrier" ::: "memory")
#define RLX_AGENT __ATOMIC_RELAXED, __HIP_MEMORY_SCOPE_AGENT

DI u32x4 lds128(u32 a) { return *(const LAS u32x4*)(size_t)a; }
DI f32x4 ldsf4(u32 a) { return *(const LAS f32x4*)(size_t)a; }
DI u32x2 lds64(u32 a) { return *(const LAS u32x2*)(size_t)a; }
DI float ldsf(u32 a) { return *(const LAS float*)(size_t)a; }
DI void st128(u32 a, u32x4 v) { *(LAS u32x4*)(size_t)a = v; }
DI void stf4(u32 a, f32x4 v) { *(LAS f32x4*)(size_t)a = v; }
DI void st64(u32 a, u32x2 v) { *(LAS u32x2*)(size_t)a = v; }
DI u32x2 ldstr(u32 a) { return __builtin_bit_cast(u32x2, __builtin_amdgcn_ds_read_tr16_b64_v4bf16((LAS bf16x4*)(size_t)a)); }
DI u32x4 ldstr2(u32 a, u32 b) { u32x2 lo = ldstr(a), hi = ldstr(b); return u32x4{lo.x, lo.y, hi.x, hi.y}; }

DI void dma(const char* sbase, u32 voff, u32 lds) {
  asm volatile("s_mov_b32 m0, %2\n\ts_nop 0\n\tglobal_load_lds_dwordx4 %0, %1" ::"v"(voff), "s"(sbase), "s"(lds) : "memory");
}
template <int IMM> DI void gld_sc1(u32x4& r, const char* sbase, u32 voff) {
  asm volatile("global_load_dwordx4 %0, %1, %2 offset:%3 sc1" : "=v"(r) : "v"(voff), "s"(sbase), "i"(IMM) : "memory");
}
template <int IMM> DI void gld(u32x4& r, const char* sbase, u32 voff) {
  asm volatile("global_load_dwordx4 %0, %1, %2 offset:%3" : "=v"(r) : "v"(voff), "s"(sbase), "i"(IMM) : "memory");
}
DI void gld64(u32x2& r, const char* sbase, u32 voff) {
  asm volatile("global_load_dwordx2 %0, %1, %2" : "=v"(r) : "v"(voff), "s"(sbase) : "memory");
}
DI void poll_ge(const char* ws, u32 off, u32 want) {
  gu32* p = (gu32*)(ws + off);
  for (u32 spins = 0; spins < SPIN_CAP; ++spins) {
    if ((u32)__builtin_amdgcn_readfirstlane(__hip_atomic_load(p, RLX_AGENT)) >= want) break;
    __builtin_amdgcn_s_sleep(1);
  }
  __builtin_amdgcn_fence(__ATOMIC_ACQUIRE, "wavefront");
}
DI void signal(const char* ws, u32 off) { __hip_atomic_fetch_add((gu32*)(ws + off), 1u, RLX_AGENT); }

struct P1 {
  const u16 *x, *wqkv, *wo, *wg, *wu, *wd, *cvec, *wmod;
  char* ws;
};
__global__ void __launch_bounds__(256) NOPK k1(P1 p) {
  __shared__ float red[32 * 64 + 4 * 64 + 640];
  const int b = bidx(), t = tidx();
  char* ws = p.ws;
  if (b < 60) {
    const int ch = t & 7, r = t >> 3, col0 = 64 * b;
    const u32x4 cv = *(const u32x4*)(p.cvec + 8 * (t < 80 ? t : 79));
    u32x4 wv[20];
#pragma unroll
    for (int i = 0; i < 20; ++i) wv[i] = *(const u32x4*)(p.wmod + (size_t)(r + 32 * i) * 3840 + col0 + 8 * ch);
    if (t < 80) {
#pragma unroll
      for (int e = 0; e < 8; ++e) {
        const float c = (e & 1) ? hi16(cv[e >> 1]) : lo16(cv[e >> 1]);
        red[2304 + 8 * t + e] = rbf(c * __builtin_amdgcn_rcpf(1.f + __builtin_amdgcn_exp2f(-1.4426950408889634f * c)));
      }
    }
    BAR();
    float acc[8] = {0, 0, 0, 0, 0, 0, 0, 0};
#pragma unroll
    for (int i = 0; i < 20; ++i) {
      const float s = red[2304 + r + 32 * i];
#pragma unroll
      for (int e = 0; e < 4; ++e) { acc[2 * e] = __builtin_fmaf(s, lo16(wv[i][e]), acc[2 * e]); acc[2 * e + 1] = __builtin_fmaf(s, hi16(wv[i][e]), acc[2 * e + 1]); }
    }
    *(f32x4*)&red[r * 64 + 8 * ch] = f32x4{acc[0], acc[1], acc[2], acc[3]};
    *(f32x4*)&red[r * 64 + 8 * ch + 4] = f32x4{acc[4], acc[5], acc[6], acc[7]};
    BAR();
    { const int c = t & 63, part = t >> 6; float s = 0;
#pragma unroll
      for (int i = 0; i < 8; ++i) s += red[(part * 8 + i) * 64 + c];
      red[2048 + part * 64 + c] = s; }
    BAR();
    if (t < 64) {
      const float s = (red[2048 + t] + red[2112 + t]) + (red[2176 + t] + red[2240 + t]);
      const int m = b / 10, k = col0 + t - 640 * m;
      float v = rbf(s);
      if (m == 1 || m == 4) v = rbf(1.f + v);
      ((float*)(ws + WS_MODF))[col0 + t] = v;
      if (m < 2) ((float*)(ws + WS_GS1))[(k >> 3) * 16 + (m == 0 ? 8 : 0) + (k & 7)] = v;
    }
    if (b == 0) {
      *(u32*)(ws + WS_KVF + t * 128) = 0u;
      if (t < 64) *(u32*)(ws + WS_CNT + t * 256) = 0u;
    }
  } else if (b < 124) {
    const int tt = b - 60, w = t >> 6, lane = t & 63, r = lane >> 3, c = lane & 7, tok = 8 * w + r;
    const u16* row = p.x + (size_t)(32 * tt + tok) * D;
    u32x4 v[10];
#pragma unroll
    for (int i = 0; i < 10; ++i) v[i] = *(const u32x4*)(row + 8 * (c + 8 * i));
    float s = 0;
#pragma unroll
    for (int i = 0; i < 10; ++i)
#pragma unroll
      for (int e = 0; e < 4; ++e) s += lo16(v[i][e]) + hi16(v[i][e]);
    s += dppf<0xB1>(s); s += dppf<0x4E>(s); s += dppf<0x141>(s);
    const float mean = s * (1.f / 640.f);
    float q = 0;
#pragma unroll
    for (int i = 0; i < 10; ++i)
#pragma unroll
      for (int e = 0; e < 4; ++e) { const float a = lo16(v[i][e]) - mean, bb = hi16(v[i][e]) - mean; q = __builtin_fmaf(a, a, q); q = __builtin_fmaf(bb, bb, q); }
    q += dppf<0xB1>(q); q += dppf<0x4E>(q); q += dppf<0x141>(q);
    const float mb = rbf(mean), den = rbf(__builtin_amdgcn_sqrtf(rbf(rbf(q * (1.f / 640.f)) + 1e-5f))), rr = __builtin_amdgcn_rcpf(den);
    char* dst = ws + WS_N1 + (size_t)tt * 40960 + tok * 16;
#pragma unroll
    for (int i = 0; i < 10; ++i) {
      u32x4 o;
#pragma unroll
      for (int e = 0; e < 4; ++e) {
        const u32 d = pk2(lo16(v[i][e]) - mb, hi16(v[i][e]) - mb);
        o[e] = pk2(lo16(d) * rr, hi16(d) * rr);
      }
      *(u32x4*)(dst + (c + 8 * i) * 512) = o;
    }
  } else {
    const u16* src; int stride; u32 off0, off1; bool act = true, perm = false;
    if (b < 284) {
      const int idx = b - 124, ks = idx >> 2, jq = idx & 3, tc = t < 240 ? t : 239;
      act = t < 240;
      const int seg = tc / 80, cp = tc - 80 * seg;
      src = p.wqkv + (size_t)(16 * ks) * 1920 + 640 * seg + 160 * jq + 2 * cp; stride = 1920;
      const int jj = (2 * cp) / 40, within = 2 * cp - 40 * jj, j = 4 * jq + jj;
      const int n = (seg == 1 ? 0 : seg == 2 ? 40 : 80) + within, nb = n >> 5, r = n & 31;
      off0 = WS_WQF + j * WQB + (nb < 3 ? ((nb * 40 + ks) * 64 + r) * 16 : 122880 + ks * 768 + r * 16);
      off1 = off0 + (nb < 3 ? 512 : 384);
    } else if (b < 524) {
      const int idx = b - 284, gu = idx >= 120, rem = idx - 120 * gu, ks = rem / 3, n = (rem - 3 * ks) * 512 + 2 * t;
      src = (gu ? p.wu : p.wg) + (size_t)(16 * ks) * 1536 + n; stride = 1536;
      const int nblk = n >> 5, j = nblk / 3, pp = nblk - 3 * j, kh = ks >= 20, i = ks - 20 * kh;
      off0 = WS_WGF + j * WGB + ((pp * 2 + kh) * 20 + i) * 2048 + gu * 1024 + (n & 31) * 16;
      off1 = off0 + 512;
    } else {
      const bool dn = b >= 604;
      const int idx = b - (dn ? 604 : 524), k16 = idx >> 1, tc = t < 160 ? t : 159, n = (idx & 1) * 320 + 2 * tc;
      act = t < 160; perm = dn;
      src = (dn ? p.wd : p.wo) + (size_t)(16 * k16) * 640 + n; stride = 640;
      const int half = dn ? 48 : 20, kh = k16 >= half, kk = k16 - half * kh, st = kk >> 1, H = kk & 1;
      const int ntg = n >> 4, n8 = ntg / 5, nt = ntg - 5 * n8;
      off0 = (dn ? WS_WDF + n8 * WDB : WS_WOF + n8 * WOB) + (st * 10 + kh * 5 + nt) * 1024 + (2 * H * 16 + (n & 15)) * 16;
      off1 = off0 + 256;
    }
    u32 v[16];
#pragma unroll
    for (int i = 0; i < 16; ++i) v[i] = *(const u32*)(src + (size_t)i * stride);
    if (perm) {
#pragma unroll
      for (int e = 0; e < 4; ++e) { const u32 tmp = v[4 + e]; v[4 + e] = v[8 + e]; v[8 + e] = tmp; }
    }
    if (act) {
#pragma unroll
      for (int h = 0; h < 2; ++h) {
        u32x4 a, bq;
#pragma unroll
        for (int e = 0; e < 4; ++e) {
          a[e] = __builtin_amdgcn_perm(v[8 * h + 2 * e + 1], v[8 * h + 2 * e], 0x05040100u);
          bq[e] = __builtin_amdgcn_perm(v[8 * h + 2 * e + 1], v[8 * h + 2 * e], 0x07060302u);
        }
        char* dst = ws + (h ? off1 : off0);
        *(u32x4*)dst = a;
        *(u32x4*)(dst + 16) = bq;
      }
    }
  }
}

struct P2 {
  const u16 *x, *wo, *wg, *wu, *wd;
  u16* out;
  char* ws;
};

DI u32 kv_piece(int p, int sl) {
  return p < 5 ? sl * 5120 + p * 1024 : p < 10 ? 15360 + sl * 5120 + (p - 5) * 1024 : p < 16 ? L_V + sl * 6144 + (p - 10) * 1024 : L_V + 18432 + sl * 6144 + (p - 16) * 1024;
}

template <bool PERM> struct WStage {
  const char* base;
  const char* basey;
  u32 vx, vy;
  int kh, qq;
  DI void init(const u16* W, int khalf_rows, int n8, int w, int lane) {
    kh = w >> 2; qq = w & 3;
    const int b5 = lane >> 5;
    const int kq_lane = PERM ? 2 * b5 : b5;
    const int kq_wave = PERM ? 4 * (qq >> 1) + (qq & 1) : 2 * qq;
    vx = (u32)((4 * kq_lane + ((lane >> 2) & 3)) * 1280 + ((lane >> 4) & 1) * 64 + (((lane & 3) ^ (2 * (qq & 1))) * 16));
    base = (const char*)W + (size_t)(kh * khalf_rows + 4 * kq_wave) * 1280 + 160 * n8;
    const int g1 = (lane >> 4) & 1, g0 = (lane >> 3) & 1;
    const int kqy = PERM ? 4 * g1 + 2 * b5 + g0 : 4 * g1 + 2 * g0 + b5;
    vy = (u32)((4 * kqy + ((lane >> 1) & 3)) * 1280 + 128 + (lane & 1) * 16);
    basey = (const char*)W + (size_t)(kh * khalf_rows) * 1280 + 160 * n8;
  }
};
DI u32 wo_slot(int s) { return s < 8 ? (66 + 10 * s) * 1024 : (s - 8) * 10240; }
DI u32 wd_slot(int s) { return (s < 15 ? s : s - 15) * 10240; }
template <bool PERM> DI u32 slot(int s) { return PERM ? wd_slot(s) : wo_slot(s); }
template <bool PERM, int S0, int NS> DI void issue_wstages(const WStage<PERM>& ws_) {
#pragma unroll
  for (int s = S0; s < S0 + NS; ++s) dma(ws_.base + (size_t)s * 40960, ws_.vx, slot<PERM>(s) + ws_.kh * 5120 + ws_.qq * 1024);
#pragma unroll
  for (int m = 0; m < (NS + 3) / 4; ++m) {
    int s = S0 + 4 * m + ws_.qq; s = s < S0 + NS ? s : S0 + NS - 1;
    dma(ws_.basey + (size_t)s * 40960, ws_.vy, slot<PERM>(s) + ws_.kh * 5120 + 4096);
  }
}

DI constexpr int gu_unit(int p, int kh, int i) {
  return p == 0 ? (i < 11 ? 10 + 11 * kh + i : 33 + 9 * kh + (i - 11))
       : p == 1 ? (i < 12 ? 51 + 12 * kh + i : i < 17 ? 5 * kh + (i - 12) : 26 + 3 * kh + (i - 17))
                : (i < 9 ? 33 + 9 * kh + i : 51 + 11 * kh + (i - 9));
}
template <int P, int I0, int I1> DI void issue_gu(const char* wgb, const char* wub, u32 vgu, int w) {
  constexpr int NI = I1 - I0, N = 4 * NI, PER = (N + 7) / 8;
#pragma unroll
  for (int m = 0; m < PER; ++m) {
    int id = w + 8 * m; id = id < N ? id : N - 1;
    const int kh = id >= 2 * NI ? 1 : 0, r = id - kh * 2 * NI;
    const int u0 = kh ? gu_unit(P, 1, I0) : gu_unit(P, 0, I0);
    dma(((r & 1) ? wub : wgb) + (size_t)(320 * kh + 16 * I0 + 16 * (r >> 1)) * 3072 + P * 64, vgu, u0 * 2048 + r * 1024);
  }
}

__global__ void __launch_bounds__(512, 2) NOPK k2(P2 p) {
  const int tid = tidx(), lane = tid & 63, w = __builtin_amdgcn_readfirstlane(tid >> 6);
  const int b = bidx(), mt = (b & 7) * 2 + (b >> 7), j = (b >> 3) & 15;
  const int tt = w & 3, kh = w >> 2, c32 = lane & 31, h = lane >> 5;
  const char* ws = p.ws;
  const __amdgpu_buffer_rsrc_t rs = __builtin_amdgcn_make_buffer_rsrc(p.ws, 0, WS_END, 0x00020000);
  const u32 l16 = lane * 16;
  const int th = j >> 3, n8 = j & 7;
#define STAMP(i) do {} while (0)
  STAMP(0);
  u32x4 a[24];
  {
    if (w < 5) dma(ws + WS_GS1 + w * 1024, l16, L_GS + w * 1024);
    if (w == 5) {
      const int m = lane < 20 ? 2 : lane < 40 ? 4 : 5, e = lane < 60 ? lane % 20 : 19;
      dma(ws + WS_MODF, (u32)(m * 2560 + n8 * 320 + e * 16), L_MODS);
    }
    if (w == 6) {
      const int m = lane < 10 ? 4 : 3, e = lane < 20 ? lane % 10 : 9;
      dma(ws + WS_MODF, (u32)(m * 2560 + mt * 160 + e * 16), L_MODS2);
    }
    const char* wq = ws + WS_WQF + (size_t)j * WQB;
    const char* n1 = ws + WS_N1 + (size_t)((mt * 4 + tt) * 40 + kh * 20) * 1024;
#pragma unroll
    for (int s = 0; s < 5; ++s) {
      const int ks = kh * 20 + 4 * s + tt;
#pragma unroll
      for (int nb = 0; nb < 3; ++nb) dma(wq + (nb * 40 + ks) * 1024, l16, (nb * 40 + ks) * 1024);
      gld<0>(a[4 * s], n1 + s * 4096, l16); gld<1024>(a[4 * s + 1], n1 + s * 4096, l16);
      gld<2048>(a[4 * s + 2], n1 + s * 4096, l16); gld<3072>(a[4 * s + 3], n1 + s * 4096, l16);
    }
#pragma unroll
    for (int i = 0; i < 4; ++i) { int pc = w + 8 * i; pc = pc < 30 ? pc : 29; dma(wq + 122880 + pc * 1024, l16, 122880 + pc * 1024); }
  }
  const int rsw = c32 ^ (16 * kh);
  f32x16 acc[3];
#pragma unroll
  for (int i = 0; i < 3; ++i)
#pragma unroll
    for (int e = 0; e < 16; ++e) acc[i][e] = 0.f;
  {
    const u32 wb = kh * 20480 + (h * 32 + rsw) * 16, gb_ = L_GS + kh * 2560 + h * 64;
#pragma unroll
    for (int s = 0; s < 5; ++s) {
      WAITV(39 - 7 * (s + 1));
      BAR();
#pragma unroll
      for (int i = 0; i < 4; ++i) PIN(a[4 * s + i]);
#pragma unroll
      for (int i = 0; i < 4; ++i) {
        const int k = 4 * s + i;
        const f32x4 g0 = ldsf4(gb_ + k * 128), g1 = ldsf4(gb_ + k * 128 + 16), s0 = ldsf4(gb_ + k * 128 + 32), s1 = ldsf4(gb_ + k * 128 + 48);
        const u32x4 v = a[k];
        u32x4 o;
        o[0] = pk2(fma1(lo16(v[0]), g0[0], s0[0]), fma1(hi16(v[0]), g0[1], s0[1]));
        o[1] = pk2(fma1(lo16(v[1]), g0[2], s0[2]), fma1(hi16(v[1]), g0[3], s0[3]));
        o[2] = pk2(fma1(lo16(v[2]), g1[0], s1[0]), fma1(hi16(v[2]), g1[1], s1[1]));
        o[3] = pk2(fma1(lo16(v[3]), g1[2], s1[2]), fma1(hi16(v[3]), g1[3], s1[3]));
        a[k] = o;
#pragma unroll
        for (int nb = 0; nb < 3; ++nb) acc[nb] = MFMA32(lds128(wb + nb * 40960 + k * 1024), o, acc[nb]);
      }
    }
  }
  STAMP(1);
  WAITV(0);
  BAR();
  const u32 exw = 67584 + w * 6144 + l16, exr = 67584 + (w ^ 4) * 6144 + l16;
#pragma unroll
  for (int nb = 0; nb < 3; ++nb) {
    stf4(exw + nb * 2048, f32x4{acc[nb][8], acc[nb][9], acc[nb][10], acc[nb][11]});
    stf4(exw + nb * 2048 + 1024, f32x4{acc[nb][12], acc[nb][13], acc[nb][14], acc[nb][15]});
  }
  f32x16 acc3;
#pragma unroll
  for (int e = 0; e < 16; ++e) acc3[e] = 0.f;
  {
    const int r3 = rsw < 24 ? rsw : (rsw < 28 ? rsw - 4 : rsw - 12);
    const u32 wb3 = 122880 + kh * 15360 + (h * 24 + r3) * 16;
#pragma unroll
    for (int k = 0; k < 20; ++k) acc3 = MFMA32(lds128(wb3 + k * 768), a[k], acc3);
  }
  BAR();
  const int tokl = 32 * tt + c32;
  auto img_addr = [&](int n0) LAM -> u32 {
    const int part = (n0 * 205) >> 12, d = n0 - 20 * part;
    const u32 base = part < 2 ? part * 15360 + 10240 + tokl * 40 : part < 4 ? L_V + (part - 2) * 18432 + 12288 + tokl * 48 : (part - 4) * 5120 + tokl * 40;
    return base + d * 2;
  };
#pragma unroll
  for (int nb = 0; nb < 3; ++nb)
#pragma unroll
    for (int q = 0; q < 2; ++q) {
      const f32x4 o = ldsf4(exr + nb * 2048 + q * 1024);
      const int n0 = 32 * nb + 16 * kh + 8 * q + 4 * h;
      const float sc = n0 >= 80 ? QSCALE : 1.f;
      float v0 = acc[nb][4 * q] + o[0], v1 = acc[nb][4 * q + 1] + o[1], v2 = acc[nb][4 * q + 2] + o[2], v3 = acc[nb][4 * q + 3] + o[3];
      if (nb == 2) { v0 *= sc; v1 *= sc; v2 *= sc; v3 *= sc; }
      st64(img_addr(n0), u32x2{pk2(v0, v1), pk2(v2, v3)});
    }
  if (tid < 256) st64(L_V + (tid >> 7) * 18432 + 12288 + (tid & 127) * 48 + 40, u32x2{0x3f803f80u, 0x3f803f80u});
  const u32 exw3 = 116736 + w * 2048 + l16, exr3 = 116736 + (w ^ 4) * 2048 + l16;
  stf4(exw3, f32x4{acc3[8], acc3[9], acc3[10], acc3[11]});
  stf4(exw3 + 1024, f32x4{acc3[12], acc3[13], acc3[14], acc3[15]});
  BAR();
  {
    const u32 dst = WS_KVX + (u32)(mt * 16 + j) * KVB + l16;
#pragma unroll
    for (int i = 0; i < 3; ++i) {
      const int pc = w + 8 * i;
      if (pc < 22) __builtin_amdgcn_raw_buffer_store_b128(lds128(kv_piece(pc, 2) + l16), rs, dst + pc * 1024, 0, 16);
    }
#pragma unroll
    for (int q = 0; q < 2; ++q) {
      const f32x4 o = ldsf4(exr3 + q * 1024);
      const int n0 = 96 + 16 * kh + 8 * q + 4 * h;
      const float v0 = (acc3[4 * q] + o[0]) * QSCALE, v1 = (acc3[4 * q + 1] + o[1]) * QSCALE, v2 = (acc3[4 * q + 2] + o[2]) * QSCALE, v3 = (acc3[4 * q + 3] + o[3]) * QSCALE;
      if (n0 < 120) st64(img_addr(n0), u32x2{pk2(v0, v1), pk2(v2, v3)});
    }
  }
  WAITV(0);
  BAR();
  if (tid == 0) __hip_atomic_store((gu32*)(ws + WS_KVF + (mt * 16 + j) * 128), 1u, RLX_AGENT);
  STAMP(2);
  const int hh = w >> 2, qb = w & 3;
  u32x4 q0f, q1f;
  {
    const u32 qa = hh * 5120 + (32 * qb + c32) * 40;
    const u32x2 x0 = lds64(qa + 16 * h), x1 = lds64(qa + 16 * h + 8), x2 = lds64(qa + 32);
    q0f = u32x4{x0.x, x0.y, x1.x, x1.y};
    q1f = u32x4{h ? 0u : x2.x, h ? 0u : x2.y, 0u, 0u};
  }
  if (w == 0) {
    if (mt >= 1) poll_ge(ws, WS_KVF + ((mt - 1) * 16 + j) * 128, 1u);
    if (mt >= 2) poll_ge(ws, WS_KVF + ((mt - 2) * 16 + j) * 128, 1u);
  }
  BAR();
  STAMP(3);
  WStage<false> wos; wos.init(p.wo, 320, n8, w, lane);
  u32x4 csw[5]; u32x2 xr[3];
  const int c16 = lane & 15, g4 = lane >> 4;
  const int tok64 = 128 * mt + 64 * th + 16 * tt + c16;
  {
    u32x4 hl[6];
#pragma unroll
    for (int sl = 0; sl < 2; ++sl) {
      const int sm = mt + sl >= 2 ? mt - 2 + sl : mt;
      const char* src = ws + WS_KVX + (size_t)(sm * 16 + j) * KVB;
#pragma unroll
      for (int i = 0; i < 3; ++i) { int pc = w + 8 * i; pc = pc < 22 ? pc : 21; gld_sc1<0>(hl[3 * sl + i], src + pc * 1024, l16); }
    }
    issue_wstages<false, 0, 8>(wos);
    {
      const int cw = (w - 1) & 3, ch = (cw < 3 ? cw : 0) * 8 + (lane & 7), rg = lane >> 3;
      const char* src = (const char*)(ch < 12 ? p.wg : p.wu);
      const u32 off = (u32)((40 * mt + 5 * rg) * 3072 + (96 * j + 8 * (ch < 12 ? ch : ch - 12)) * 2);
      const char* pl = src + off;
#pragma unroll
      for (int i = 0; i < 5; ++i) asm volatile("global_load_dwordx4 %0, %1, off offset:%2" : "=v"(csw[i]) : "v"(pl + (i >> 1) * 6144), "i"((i & 1) * 3072) : "memory");
    }
#pragma unroll
    for (int m = 0; m < 3; ++m) {
      const int nt = (m + 3 * kh) % 5;
      gld64(xr[m], (const char*)p.x, (u32)(tok64 * 1280 + (80 * n8 + 16 * nt + 4 * g4) * 2));
    }
    WAITV(18);
#pragma unroll
    for (int i = 0; i < 6; ++i) PIN(hl[i]);
#pragma unroll
    for (int sl = 0; sl < 2; ++sl)
#pragma unroll
      for (int i = 0; i < 3; ++i) {
        int pc = w + 8 * i; pc = pc < 22 ? pc : 21;
        if (mt + sl >= 2) st128(kv_piece(pc, sl) + l16, hl[3 * sl + i]); else st128(kv_piece(pc, sl) + l16, u32x4{0u, 0u, 0u, 0u});
      }
  }
  BAR();
  STAMP(4);
  {
    const int tmin = mt >= 2 ? 0 : 8 - 4 * mt - qb;
    const u32 kb = L_K + hh * 15360 + (32 * qb + c32) * 40;
    const u32 vb = L_V + hh * 18432 + (32 * qb + 4 * h + ((lane >> 2) & 3)) * 48 + ((lane >> 4) & 1) * 32 + (lane & 3) * 8;
    const int qlim = c32 - 4 * h;
    auto qk = [&](int t, const f32x16& cin) LAM -> f32x16 {
      const u32x2 k0 = lds64(kb + t * 1280 + 16 * h), k1 = lds64(kb + t * 1280 + 16 * h + 8), k2 = lds64(kb + t * 1280 + 32);
      f32x16 s = MFMA32((u32x4{k0.x, k0.y, k1.x, k1.y}), q0f, cin);
      return MFMA32((u32x4{k2.x, k2.y, k2.x, k2.y}), q1f, s);
    };
    f32x16 cz;
#pragma unroll
    for (int e = 0; e < 16; ++e) cz[e] = 0.f;
    float mx;
    {
      f32x16 s8 = qk(8, cz);
      if (tmin == 0) {
        const f32x16 s0 = qk(0, cz);
#pragma unroll
        for (int e = 0; e < 16; ++e) s8[e] = ((e & 3) + 8 * (e >> 2) > qlim) ? s0[e] : s8[e];
      } else {
#pragma unroll
        for (int e = 0; e < 16; ++e) s8[e] = ((e & 3) + 8 * (e >> 2) > qlim) ? -3.0e38f : s8[e];
      }
      mx = __builtin_fmaxf(s8[0], s8[1]);
#pragma unroll
      for (int e = 2; e < 16; e += 2) mx = max3(mx, s8[e], s8[e + 1]);
    }
#pragma unroll
    for (int t = 1; t < 8; ++t)
      if (t >= tmin) {
        const f32x16 s = qk(t, cz);
#pragma unroll
        for (int e = 0; e < 16; e += 2) mx = max3(mx, s[e], s[e + 1]);
      }
    mx = max32(mx);
    f32x16 cm, oacc;
#pragma unroll
    for (int e = 0; e < 16; ++e) { cm[e] = -mx; oacc[e] = 0.f; }
    auto pv = [&](int t, const float (&pp)[16]) LAM {
#pragma unroll
      for (int s2 = 0; s2 < 2; ++s2) {
        const u32x4 pf = {pk2(pp[8 * s2], pp[8 * s2 + 1]), pk2(pp[8 * s2 + 2], pp[8 * s2 + 3]), pk2(pp[8 * s2 + 4], pp[8 * s2 + 5]), pk2(pp[8 * s2 + 6], pp[8 * s2 + 7])};
        const u32 va = vb + (32 * t + 16 * s2) * 48;
        oacc = MFMA32(ldstr2(va, va + 384), pf, oacc);
      }
    };
    {
      const f32x16 s8 = qk(8, cm);
      float p8[16], p0[16];
      if (tmin == 0) {
        const f32x16 s0 = qk(0, cm);
#pragma unroll
        for (int e = 0; e < 16; ++e) {
          const bool m0 = (e & 3) + 8 * (e >> 2) > qlim;
          const float pe = __builtin_amdgcn_exp2f(m0 ? s0[e] : s8[e]);
          p0[e] = m0 ? pe : 0.f; p8[e] = m0 ? 0.f : pe;
        }
        pv(0, p0);
      } else {
#pragma unroll
        for (int e = 0; e < 16; ++e) { const bool m0 = (e & 3) + 8 * (e >> 2) > qlim; const float pe = __builtin_amdgcn_exp2f(s8[e]); p8[e] = m0 ? 0.f : pe; }
      }
      pv(8, p8);
    }
#pragma unroll
    for (int t = 1; t < 8; ++t)
      if (t >= tmin) {
        const f32x16 s = qk(t, cm);
        float pp[16];
#pragma unroll
        for (int e = 0; e < 16; ++e) pp[e] = __builtin_amdgcn_exp2f(s[e]);
        pv(t, pp);
      }
    const float lx = xor32f(oacc[8]);
    const float inv = __builtin_amdgcn_rcpf(h ? oacc[8] : lx);
    const u32 ob = WS_OAF + (u32)((mt * 4 + qb) * 40) * 1024 + c32 * 16;
#pragma unroll
    for (int g = 0; g < 3; ++g) {
      const int k = 40 * j + 20 * hh + 8 * g + 4 * h;
      const u32x2 o = {pk2(oacc[4 * g] * inv, oacc[4 * g + 1] * inv), pk2(oacc[4 * g + 2] * inv, oacc[4 * g + 3] * inv)};
      if (g < 2 || h == 0) __builtin_amdgcn_raw_buffer_store_b64(o, rs, ob + (k >> 3) * 512 + ((k >> 2) & 1) * 8, 0, 16);
    }
  }
  STAMP(5);
  WAITV(0);
  BAR();
  if (tid == 0) signal(ws, WS_CNT + mt * 256);
  STAMP(6);
  const char* wgb = (const char*)p.wg + 192 * j;
  const char* wub = (const char*)p.wu + 192 * j;
  const u32 vgu = (u32)((lane >> 2) * 3072 + (lane & 3) * 16);
  issue_wstages<false, 8, 2>(wos);
  issue_gu<0, 0, 11>(wgb, wub, vgu, w);
  if (w >= 1 && w <= 3) {
#pragma unroll
    for (int i = 0; i < 5; ++i) PIN(csw[i]);
    const int rg = lane >> 3;
    float cs[16];
#pragma unroll
    for (int e = 0; e < 16; ++e) cs[e] = 0.f;
#pragma unroll
    for (int i = 0; i < 5; ++i) {
      const float g = ldsf(L_MODS2 + (5 * rg + i) * 4), s = ldsf(L_MODS2 + 160 + (5 * rg + i) * 4);
#pragma unroll
      for (int e = 0; e < 4; ++e) {
        const float x0 = lo16(csw[i][e]), x1 = hi16(csw[i][e]);
        cs[2 * e] = __builtin_fmaf(g, x0, cs[2 * e]); cs[2 * e + 1] = __builtin_fmaf(g, x1, cs[2 * e + 1]);
        cs[8 + 2 * e] = __builtin_fmaf(s, x0, cs[8 + 2 * e]); cs[8 + 2 * e + 1] = __builtin_fmaf(s, x1, cs[8 + 2 * e + 1]);
      }
    }
    float r8[8], r4[4];
#pragma unroll
    for (int e = 0; e < 8; ++e) {
      auto r = __builtin_amdgcn_permlane32_swap(f2u(cs[e]), f2u(cs[8 + e]), false, false);
      r8[e] = u2f(r[0]) + u2f(r[1]);
    }
#pragma unroll
    for (int e = 0; e < 4; ++e) {
      auto r = __builtin_amdgcn_permlane16_swap(f2u(r8[e]), f2u(r8[4 + e]), false, false);
      r4[e] = u2f(r[0]) + u2f(r[1]);
    }
#pragma unroll
    for (int e = 0; e < 4; ++e) r4[e] += dppf<0x128>(r4[e]);
    const int ch = (w - 1) * 8 + (lane & 7);
    const u32 off = WS_CSP + (u32)(j * 16 + mt) * 1536 + ch * 64 + (lane >> 5) * 32 + ((lane >> 4) & 1) * 16;
    if ((lane & 8) == 0)
      __builtin_amdgcn_raw_buffer_store_b128(u32x4{f2u(r4[0]), f2u(r4[1]), f2u(r4[2]), f2u(r4[3])}, rs, off, 0, 16);
    WAITV(0);
    if (lane == 0) signal(ws, WS_CNT + (48 + j) * 256);
  }
  if (w == 0) poll_ge(ws, WS_CNT + mt * 256, 16u);
  BAR();
  STAMP(7);
  u32 wbase[5];
#pragma unroll
  for (int m = 0; m < 5; ++m) {
    const int nt = (m + 3 * kh) % 5, q = (lane >> 2) & 3, pp = lane & 3;
    const u32 bx = g4 * 1024 + (nt >> 1) * 256 + q * 64 + (((nt & 1) ^ (g4 & 1)) * 32) + pp * 8;
    const u32 by = 4096 + (g4 >> 1) * 256 + (g4 & 1) * 128 + q * 32 + pp * 8;
    wbase[m] = kh * 5120 + (nt < 4 ? bx : by);
  }
  const u32 avoff = (g4 >> 1) * 1024 + (g4 & 1) * 512 + (16 * (tt & 1) + c16) * 16;
  const int tt64 = mt * 4 + 2 * th + (tt >> 1);
  f32x4 c5[5];
  u32x2 x1k[3];
  {
    const char* ab = ws + WS_OAF + (size_t)(tt64 * 40 + 20 * kh) * 1024;
#pragma unroll
    for (int s = 0; s < 10; s += 2) { gld_sc1<0>(a[s], ab + s * 2048, avoff); gld_sc1<2048>(a[s + 1], ab + s * 2048, avoff); }
#pragma unroll
    for (int m = 0; m < 5; ++m) c5[m] = f32x4{0.f, 0.f, 0.f, 0.f};
#pragma unroll
    for (int s = 0; s < 10; ++s) {
      WAITV(9 - s);
      if (s == 8) BAR();
      PIN(a[s]);
#pragma unroll
      for (int m = 0; m < 5; ++m) c5[m] = MFMA16(ldstr2(wbase[m] + wo_slot(s), wbase[m] + wo_slot(s) + 512), a[s], c5[m]);
    }
    STAMP(8);
    const u32 ew = 67584 + (tt * 5) * 1024 + l16;
    stf4(ew + ((3 + 3 * kh) % 5) * 1024, c5[3]);
    stf4(ew + ((4 + 3 * kh) % 5) * 1024, c5[4]);
    if (kh) stf4(ew, c5[2]);
    BAR();
#pragma unroll
    for (int m = 0; m < 3; ++m) PIN(xr[m]);
    float xs[12];
    const u32 xb = WS_X1 + (u32)(tt64 * 40 + 5 * n8) * 1024 + (g4 >> 1) * 512 + (16 * (tt & 1) + c16) * 16 + (g4 & 1) * 8;
#pragma unroll
    for (int m = 0; m < 3; ++m) {
      if (m < 2 || kh == 0) {
        const int nt = (m + 3 * kh) % 5;
        const f32x4 o = ldsf4(ew + nt * 1024), ga = ldsf4(L_MODS + (16 * nt + 4 * g4) * 4), g2 = ldsf4(L_MODS + 320 + (16 * nt + 4 * g4) * 4);
        const u32 y01 = pk2(c5[m][0] + o[0], c5[m][1] + o[1]), y23 = pk2(c5[m][2] + o[2], c5[m][3] + o[3]);
        const u32 t01 = pk2(ga[0] * lo16(y01), ga[1] * hi16(y01)), t23 = pk2(ga[2] * lo16(y23), ga[3] * hi16(y23));
        x1k[m] = u32x2{pk2(lo16(xr[m].x) + lo16(t01), hi16(xr[m].x) + hi16(t01)), pk2(lo16(xr[m].y) + lo16(t23), hi16(xr[m].y) + hi16(t23))};
        xs[4 * m] = lo16(x1k[m].x); xs[4 * m + 1] = hi16(x1k[m].x); xs[4 * m + 2] = lo16(x1k[m].y); xs[4 * m + 3] = hi16(x1k[m].y);
        __builtin_amdgcn_raw_buffer_store_b64(u32x2{pk2(xs[4 * m] * g2[0], xs[4 * m + 1] * g2[1]), pk2(xs[4 * m + 2] * g2[2], xs[4 * m + 3] * g2[3])}, rs, xb + nt * 1024, 0, 16);
      } else {
        x1k[m] = u32x2{0u, 0u};
        xs[8] = xs[9] = xs[10] = xs[11] = 0.f;
      }
    }
    float s = 0.f;
#pragma unroll
    for (int e = 0; e < 12; ++e) s += xs[e];
    s = sum32(sum16(s));
    const float mean = s * (kh ? 1.f / 32.f : 1.f / 48.f);
    float q = 0.f;
#pragma unroll
    for (int e = 0; e < 8; ++e) { const float d = xs[e] - mean; q = __builtin_fmaf(d, d, q); }
    if (kh == 0) {
#pragma unroll
      for (int e = 8; e < 12; ++e) { const float d = xs[e] - mean; q = __builtin_fmaf(d, d, q); }
    }
    q = sum32(sum16(q));
    if (g4 == 0)
      __builtin_amdgcn_raw_buffer_store_b64(u32x2{f2u(mean), f2u(q)}, rs, WS_STAT + (u32)((tt64 * 8 + n8) * 32 + 16 * (tt & 1) + c16) * 16 + kh * 8, 0, 16);
  }
  WAITV(0);
  BAR();
  if (tid == 0) signal(ws, WS_CNT + (16 + mt) * 256);
  STAMP(9);
  issue_gu<0, 11, 20>(wgb, wub, vgu, w);
  issue_gu<1, 0, 12>(wgb, wub, vgu, w);
  issue_gu<1, 12, 17>(wgb, wub, vgu, w);
  if (w == 0) { poll_ge(ws, WS_CNT + (16 + mt) * 256, 16u); poll_ge(ws, WS_CNT + (48 + j) * 256, 48u); }
  BAR();
  STAMP(10);
  {
    u32x4 stv[4], csv[4];
    {
      const char* sb = ws + WS_STAT + (size_t)(mt * 4 + tt) * 4096;
      const u32 so = h * 2048 + c32 * 16;
      gld_sc1<0>(stv[0], sb, so); gld_sc1<512>(stv[1], sb, so); gld_sc1<1024>(stv[2], sb, so); gld_sc1<1536>(stv[3], sb, so);
      const char* cb = ws + WS_CSP + (size_t)(j * 16 + 4 * (w >> 1)) * 1536;
      const u32 co = ((w & 1) * 48 + (lane < 48 ? lane : 47)) * 16;
      gld_sc1<0>(csv[0], cb, co); gld_sc1<1536>(csv[1], cb, co); gld_sc1<0>(csv[2], cb + 3072, co); gld_sc1<1536>(csv[3], cb + 3072, co);
      const char* ab = ws + WS_X1 + (size_t)((mt * 4 + tt) * 40 + 20 * kh) * 1024;
#pragma unroll
      for (int s = 0; s < 5; ++s) {
        gld_sc1<0>(a[4 * s], ab + s * 4096, l16); gld_sc1<1024>(a[4 * s + 1], ab + s * 4096, l16);
        gld_sc1<2048>(a[4 * s + 2], ab + s * 4096, l16); gld_sc1<3072>(a[4 * s + 3], ab + s * 4096, l16);
      }
    }
    WAITV(20);
#pragma unroll
    for (int i = 0; i < 4; ++i) { PIN(stv[i]); PIN(csv[i]); }
    {
      f32x4 sm;
#pragma unroll
      for (int e = 0; e < 4; ++e) sm[e] = (u2f(csv[0][e]) + u2f(csv[1][e])) + (u2f(csv[2][e]) + u2f(csv[3][e]));
      if (lane < 48) stf4(L_GS + (w >> 1) * 1536 + ((w & 1) * 48 + lane) * 16, sm);
    }
    float rr, rm;
    {
      float s = 0.f;
#pragma unroll
      for (int i = 0; i < 4; ++i) s += 48.f * u2f(stv[i][0]) + 32.f * u2f(stv[i][2]);
      const float mean = sum32(s) * (1.f / 640.f);
      float q = 0.f;
#pragma unroll
      for (int i = 0; i < 4; ++i) {
        const float d0 = u2f(stv[i][0]) - mean, d1 = u2f(stv[i][2]) - mean;
        q += u2f(stv[i][1]) + u2f(stv[i][3]) + 48.f * d0 * d0 + 32.f * d1 * d1;
      }
      q = sum32(q);
      const float den = rbf(__builtin_amdgcn_sqrtf(rbf(rbf(q * (1.f / 640.f)) + 1e-5f)));
      rr = __builtin_amdgcn_rcpf(den); rm = -rr * rbf(mean);
    }
    const u32 gl = h * 512 + ((lane >> 2) & 3) * 64 + ((((lane >> 4) & 1) ^ kh) * 32) + (lane & 3) * 8;
    const u32 sw = 20480 + w * 4096 + l16, sr = 20480 + (w ^ 4) * 4096 + l16;
    const u32 actb = WS_ACT + (u32)((mt * 4 + tt) * 96 + 6 * j + kh) * 1024 + l16;
    f32x16 ag, au;
    float keep[16];
    auto epilogue = [&](int pp) LAM {
      u32x4 o;
#pragma unroll
      for (int q = 0; q < 2; ++q) {
        const u32 tb = L_T2 + (4 * pp + 2 * kh + q) * 64 + h * 16;
        const f32x4 cg = ldsf4(tb), bg = ldsf4(tb + 32), cu = ldsf4(tb + 768), bu = ldsf4(tb + 800);
        float r4[4];
#pragma unroll
        for (int i = 0; i < 4; ++i) {
          const float g = fma1(keep[4 * q + i], rr, fma1(cg[i], rm, bg[i]));
          const float u = fma1(keep[8 + 4 * q + i], rr, fma1(cu[i], rm, bu[i]));
          const float e = __builtin_amdgcn_exp2f(mul1(g, -1.4426950408889634f));
          r4[i] = mul1(mul1(g, u), __builtin_amdgcn_rcpf(add1(e, 1.f)));
        }
        o[2 * q] = pk2(r4[0], r4[1]); o[2 * q + 1] = pk2(r4[2], r4[3]);
      }
      __builtin_amdgcn_raw_buffer_store_b128(o, rs, actb + pp * 2048, 0, 16);
    };
#pragma unroll
    for (int pp = 0; pp < 3; ++pp) {
#pragma unroll
      for (int e = 0; e < 16; ++e) { ag[e] = 0.f; au[e] = 0.f; }
      constexpr int MID[3] = {11, 12, 9};
#pragma unroll
      for (int i = 0; i < 20; ++i) {
        if (i == MID[pp]) {
          if (pp == 1) WAITV(6);
          if (pp == 2) WAITV(1);
          BAR();
          if (pp == 0) {
            issue_gu<1, 17, 20>(wgb, wub, vgu, w);
            if (tid < 96) {
              const u32 t4 = L_GS + tid * 16;
              stf4(L_T2 + tid * 16, (ldsf4(t4) + ldsf4(t4 + 1536)) + (ldsf4(t4 + 3072) + ldsf4(t4 + 4608)));
            }
          }
          if (pp == 1) issue_gu<2, 9, 20>(wgb, wub, vgu, w);
        }
        if (pp == 0) { if (i < 11) WAITV(19 - i); else WAITV(19 - i + 2); PIN(a[i]); }
        const u32 ua = gl + (kh ? gu_unit(pp, 1, i) : gu_unit(pp, 0, i)) * 2048;
        ag = MFMA32(ldstr2(ua, ua + 256), a[i], ag);
        au = MFMA32(ldstr2(ua + 1024, ua + 1280), a[i], au);
        if (pp > 0 && i == 2) epilogue(pp - 1);
      }
      stf4(sw, f32x4{ag[8], ag[9], ag[10], ag[11]}); stf4(sw + 1024, f32x4{ag[12], ag[13], ag[14], ag[15]});
      stf4(sw + 2048, f32x4{au[8], au[9], au[10], au[11]}); stf4(sw + 3072, f32x4{au[12], au[13], au[14], au[15]});
      if (pp == 1) WAITV(7);
      BAR();
      if (pp == 0) issue_gu<2, 0, 9>(wgb, wub, vgu, w);
#pragma unroll
      for (int q = 0; q < 2; ++q) {
        const f32x4 og = ldsf4(sr + q * 1024), ou = ldsf4(sr + 2048 + q * 1024);
#pragma unroll
        for (int i = 0; i < 4; ++i) { keep[4 * q + i] = ag[4 * q + i] + og[i]; keep[8 + 4 * q + i] = au[4 * q + i] + ou[i]; }
      }
    }
    STAMP(11);
    epilogue(2);
  }
  WAITV(0);
  BAR();
  if (tid == 0) signal(ws, WS_CNT + (32 + mt) * 256);
  STAMP(12);
  {
    WStage<true> wds; wds.init(p.wd, 768, n8, w, lane);
    issue_wstages<true, 0, 15>(wds);
    if (w == 0) poll_ge(ws, WS_CNT + (32 + mt) * 256, 16u);
    BAR();
    STAMP(13);
    const char* ab = ws + WS_ACT + (size_t)(tt64 * 96 + 48 * kh) * 1024;
#pragma unroll
    for (int s = 0; s < 24; s += 2) { gld_sc1<0>(a[s], ab + s * 2048, avoff); gld_sc1<2048>(a[s + 1], ab + s * 2048, avoff); }
#pragma unroll
    for (int m = 0; m < 5; ++m) c5[m] = f32x4{0.f, 0.f, 0.f, 0.f};
#pragma unroll
    for (int s = 0; s < 24; ++s) {
      if (s == 5) { BAR(); issue_wstages<true, 15, 5>(wds); }
      if (s == 9) { BAR(); issue_wstages<true, 20, 4>(wds); }
      if (s < 5) WAITV(23 - s); else if (s < 9) WAITV(23 - s + 7); else if (s < 15) WAITV(23 - s + 12);
      if (s == 0) BAR();
      if (s == 15) { WAITV(5); BAR(); }
      if (s == 20) { WAITV(0); BAR(); }
      PIN(a[s]);
#pragma unroll
      for (int m = 0; m < 5; ++m) c5[m] = MFMA16(ldstr2(wbase[m] + wd_slot(s), wbase[m] + wd_slot(s) + 512), a[s], c5[m]);
    }
    STAMP(14);
    const u32 ew = 92160 + (tt * 5) * 1024 + l16;
    stf4(ew + ((3 + 3 * kh) % 5) * 1024, c5[3]);
    stf4(ew + ((4 + 3 * kh) % 5) * 1024, c5[4]);
    if (kh) stf4(ew, c5[2]);
    BAR();
#pragma unroll
    for (int m = 0; m < 3; ++m)
      if (m < 2 || kh == 0) {
        const int nt = (m + 3 * kh) % 5;
        const f32x4 o = ldsf4(ew + nt * 1024), gb = ldsf4(L_MODS + 640 + (16 * nt + 4 * g4) * 4);
        const u32 y01 = pk2(c5[m][0] + o[0], c5[m][1] + o[1]), y23 = pk2(c5[m][2] + o[2], c5[m][3] + o[3]);
        const u32 t01 = pk2(gb[0] * lo16(y01), gb[1] * hi16(y01)), t23 = pk2(gb[2] * lo16(y23), gb[3] * hi16(y23));
        const u32x2 r = {pk2(lo16(x1k[m].x) + lo16(t01), hi16(x1k[m].x) + hi16(t01)), pk2(lo16(x1k[m].y) + lo16(t23), hi16(x1k[m].y) + hi16(t23))};
        *(GAS u32x2*)((GAS char*)p.out + (size_t)tok64 * 1280 + (80 * n8 + 16 * nt + 4 * g4) * 2) = r;
      }
  }
}
}

extern "C" void kernel_launch(void* const* d_in, const int* in_sizes, int n_in, void* d_out, int out_size, void* d_ws, size_t ws_size,
                              hipStream_t stream) {
  (void)in_sizes; (void)n_in; (void)out_size; (void)ws_size;
  hipFuncSetAttribute((const void*)k2, hipFuncAttributeMaxDynamicSharedMemorySize, LDS_BYTES);
  P1 q;
  q.x = (const u16*)d_in[0]; q.wqkv = (const u16*)d_in[1]; q.wo = (const u16*)d_in[2]; q.wg = (const u16*)d_in[4]; q.wu = (const u16*)d_in[5];
  q.wd = (const u16*)d_in[6]; q.cvec = (const u16*)d_in[7]; q.wmod = (const u16*)d_in[8]; q.ws = (char*)d_ws + ((ws_size - WS_END) & ~(size_t)0xfffff);
  k1<<<K1_BLOCKS, 256, 0, stream>>>(q);
  P2 p;
  p.x = (const u16*)d_in[0]; p.wo = (const u16*)d_in[2]; p.wg = (const u16*)d_in[4]; p.wu = (const u16*)d_in[5]; p.wd = (const u16*)d_in[6];
  p.out = (u16*)d_out; p.ws = (char*)d_ws + ((ws_size - WS_END) & ~(size_t)0xfffff);
  k2<<<256, 512, LDS_BYTES, stream>>>(p);
}
```

```cpp
#include <hip/hip_runtime.h>
#include <hip/hip_bf16.h>
#include <stdint.h>

#define GAS __attribute__((address_space(1)))
#define LAS __attribute__((address_space(3)))
#if defined(__HIP_DEVICE_COMPILE__)
#define NOPK __attribute__((target("no-packed-fp32-ops")))
#else
#define NOPK
#endif
#define DI __device__ __forceinline__ NOPK
#define LAM __attribute__((always_inline)) NOPK
typedef unsigned int u32;
typedef unsigned short u16;
typedef u32 u32x4 __attribute__((ext_vector_type(4)));
typedef u32 u32x2 __attribute__((ext_vector_type(2)));
typedef float f32x2 __attribute__((ext_vector_type(2)));
typedef float f32x4 __attribute__((ext_vector_type(4)));
typedef float f32x16 __attribute__((ext_vector_type(16)));
typedef __bf16 bf16x2 __attribute__((ext_vector_type(2)));
typedef __bf16 bf16x4 __attribute__((ext_vector_type(4)));
typedef __bf16 bf16x8 __attribute__((ext_vector_type(8)));
typedef GAS u32 gu32;
typedef _Float16 f16x8 __attribute__((ext_vector_type(8)));
typedef _Float16 f16x2 __attribute__((ext_vector_type(2)));

#define STAMPS 0

namespace {
constexpr int S = 2048, D = 640, F = 1536;
constexpr u32 WS_KVF = 0;
constexpr u32 WS_CNT = 32768;
constexpr u32 WS_MODP = 65536;
constexpr u32 WS_CSP = WS_MODP + 61440;
constexpr u32 WS_STAT = WS_CSP + 16 * 16 * 1536;
constexpr u32 WS_N1 = WS_STAT + 64 * 8 * 32 * 16;
constexpr u32 WS_OAF = WS_N1 + 64 * 40 * 1024;
constexpr u32 WS_X1 = WS_OAF + 64 * 40 * 1024;
constexpr u32 WS_ACT = WS_X1 + 64 * 40 * 1024;
constexpr u32 KVB = 24576;
constexpr u32 WS_KVX = WS_ACT + 64 * 96 * 1024;
constexpr u32 WQB = 153600;
constexpr u32 WS_WQF = WS_KVX + 256 * KVB;
constexpr u32 WOB = 102400, WGB = 245760, WDB = 245760;
constexpr u32 WS_WOF = WS_WQF + 16 * WQB;
constexpr u32 WS_WGF = WS_WOF + 8 * WOB;
constexpr u32 WS_WDF = WS_WGF + 16 * WGB;
constexpr u32 WS_DBG = WS_WDF + 8 * WDB;
constexpr u32 WS_DBG1 = WS_DBG + 256 * 32 * 8;
constexpr u32 WS_END = WS_DBG1 + 1024 * 16;
constexpr u32 L_MISC = 153600;
constexpr u32 L_GS = L_MISC;
constexpr u32 L_MODS = L_MISC + 6144;
constexpr u32 L_T2 = L_MISC + 7168;
constexpr u32 L_FLAG = L_MISC + 9728;
constexpr u32 LDS_BYTES = 163840;
constexpr u32 L_K = 0, L_V = 30720;
constexpr float QSCALE = 0.22360679774997896f * 1.4426950408889634f;
constexpr int K1_BLOCKS = 488;
#define ATT_LIMIT 1.0e18f
constexpr u32 SPIN_CAP = 1u << 20;

DI float u2f(u32 v) { return __builtin_bit_cast(float, v); }
DI u32 f2u(float v) { return __builtin_bit_cast(u32, v); }
DI int tidx() { return (int)__builtin_amdgcn_workitem_id_x(); }
DI int bidx() { return (int)__builtin_amdgcn_workgroup_id_x(); }
DI float bf2f(u32 v) { return u2f(v << 16); }
DI float lo16(u32 v) { return u2f(v << 16); }
DI float hi16(u32 v) { return u2f(v & 0xffff0000u); }
DI u32 pk2(float a, float b) { f32x2 v = {a, b}; return __builtin_bit_cast(u32, __builtin_convertvector(v, bf16x2)); }
DI u32 pkh2(float a, float b) { f32x2 v = {a, b}; return __builtin_bit_cast(u32, __builtin_convertvector(v, f16x2)); }
DI float hlo(u32 v) { return (float)__builtin_bit_cast(f16x2, v).x; }
DI float hhi(u32 v) { return (float)__builtin_bit_cast(f16x2, v).y; }
DI float rbf(float a) { return lo16(pk2(a, a)); }
DI float fma1(float a, float b, float c) { return __builtin_fmaf(a, b, c); }
DI float mul1(float a, float b) { return a * b; }
DI float add1(float a, float b) { return a + b; }
DI float max3(float a, float b, float c) { return __builtin_fmaxf(__builtin_fmaxf(a, b), c); }
DI float xor32f(float v) {
  auto r = __builtin_amdgcn_permlane32_swap(f2u(v), f2u(v), false, false);
  return u2f((tidx() & 32) ? r[0] : r[1]);
}
DI float sum32(float v) { auto r = __builtin_amdgcn_permlane32_swap(f2u(v), f2u(v), false, false); return u2f(r[0]) + u2f(r[1]); }
DI float sum16(float v) { auto r = __builtin_amdgcn_permlane16_swap(f2u(v), f2u(v), false, false); return u2f(r[0]) + u2f(r[1]); }
DI float max32(float v) { auto r = __builtin_amdgcn_permlane32_swap(f2u(v), f2u(v), false, false); return __builtin_fmaxf(u2f(r[0]), u2f(r[1])); }
template <int CTRL> DI float dppf(float v) { return u2f(__builtin_amdgcn_update_dpp(0u, f2u(v), CTRL, 0xf, 0xf, true)); }

#define MFMA32(a, b, c) __builtin_amdgcn_mfma_f32_32x32x16_bf16(__builtin_bit_cast(bf16x8, a), __builtin_bit_cast(bf16x8, b), c, 0, 0, 0)
#define MFMA32H(a, b, c) __builtin_amdgcn_mfma_f32_32x32x16_f16(__builtin_bit_cast(f16x8, a), __builtin_bit_cast(f16x8, b), c, 0, 0, 0)
#define MFMA16(a, b, c) __builtin_amdgcn_mfma_f32_16x16x32_bf16(__builtin_bit_cast(bf16x8, a), __builtin_bit_cast(bf16x8, b), c, 0, 0, 0)
#define SGB(m, n, id) __builtin_amdgcn_sched_group_barrier(m, n, id)
#define SB() __builtin_amdgcn_sched_barrier(0)
template <bool B> struct BoolC { static constexpr bool v = B; };
template <int I> struct IC { static constexpr int v = I; };
template <int I, int N, class Fn> DI void sfor(Fn&& f) { if constexpr (I < N) { f(IC<I>{}); sfor<I + 1, N>(f); } }
#define PIN(x) asm volatile("" : "+v"(x))
#define WAITV(n) asm volatile("s_waitcnt vmcnt(%0)" ::"i"(n))
#define WAITVM(n) asm volatile("s_waitcnt vmcnt(%0)" ::"i"(n) : "memory")
#define BAR() asm volatile("s_waitcnt lgkmcnt(0)\n\ts_barrier" ::: "memory")
#define RLX_AGENT __ATOMIC_RELAXED, __HIP_MEMORY_SCOPE_AGENT

DI u32x4 lds128(u32 a) { return *(const LAS u32x4*)(size_t)a; }
DI f32x4 ldsf4(u32 a) { return *(const LAS f32x4*)(size_t)a; }
DI u32x2 lds64(u32 a) { return *(const LAS u32x2*)(size_t)a; }
DI float ldsf(u32 a) { return *(const LAS float*)(size_t)a; }
DI void st128(u32 a, u32x4 v) { *(LAS u32x4*)(size_t)a = v; }
DI void stf4(u32 a, f32x4 v) { *(LAS f32x4*)(size_t)a = v; }
DI void st64(u32 a, u32x2 v) { *(LAS u32x2*)(size_t)a = v; }
DI u32x2 ldstr(u32 a) { return __builtin_bit_cast(u32x2, __builtin_amdgcn_ds_read_tr16_b64_v4bf16((LAS bf16x4*)(size_t)a)); }
DI u32x4 ldstr2(u32 a, u32 b) { u32x2 lo = ldstr(a), hi = ldstr(b); return u32x4{lo.x, lo.y, hi.x, hi.y}; }

template <int IMM, bool SC1> DI void dmai(const char* sbase, u32 voff, u32 lds) {
  if constexpr (SC1) asm volatile("s_mov_b32 m0, %2\n\ts_nop 0\n\tglobal_load_lds_dwordx4 %0, %1 offset:%3 sc1" ::"v"(voff), "s"(sbase), "s"(lds), "i"(IMM));
  else asm volatile("s_mov_b32 m0, %2\n\ts_nop 0\n\tglobal_load_lds_dwordx4 %0, %1 offset:%3" ::"v"(voff), "s"(sbase), "s"(lds), "i"(IMM));
}
DI void dma(const char* sbase, u32 voff, u32 lds) { dmai<0, false>(sbase, voff, lds); }
DI void gld32(u32& r, const char* sbase, u32 voff) { asm volatile("global_load_dword %0, %1, %2" : "=v"(r) : "v"(voff), "s"(sbase)); }
DI void gld32_sc1(u32& r, const char* sbase, u32 voff) { asm volatile("global_load_dword %0, %1, %2 sc1" : "=v"(r) : "v"(voff), "s"(sbase)); }
template <int IMM> DI void gld_sc1(u32x4& r, const char* sbase, u32 voff) {
  asm volatile("global_load_dwordx4 %0, %1, %2 offset:%3 sc1" : "=v"(r) : "v"(voff), "s"(sbase), "i"(IMM));
}
template <int IMM> DI void gld(u32x4& r, const char* sbase, u32 voff) {
  asm volatile("global_load_dwordx4 %0, %1, %2 offset:%3" : "=v"(r) : "v"(voff), "s"(sbase), "i"(IMM));
}
DI void gld64(u32x2& r, const char* sbase, u32 voff) {
  asm volatile("global_load_dwordx2 %0, %1, %2" : "=v"(r) : "v"(voff), "s"(sbase));
}
DI void poll_ge(const char* ws, u32 off, u32 want) {
  gu32* p = (gu32*)(ws + off);
  for (u32 spins = 0; spins < SPIN_CAP; ++spins) {
    if ((u32)__builtin_amdgcn_readfirstlane(__hip_atomic_load(p, RLX_AGENT)) >= want) break;
    __builtin_amdgcn_s_sleep(1);
  }
  __builtin_amdgcn_fence(__ATOMIC_ACQUIRE, "wavefront");
}
DI void signal(const char* ws, u32 off) { __hip_atomic_fetch_add((gu32*)(ws + off), 1u, RLX_AGENT); }

struct P1 {
  const u16 *x, *wqkv, *wo, *cvec, *wmod;
  char* ws;
};
#define K1AUX 0
__global__ void __launch_bounds__(256) NOPK k1(P1 p) {
  __shared__ float red[32 * 64 + 4 * 64 + 320];
  const int b = bidx(), t = tidx();
  char* ws = p.ws;
  const __amdgpu_buffer_rsrc_t rs = __builtin_amdgcn_make_buffer_rsrc(p.ws, 0, WS_END, 0x00020000);
  if (b < 120) {
    const int ch = t & 7, r = t >> 3, col0 = 64 * (b >> 1), row0 = 320 * (b & 1);
    const u32x4 cv = *(const u32x4*)(p.cvec + row0 + 8 * (t < 40 ? t : 39));
    u32x4 wv[10];
#pragma unroll
    for (int i = 0; i < 10; ++i) wv[i] = *(const u32x4*)(p.wmod + (size_t)(row0 + r + 32 * i) * 3840 + col0 + 8 * ch);
    if (t < 40) {
#pragma unroll
      for (int e = 0; e < 8; ++e) {
        const float c = (e & 1) ? hi16(cv[e >> 1]) : lo16(cv[e >> 1]);
        red[2304 + 8 * t + e] = rbf(c * __builtin_amdgcn_rcpf(1.f + __builtin_amdgcn_exp2f(-1.4426950408889634f * c)));
      }
    }
    BAR();
    float acc[8] = {0, 0, 0, 0, 0, 0, 0, 0};
#pragma unroll
    for (int i = 0; i < 10; ++i) {
      const float s = red[2304 + r + 32 * i];
#pragma unroll
      for (int e = 0; e < 4; ++e) { acc[2 * e] = fma1(s, lo16(wv[i][e]), acc[2 * e]); acc[2 * e + 1] = fma1(s, hi16(wv[i][e]), acc[2 * e + 1]); }
    }
    *(f32x4*)&red[r * 64 + 8 * ch] = f32x4{acc[0], acc[1], acc[2], acc[3]};
    *(f32x4*)&red[r * 64 + 8 * ch + 4] = f32x4{acc[4], acc[5], acc[6], acc[7]};
    BAR();
    { const int c = t & 63, part = t >> 6; float s = 0;
#pragma unroll
      for (int i = 0; i < 8; ++i) s += red[(part * 8 + i) * 64 + c];
      red[2048 + part * 64 + c] = s; }
    BAR();
    if (t < 64) {
      const float s = (red[2048 + t] + red[2112 + t]) + (red[2176 + t] + red[2240 + t]);
      __builtin_amdgcn_raw_buffer_store_b32(f2u(s), rs, WS_MODP + ((b & 1) * 3840 + col0 + t) * 4, 0, K1AUX);
    }
    if (b == 0) {
      *(u32*)(ws + WS_KVF + t * 128) = 0u;
      if (t < 72) *(u32*)(ws + WS_CNT + t * 256) = 0u;
    }
  } else if (b < 248) {
    const int hb = b - 120, c = t & 15, tok = 16 * (hb & 1) + (t >> 4);
    const u16* row = p.x + (size_t)(32 * (hb >> 1) + tok) * D;
    u32x4 v[5];
#pragma unroll
    for (int i = 0; i < 5; ++i) v[i] = *(const u32x4*)(row + 8 * (c + 16 * i));
    float s = 0;
#pragma unroll
    for (int i = 0; i < 5; ++i)
#pragma unroll
      for (int e = 0; e < 4; ++e) s += lo16(v[i][e]) + hi16(v[i][e]);
    s += dppf<0xB1>(s); s += dppf<0x4E>(s); s += dppf<0x141>(s); s += dppf<0x140>(s);
    const float mean = s * (1.f / 640.f);
    float q = 0;
#pragma unroll
    for (int i = 0; i < 5; ++i)
#pragma unroll
      for (int e = 0; e < 4; ++e) { const float a = lo16(v[i][e]) - mean, bb = hi16(v[i][e]) - mean; q = fma1(a, a, q); q = fma1(bb, bb, q); }
    q += dppf<0xB1>(q); q += dppf<0x4E>(q); q += dppf<0x141>(q); q += dppf<0x140>(q);
    const float mb = rbf(mean), den = rbf(__builtin_amdgcn_sqrtf(rbf(rbf(q * (1.f / 640.f)) + 1e-5f))), rr = __builtin_amdgcn_rcpf(den);
    const u32 dst = WS_N1 + (u32)(hb >> 1) * 40960 + tok * 16;
#pragma unroll
    for (int i = 0; i < 5; ++i) {
      u32x4 o;
#pragma unroll
      for (int e = 0; e < 4; ++e) {
        const u32 d = pk2(lo16(v[i][e]) - mb, hi16(v[i][e]) - mb);
        const u32 n = pk2(lo16(d) * rr, hi16(d) * rr);
        o[e] = pkh2(lo16(n), hi16(n));
      }
      __builtin_amdgcn_raw_buffer_store_b128(o, rs, dst + (c + 16 * i) * 512, 0, K1AUX);
    }
  } else {
    const u16* src; int stride; u32 off0, off1; bool act = true;
    if (b < 408) {
      const int idx = b - 248, ks = idx >> 2, jq = idx & 3, tc = t < 240 ? t : 239;
      act = t < 240;
      const int seg = tc / 80, cp = tc - 80 * seg;
      src = p.wqkv + (size_t)(16 * ks) * 1920 + 640 * seg + 160 * jq + 2 * cp; stride = 1920;
      const int jj = (2 * cp) / 40, within = 2 * cp - 40 * jj, j = 4 * jq + jj;
      const int n = (seg == 1 ? 0 : seg == 2 ? 40 : 80) + within, nb = n >> 5, r = n & 31;
      off0 = WS_WQF + j * WQB + (nb < 3 ? ((nb * 40 + ks) * 64 + r) * 16 : 122880 + ks * 768 + r * 16);
      off1 = off0 + (nb < 3 ? 512 : 384);
    } else {
      const int idx = b - 408, k16 = idx >> 1, tc = t < 160 ? t : 159, n = (idx & 1) * 320 + 2 * tc;
      act = t < 160;
      src = p.wo + (size_t)(16 * k16) * 640 + n; stride = 640;
      const int kh = k16 >= 20, kk = k16 - 20 * kh, st = kk >> 1, H = kk & 1;
      const int ntg = n >> 4, n8 = ntg / 5, nt = ntg - 5 * n8;
      off0 = WS_WOF + n8 * WOB + (st * 10 + kh * 5 + nt) * 1024 + (2 * H * 16 + (n & 15)) * 16;
      off1 = off0 + 256;
    }
    u32 v[16];
#pragma unroll
    for (int i = 0; i < 16; ++i) v[i] = *(const u32*)(src + (size_t)i * stride);
    if (b < 408) {
#pragma unroll
      for (int i = 0; i < 16; ++i) v[i] = pkh2(lo16(v[i]) * 64.f, hi16(v[i]) * 64.f);
    }
    if (act) {
#pragma unroll
      for (int h = 0; h < 2; ++h) {
        u32x4 a, bq;
#pragma unroll
        for (int e = 0; e < 4; ++e) {
          a[e] = __builtin_amdgcn_perm(v[8 * h + 2 * e + 1], v[8 * h + 2 * e], 0x05040100u);
          bq[e] = __builtin_amdgcn_perm(v[8 * h + 2 * e + 1], v[8 * h + 2 * e], 0x07060302u);
        }
        __builtin_amdgcn_raw_buffer_store_b128(a, rs, h ? off1 : off0, 0, K1AUX);
        __builtin_amdgcn_raw_buffer_store_b128(bq, rs, (h ? off1 : off0) + 16, 0, K1AUX);
      }
    }
  }
}

struct P2 {
  const u16 *x, *wg, *wu, *wd;
  u16* out;
  char* ws;
};
#define MAP 0
constexpr u32 C_O = WS_CNT, C_RG = WS_CNT + 48 * 256, C_RD = WS_CNT + 64 * 256;
#define OPQ(x) asm volatile("" : "+s"(x))

DI constexpr int gu_unit(int p, int kh, int i) {
  return p == 0 ? 20 * kh + i
       : p == 1 ? (i < 16 ? 40 + 16 * kh + i : 8 + 20 * kh + (i - 16))
                : (i < 8 ? 12 + 20 * kh + i : 40 + 16 * kh + (i - 8));
}
DI constexpr u32 wo_slot(int s) { return s < 7 ? (72 + 10 * s) * 1024 : (s - 7) * 10240; }
DI constexpr u32 wd_slot(int s) { return (s < 15 ? s : s - 15) * 10240; }
template <int N, int NW, bool SC1> struct Run {
  static constexpr int C = (N + NW - 1) / NW;
  const char* src; u32 lds;
  DI Run(const char* s, u32 l, int x) { int o = x * C; o = (o < N - C ? o : N - C) * 1024; src = s + o; lds = l + o; }
  template <int M> DI void go(u32 l16) const { if constexpr (M < C) dmai<(M & 3) * 1024, SC1>(src + (M >> 2) * 4096, l16, lds + (M >> 2) * 4096); }
  DI void all(u32 l16) const { sfor<0, C>([&](auto m_) LAM { go<decltype(m_)::v>(l16); }); }
};
template <int P, int I0, int I1, int NW> DI auto gu_run(const char* blob, int x) {
  const int k = x >= NW / 2;
  return Run<2 * (I1 - I0), NW / 2, true>(blob + ((P * 2 + k) * 20 + I0) * 2048, (k ? gu_unit(P, 1, I0) : gu_unit(P, 0, I0)) * 2048, x - k * (NW / 2));
}
DI u32 pkfma16(u32 x, u32 y, u32 z) { return __builtin_bit_cast(u32, __builtin_elementwise_fma(__builtin_bit_cast(f16x2, x), __builtin_bit_cast(f16x2, y), __builtin_bit_cast(f16x2, z))); }
DI void gld4_if(int cond, u32x4& r0, u32x4& r1, u32x4& r2, u32x4& r3, const char* sbase, u32 voff) {
  asm volatile("s_cmp_eq_u32 %6, 0\n\ts_cbranch_scc1 .Lskip%=\n\tglobal_load_dwordx4 %0, %4, %5\n\tglobal_load_dwordx4 %1, %4, %5 offset:1024\n\t"
               "global_load_dwordx4 %2, %4, %5 offset:2048\n\tglobal_load_dwordx4 %3, %4, %5 offset:3072\n.Lskip%=:"
               : "+v"(r0), "+v"(r1), "+v"(r2), "+v"(r3) : "v"(voff), "s"(sbase), "s"(cond) : "scc");
}
DI bool has_sent(const u32x4& v) {
  const u32 m = __builtin_elementwise_max(__builtin_elementwise_max(v.x, v.y), __builtin_elementwise_max(v.z, v.w));
  return __builtin_amdgcn_ballot_w64(m == 0xffffffffu) != 0ull;
}
template <int IMM> DI void ensure(u32x4& v, const char* sbase, u32 voff) {
  if (__builtin_expect(has_sent(v), 0)) {
    for (u32 spins = 0; spins < SPIN_CAP; ++spins) {
      gld_sc1<IMM>(v, sbase, voff); WAITV(0); PIN(v);
      if (!has_sent(v)) break;
    }
  }
}
DI void gate(const char* sbase, u32 voff) {
  for (u32 spins = 0; spins < SPIN_CAP; ++spins) {
    u32 v;
    gld32_sc1(v, sbase, voff); WAITV(0); PIN(v);
    if (__builtin_amdgcn_ballot_w64(v == 0xffffffffu) == 0ull) break;
  }
}

__global__ void __launch_bounds__(512, 2) NOPK k2(P2 p) {
  const int tid = tidx(), lane = tid & 63;
  int w = __builtin_amdgcn_readfirstlane(tid >> 6);
  const int b = bidx();
  int mt = MAP == 0 ? 4 * ((b & 7) >> 1) + (b >> 6) : (b & 7) * 2 + (b >> 7), j = MAP == 0 ? 8 * (b & 1) + ((b >> 3) & 7) : (b >> 3) & 15;
  const int c32 = lane & 31, h = lane >> 5;
  const char* ws = p.ws;
  const __amdgpu_buffer_rsrc_t rs = __builtin_amdgcn_make_buffer_rsrc(p.ws, 0, WS_END, 0x00020000);
  const u32 l16 = lane * 16;
#define PHASE() OPQ(w); OPQ(mt); OPQ(j); OPQ(ws); const int tt = w & 3, kh = w >> 2, th = j & 1, n8 = j >> 1; (void)tt; (void)kh; (void)th; (void)n8
#define FINE 0
#define CSEL 0
#define FST(k, i) do {} while (0)
#define STAMP(i) do {} while (0)
#define CST(k, i) do {} while (0)
  STAMP(0); CST(3, 18);
  u32x4 a[24];
  u32x4 st[18];
  const int rsw = c32 ^ (16 * (w >> 2));
  const u32 frow = (h * 32 + rsw) * 16;
  u32 imga[8];
  {
    PHASE();
    const char* wq = ws + WS_WQF + (size_t)j * WQB;
    const char* wqw = wq + (kh * 20 + tt) * 1024;
    const u32 wql = (kh * 20 + tt) * 1024;
    const char* n1 = ws + WS_N1 + (size_t)((mt * 4 + tt) * 40 + kh * 20) * 1024;
    auto issA = [&](auto k_) LAM { constexpr int k = decltype(k_)::v; gld<(k & 3) * 1024>(a[k], n1 + (k >> 2) * 4096, l16); };
    auto issW = [&](int s, int nb) LAM { dma(wqw + nb * 40960 + s * 4096, l16, wql + nb * 40960 + s * 4096); };
    u32x4 mp[2];
    u32 mdst; bool plus1;
    {
      const int m1 = tid >= 160, k1 = 4 * (tid - 160 * m1);
      const int u2 = tid - 320, m2 = (u2 * 205) >> 12, e2 = u2 - 20 * m2;
      const int col2 = (m2 == 0 ? 1280 : m2 == 2 ? 3200 : m2 == 4 ? 1920 : 2560) + 80 * (m2 < 3 ? n8 : (mt >> 1)) + 4 * (m2 < 5 ? e2 : 0);
      const int col = tid < 320 ? 640 * m1 + k1 : col2;
#pragma unroll
      for (int q = 0; q < 2; ++q) gld<0>(mp[q], ws + WS_MODP + q * 15360, (u32)col * 4);
      sfor<0, 4>(issA);
      issW(0, 0); issW(0, 1); issW(0, 2);
      mdst = tid < 320 ? L_GS + (k1 >> 3) * 32 + (m1 ? 0 : 16) + (k1 & 4) * 2 : L_MODS + u2 * 16;
      plus1 = tid < 320 ? m1 != 0 : (m2 & 1) != 0;
    }
    if (tid == 0) *(LAS u32*)(size_t)L_FLAG = 0u;
    const int rk16 = w < 5 ? 5 * (mt >> 1) + w : (mt * 2 + th) * 3 + (w - 5);
    const char* rsrc = w < 5 ? (const char*)((mt & 1) ? p.wu : p.wg) + (size_t)rk16 * 49152 + 192 * j : (const char*)p.wd + (size_t)rk16 * 20480 + 160 * n8;
    const u32 rimg = 122880 + w * 3072;
    auto issR = [&]() LAM {
#pragma unroll
      for (int m = 0; m < 3; ++m) {
        const u32 x = 64 * m + lane, i5 = x < 160 ? x : 159, r12 = (x * 171) >> 11, r10 = (i5 * 205) >> 11;
        dma(rsrc, w < 5 ? r12 * 2880 + x * 16 : r10 * 1120 + i5 * 16, rimg + m * 1024);
      }
    };
    f32x16 acc[3];
    const u32 wb = kh * 20480 + frow, gb_ = L_GS + kh * 1280 + h * 32;
    u32x4 gs[3][2], wf[2][3], ro[3];
    auto ldgs = [&](int k) LAM { gs[k % 3][0] = lds128(gb_ + k * 64); gs[k % 3][1] = lds128(gb_ + k * 64 + 16); };
    auto modulate = [&](int k) LAM {
      PIN(a[k]);
#pragma unroll
      for (int e = 0; e < 4; ++e)
        a[k][e] = pkfma16(a[k][e], gs[k % 3][0][e], gs[k % 3][1][e]);
    };
    FST(3, 1);
    WAITV(7);
    FST(3, 2);
    f32x4 mv;
    {
#pragma unroll
      for (int q = 0; q < 2; ++q) PIN(mp[q]);
      float mxv = 0.f;
#pragma unroll
      for (int e = 0; e < 4; ++e) {
        const float s = rbf(u2f(mp[0][e]) + u2f(mp[1][e]));
        mv[e] = plus1 ? rbf(1.f + s) : s;
        mxv = __builtin_fmaxf(mxv, __builtin_fabsf(mv[e]));
      }
      if (tid < 320) st64(mdst, u32x2{pkh2(mv[0], mv[1]), pkh2(mv[2], mv[3])}); else if (tid < 420) stf4(mdst, mv);
      const bool big = __builtin_amdgcn_ballot_w64(tid < 320 && !(mxv < 2000.f)) != 0ull;
      if (lane == 0) *(LAS u32*)(size_t)(L_FLAG + 16 + w * 4) = big;
    }
    WAITV(0);
    BAR();
    FST(3, 3);
    auto grpA = [&](int cond, int s) LAM { gld4_if(cond, a[4 * s], a[4 * s + 1], a[4 * s + 2], a[4 * s + 3], n1 + s * 4096, l16); };
    const int kl = kh ^ 1;
    auto grpW = [&](int s) LAM { issW(s, 0); issW(s, 1); issW(s, 2); };
    grpA(kh, 1); if (kh) { grpW(1); issR(); }
    bool scaled;
    {
      const u32x4 f0 = lds128(L_FLAG + 16);
      const u32 f1 = *(const LAS u32*)(size_t)(L_FLAG + 32);
      ldgs(0); ldgs(1);
      scaled = __builtin_amdgcn_readfirstlane(f0.x | f0.y | f0.z | f0.w | f1) != 0;
      if (scaled) {
        BAR();
        const float sc = 9.765625e-4f;
        if (tid < 320) st64(mdst, u32x2{pkh2(mv[0] * sc, mv[1] * sc), pkh2(mv[2] * sc, mv[3] * sc)});
        BAR();
        ldgs(0); ldgs(1);
      }
    }
    modulate(0);
    SB();
    const Run<30, 8, false> rb3(wq + 122880, 122880, w);
    sfor<0, 21>([&](auto k_) LAM {
      constexpr int k = decltype(k_)::v, s = k >> 2, i = k & 3;
      if constexpr (i == 3 && k < 16) WAITV(k == 3 || k == 15 ? 13 : 10);
      if constexpr (i == 0 && k > 0 && k < 20) {
        FST(3, 3 + 3 * s); WAITV(k == 4 || k == 12 ? 10 : 7); FST(3, 4 + 3 * s); BAR(); FST(3, 5 + 3 * s);
        if (s == 2 && CSEL == 4) { CST(4, 18); STAMP(16); } if (s == 3 && CSEL == 4) { CST(4, 19); STAMP(17); }
      }
      if constexpr (k < 20) {
#pragma unroll
        for (int nb = 0; nb < 3; ++nb) wf[k & 1][nb] = lds128(wb + nb * 40960 + k * 1024);
      }
      if constexpr (k == 1) {
        f32x16 z;
#pragma unroll
        for (int e = 0; e < 16; ++e) z[e] = 0.f;
#pragma unroll
        for (int nb = 0; nb < 3; ++nb) acc[nb] = MFMA32H(wf[0][nb], a[0], z);
      } else if constexpr (k > 1) {
#pragma unroll
        for (int nb = 0; nb < 3; ++nb) acc[nb] = MFMA32H(wf[(k - 1) & 1][nb], a[k - 1], acc[nb]);
      }
      if constexpr (k + 1 < 20) modulate(k + 1);
      if constexpr (k + 2 < 20) ldgs(k + 2);
      if constexpr (k == 8) {
        const u32 q4 = (lane >> 2) & 3, nh = (lane >> 4) & 1;
        const u32 r0 = rimg + (w < 5 ? (8 * h + q4) * 192 + 32 * nh : (4 * nh + q4) * 160 + 32 * h) + (lane & 3) * 8, r1 = r0 + (w < 5 ? 768 : 1280);
#pragma unroll
        for (int u = 0; u < 3; ++u) ro[u] = ldstr2(r0 + 64 * u, r1 + 64 * u);
      }
      if constexpr (k == 9 || k == 10) {
        if (w < 5) {
          const u32 ga_ = L_MODS + 960 + w * 64 + h * 32;
          const f32x4 g0 = ldsf4(ga_), g1 = ldsf4(ga_ + 16), s0 = ldsf4(ga_ + 320), s1 = ldsf4(ga_ + 336);
#pragma unroll
          for (int u = (k == 9 ? 0 : 2); u < (k == 9 ? 2 : 3); ++u) {
            float c = 0.f, bi = 0.f;
#pragma unroll
            for (int e = 0; e < 4; ++e) {
              const u32 v = ro[u][e];
              const float x0 = lo16(v), x1 = hi16(v), ge = e < 2 ? g0[2 * e] : g1[2 * e - 4], go = e < 2 ? g0[2 * e + 1] : g1[2 * e - 3];
              const float se = e < 2 ? s0[2 * e] : s1[2 * e - 4], so_ = e < 2 ? s0[2 * e + 1] : s1[2 * e - 3];
              c = fma1(ge, x0, c); c = fma1(go, x1, c); bi = fma1(se, x0, bi); bi = fma1(so_, x1, bi);
            }
            auto r = __builtin_amdgcn_permlane32_swap(f2u(c), f2u(bi), false, false);
            *(LAS float*)(size_t)(w * 768 + u * 256 + (c32 >> 3) * 64 + h * 32 + (c32 & 7) * 4) = u2f(r[0]) + u2f(r[1]);
          }
        }
      }
      if constexpr (k == 13) {
        if (w == 7) {
          f32x4 csum = {0.f, 0.f, 0.f, 0.f};
#pragma unroll
          for (int u = 0; u < 5; ++u) csum += ldsf4(u * 768 + (lane < 48 ? l16 : 0u));
          if (lane < 48) __builtin_amdgcn_raw_buffer_store_b128(__builtin_bit_cast(u32x4, csum), rs, WS_CSP + (u32)(j * 16 + mt) * 768 + l16, 0, 16);
        }
      }
      if constexpr (k == 16) {
        const u32 x2 = 16 * kh + 4 * h, tb = 12288 + (32 * tt + c32) * 48 + 2 * x2;
#pragma unroll
        for (int u = 0; u < 8; ++u) {
          const u32 part = ((x2 + 32 * (u >> 1) + 8 * (u & 1)) * 205) >> 12;
          imga[u] = part * 18392 + tb + 64 * (u >> 1) + 16 * (u & 1); PIN(imga[u]);
        }
      }
      SB();
      if constexpr (k == 0) { grpA(kl, 1); if (!kh) { grpW(1); issR(); } }
      if constexpr (k == 1) grpA(kl, 2);
      if constexpr (k == 2) { if (!kh) grpW(2); grpA(kh, 2); }
      if constexpr (k == 3) { if (kh) grpW(2); }
      if constexpr (s == 1 || s == 2) {
        if constexpr (i == 0) grpA(kl, s + 2);
        if constexpr (i == 1) { if (!kh) grpW(s + 2); }
        if constexpr (i == 2) grpA(kh, s + 2);
        if constexpr (i == 3) { if (kh) grpW(s + 2); }
      }
      if constexpr (k == 11) {
        u32 off0, du;
        if (w < 5) { const int k2 = rk16 >= 20, i2 = rk16 - 20 * k2; off0 = WS_WGF + j * WGB + (k2 * 20 + i2) * 2048 + (mt & 1) * 1024 + l16; du = 81920; }
        else { const int k2 = rk16 >= 48, kk = rk16 - 48 * k2; off0 = WS_WDF + n8 * WDB + ((kk >> 1) * 10 + k2 * 5 + h) * 1024 + (kk & 1) * 512 + c32 * 16; du = 2048; }
        asm volatile("" ::: "memory");
        __builtin_amdgcn_raw_buffer_store_b128(ro[0], rs, off0, 0, 16);
        __builtin_amdgcn_raw_buffer_store_b128(ro[1], rs, off0 + du, 0, 16);
        __builtin_amdgcn_raw_buffer_store_b128(ro[2], rs, (w < 5 || h == 0) ? off0 + 2 * du : 0xfffffff0u, 0, 16);
        asm volatile("" ::: "memory");
      }
      if constexpr (k == 12) { if (!kh) rb3.all(l16); }
      if constexpr (k == 14) { if (kh) rb3.all(l16); }
      SB();
    });
    STAMP(1); CST(3, 19); FST(6, 1);
    WAITV(0);
    BAR();
    FST(6, 2);
    const u32 exw = (w >> 1) * 18432 + (w & 1) * 6144 + l16, exr = ((w ^ 4) >> 1) * 18432 + (w & 1) * 6144 + l16;
#pragma unroll
    for (int nb = 0; nb < 3; ++nb) {
      stf4(exw + nb * 2048, f32x4{acc[nb][8], acc[nb][9], acc[nb][10], acc[nb][11]});
      stf4(exw + nb * 2048 + 1024, f32x4{acc[nb][12], acc[nb][13], acc[nb][14], acc[nb][15]});
    }
    if (kh) st64((tid >> 7) * 18432 + 12288 + (tid & 127) * 48 + 40, u32x2{0x3f803f80u, 0x3f803f80u});
    BAR();
    FST(6, 3);
    const float osc = scaled ? 1024.f : 1.f, qs = QSCALE * (1.f / 4096.f) * osc, sc2 = kh ? qs : osc;
#pragma unroll
    for (int nb = 0; nb < 3; ++nb)
#pragma unroll
      for (int q = 0; q < 2; ++q) {
        const f32x4 o = ldsf4(exr + nb * 2048 + q * 1024);
        float v0 = acc[nb][4 * q] + o[0], v1 = acc[nb][4 * q + 1] + o[1], v2 = acc[nb][4 * q + 2] + o[2], v3 = acc[nb][4 * q + 3] + o[3];
        if (nb == 2) { v0 *= sc2; v1 *= sc2; v2 *= sc2; v3 *= sc2; }
        else if (scaled) { v0 *= osc; v1 *= osc; v2 *= osc; v3 *= osc; }
        st64(imga[2 * nb + q], u32x2{pk2(v0, v1), pk2(v2, v3)});
      }
    BAR();
    FST(6, 4);
    {
      const u32 dst = WS_KVX + (u32)(mt * 16 + j) * KVB + w * 3072 + l16, src = (w >> 1) * 18432 + 12288 + (w & 1) * 3072 + l16;
#pragma unroll
      for (int i = 0; i < 3; ++i) __builtin_amdgcn_raw_buffer_store_b128(lds128(src + i * 1024), rs, dst + i * 1024, 0, 16);
    }
    f32x16 acc3;
#pragma unroll
    for (int e = 0; e < 16; ++e) acc3[e] = 0.f;
    {
      const int r3 = rsw < 24 ? rsw : (rsw < 28 ? rsw - 4 : rsw - 12);
      const u32 wb3 = 122880 + kh * 15360 + (h * 24 + r3) * 16;
      u32x4 w3[4];
#pragma unroll
      for (int k = 0; k < 3; ++k) w3[k] = lds128(wb3 + k * 768);
#pragma unroll
      for (int k = 0; k < 20; ++k) {
        if (k + 3 < 20) w3[(k + 3) & 3] = lds128(wb3 + (k + 3) * 768);
        acc3 = MFMA32H(w3[k & 3], a[k], acc3);
        if (k == 9) {
          FST(6, 5);
          WAITVM(0);
          FST(6, 6);
          if (lane == 0) {
            const u32 old = __hip_atomic_fetch_add((LAS u32*)(size_t)L_FLAG, 1u, __ATOMIC_RELAXED, __HIP_MEMORY_SCOPE_WORKGROUP);
            if (old == 7u) {
              __hip_atomic_store((gu32*)(ws + WS_KVF + (mt * 16 + j) * 128), 1u, RLX_AGENT);
              signal(ws, C_RG + j * 256); signal(ws, C_RD + n8 * 256);
            }
          }
        }
        SB();
      }
    }
    const u32 ex3w = (w < 4 ? 73728 : 84992) + w * 2048 + l16, ex3r = (w < 4 ? 93184 : 65536) + w * 2048 + l16;
    stf4(ex3w, f32x4{acc3[8], acc3[9], acc3[10], acc3[11]});
    stf4(ex3w + 1024, f32x4{acc3[12], acc3[13], acc3[14], acc3[15]});
    FST(6, 7);
    BAR();
    FST(6, 8);
    STAMP(2);
#pragma unroll
    for (int q = 0; q < 2; ++q) {
      const f32x4 o = ldsf4(ex3r + q * 1024);
      st64(imga[6 + q], u32x2{pk2((acc3[4 * q] + o[0]) * qs, (acc3[4 * q + 1] + o[1]) * qs), pk2((acc3[4 * q + 2] + o[2]) * qs, (acc3[4 * q + 3] + o[3]) * qs)});
    }
    BAR();
    FST(6, 9);
  }
  u32x2 xr[3];
  const int c16 = lane & 15, g4 = lane >> 4;
  {
    PHASE();
    const int hh = w >> 2, qb = w & 3;
    u32x4 q0f, q1f;
    {
      const u32 qa = (4 + hh) * 18432 + 12288 + (32 * qb + c32) * 48;
      const u32x2 x2 = lds64(qa + 32);
      q0f = lds128(qa + 16 * h);
      q1f = u32x4{h ? 0u : x2.x, h ? 0u : x2.y, 0u, 0u};
    }
    if (w == 0) {
      const int l3 = lane & 3, back = l3 + 1 <= mt ? l3 + 1 : 0;
      const u32 fo = l3 == 3 ? C_RD + n8 * 256 : (l3 == 2 || back == 0) ? C_RG + j * 256 : WS_KVF + ((mt - back) * 16 + j) * 128;
      const u32 want = l3 == 3 ? 32u : l3 == 2 ? 16u : 1u;
      for (u32 spins = 0; spins < SPIN_CAP; ++spins) {
        u32 v;
        gld32_sc1(v, ws, fo); WAITV(0); PIN(v);
        if (__builtin_amdgcn_ballot_w64(v < want) == 0ull) break;
      }
    }
    BAR();
    STAMP(3); FST(6, 10);
    {
      const u32 l0 = (w >> 1) * 18432 + (w & 1) * 3072;
#pragma unroll
      for (int sl = 1; sl >= 0; --sl) {
        if (mt + sl >= 2) {
          const char* src = ws + WS_KVX + (size_t)((mt - 2 + sl) * 16 + j) * KVB + w * 3072;
          dmai<0, true>(src, l16, l0 + sl * 6144); dmai<1024, true>(src, l16, l0 + sl * 6144); dmai<2048, true>(src, l16, l0 + sl * 6144);
        } else {
          const u32x4 z = {0u, 0u, 0u, 0u};
          st128(l0 + sl * 6144 + l16, z); st128(l0 + sl * 6144 + 1024 + l16, z); st128(l0 + sl * 6144 + 2048 + l16, z);
        }
      }
    }
    FST(6, 11);
    {
      const int tok64 = 128 * mt + 64 * th + 16 * tt + c16;
#pragma unroll
      for (int m = 0; m < 3; ++m) gld64(xr[m], (const char*)p.x, (u32)(tok64 * 1280 + (80 * n8 + 16 * ((m + 3 * kh) % 5) + 4 * g4) * 2));
    }
    FST(6, 12);
    WAITV(3);
    FST(6, 13);
    BAR();
    STAMP(4); CST(1, 18); FST(4, 12); FST(6, 14);
    const char* sg0 = ws + WS_WGF + (size_t)j * WGB + (kh * 40 + tt * 10) * 1024;
    const int tmin = mt >= 2 ? 0 : 8 - 4 * mt - qb;
    const u32 kb = hh * 18432 + (32 * qb + c32) * 48, kb2 = kb + 16 * h;
    const u32 vb = (2 + hh) * 18432 + (32 * qb + 4 * h + ((lane >> 2) & 3)) * 48 + ((lane >> 4) & 1) * 32 + (lane & 3) * 8;
    const int qlim = c32 - 4 * h;
    const Run<70, 4, false> rwo(ws + WS_WOF + (size_t)n8 * WOB, wo_slot(0), w & 3);
    const u32 fa = WS_ACT + (u32)((mt * 4 + (w & 3)) * 96 + 6 * j) * 1024 + l16;
    const u32 fx = w < 2 ? WS_X1 + (u32)((mt * 4 + 2 * th + w) * 40 + 5 * n8) * 1024 + l16 : WS_STAT + (u32)((mt * 4 + 2 * th + h) * 8 + n8) * 512 + c32 * 16;
    f32x16 oacc;
    auto attn = [&](auto full_) LAM {
      constexpr bool FULL = decltype(full_)::v;
      u32x4 ka[2], kq[2], vf[2][2];
      kq[0] = u32x4{0u, 0u, 0u, 0u}; kq[1] = kq[0];
      auto ldk = [&](int t, int sl) LAM {
        const u32x2 x2 = lds64(kb + t * 1536 + 32);
        ka[sl] = lds128(kb2 + t * 1536); kq[sl].x = x2.x; kq[sl].y = x2.y;
      };
      auto ldv = [&](int t, int sl) LAM {
#pragma unroll
        for (int s2 = 0; s2 < 2; ++s2) { const u32 va = vb + (32 * t + 16 * s2) * 48; vf[sl][s2] = ldstr2(va, va + 384); }
      };
      auto splat = [&](float v) LAM -> f32x16 {
        f32x16 c;
#pragma unroll
        for (int e = 0; e < 16; ++e) c[e] = v;
        return c;
      };
      auto pre = [&](auto m_) LAM {
        constexpr int m = decltype(m_)::v;
        gld_sc1<(m & 3) * 1024>(st[m], sg0 + (m >> 2) * 4096, l16);
        if (w < 4) {
          rwo.go<2 * m>(l16); rwo.go<2 * m + 1>(l16);
          const u32x4 f = {~0u, ~0u, ~0u, ~0u};
          if constexpr (m < 6) __builtin_amdgcn_raw_buffer_store_b128(f, rs, fa + m * 1024, 0, 16);
          if constexpr (m >= 5) { if (w < 2 || (w == 2 && m == 5)) __builtin_amdgcn_raw_buffer_store_b128(f, rs, fx + (m - 5) * 1024, 0, 16); }
        }
        SB();
      };
      f32x16 sb[2], cm;
      float pe[16];
      FST(4, 1);
      ldk(8, 0); ldk(0, 1); ldv(0, 0); ldv(8, 1);
      sb[0] = MFMA32(ka[0], q0f, splat(0.f));
      sb[1] = MFMA32(ka[1], q0f, splat(FULL ? 0.f : (tmin == 0 ? 0.f : -1.0e30f)));
      sb[0] = MFMA32(kq[0], q1f, sb[0]);
      sb[1] = MFMA32(kq[1], q1f, sb[1]);
      ldk(1, 0); ldk(2, 1);
      SB();
      pre(IC<0>{});
#pragma unroll
      for (int e = 0; e < 16; ++e) pe[e] = ((e & 3) + 8 * (e >> 2) > qlim) ? sb[1][e] : sb[0][e];
      float mx = __builtin_fmaxf(pe[0], pe[1]);
#pragma unroll
      for (int e = 2; e < 16; e += 2) mx = max3(mx, pe[e], pe[e + 1]);
      mx = -max32(mx);
      cm = splat(mx);
      auto cin = [&](int t) LAM -> f32x16 { if constexpr (FULL) return cm; else return splat(t >= tmin ? mx : -1.0e30f); };
      sb[0] = MFMA32(ka[0], q0f, cin(1));
      FST(4, 2);
      SB();
#pragma unroll
      for (int e = 0; e < 8; ++e) pe[e] = __builtin_amdgcn_exp2f(pe[e] + mx);
      SB();
      sb[0] = MFMA32(kq[0], q1f, sb[0]);
      SB();
#pragma unroll
      for (int e = 8; e < 16; ++e) pe[e] = __builtin_amdgcn_exp2f(pe[e] + mx);
      SB();
      sfor<0, 4>([&](auto u_) LAM {
        constexpr int u = decltype(u_)::v, s2 = u & 1, t8 = u >> 1;
        u32x4 pf;
#pragma unroll
        for (int e = 0; e < 4; ++e) {
          const int e0 = 8 * s2 + 2 * e, e1 = e0 + 1;
          const bool m0 = ((e0 & 3) + 8 * (e0 >> 2) > qlim) != (t8 != 0), m1 = ((e1 & 3) + 8 * (e1 >> 2) > qlim) != (t8 != 0);
          pf[e] = pk2(m0 ? pe[e0] : 0.f, m1 ? pe[e1] : 0.f);
        }
        if constexpr (u == 0) oacc = MFMA32(vf[0][0], pf, splat(0.f)); else oacc = MFMA32(vf[t8][s2], pf, oacc);
        if constexpr (u == 1) ldv(1, 0);
        SB();
        if constexpr (u == 1) pre(IC<1>{});
        if constexpr (u == 3) pre(IC<2>{});
      });
      sfor<1, 8>([&](auto t_) LAM {
        constexpr int t = decltype(t_)::v, c = (t + 1) & 1, n = t & 1;
        u32x4 pf0, pf1;
        FST(4, 2 + t);
        if constexpr (t < 7) { sb[n] = MFMA32(ka[n], q0f, cin(t + 1)); SB(); ldv(t + 1, n); }
#pragma unroll
        for (int e = 0; e < 8; ++e) pe[e] = __builtin_amdgcn_exp2f(sb[c][e]);
        SB();
        if constexpr (t < 7) { sb[n] = MFMA32(kq[n], q1f, sb[n]); SB(); }
#pragma unroll
        for (int e = 8; e < 12; ++e) pe[e] = __builtin_amdgcn_exp2f(sb[c][e]);
#pragma unroll
        for (int e = 0; e < 4; ++e) pf0[e] = pk2(pe[2 * e], pe[2 * e + 1]);
        SB();
        oacc = MFMA32(vf[c][0], pf0, oacc);
        SB();
        if constexpr (t + 2 < 8) ldk(t + 2, c);
#pragma unroll
        for (int e = 12; e < 16; ++e) pe[e] = __builtin_amdgcn_exp2f(sb[c][e]);
#pragma unroll
        for (int e = 0; e < 4; ++e) pf1[e] = pk2(pe[8 + 2 * e], pe[8 + 2 * e + 1]);
        SB();
        oacc = MFMA32(vf[c][1], pf1, oacc);
        SB();
        pre(IC<2 + t>{});
      });
    };
    auto exact = [&]() LAM {
      auto sc = [&](int t, float c0, f32x16& s) LAM {
        const u32x2 x2 = lds64(kb + t * 1536 + 32);
#pragma unroll
        for (int e = 0; e < 16; ++e) s[e] = c0;
        s = MFMA32(lds128(kb2 + t * 1536), q0f, s);
        s = MFMA32((u32x4{x2.x, x2.y, 0u, 0u}), q1f, s);
#pragma unroll
        for (int e = 0; e < 16; ++e) s[e] = (u32)(32 * t + (e & 3) + 8 * (e >> 2) - qlim - 1) < 256u ? s[e] : -1.0e30f;
      };
      float mx = -1.0e30f;
      f32x16 s;
#pragma nounroll
      for (int t = tmin; t < 9; ++t) {
        sc(t, 0.f, s);
#pragma unroll
        for (int e = 0; e < 16; e += 2) mx = max3(mx, s[e], s[e + 1]);
      }
      mx = -max32(mx);
#pragma unroll
      for (int e = 0; e < 16; ++e) oacc[e] = 0.f;
#pragma nounroll
      for (int t = tmin; t < 9; ++t) {
        sc(t, mx, s);
#pragma unroll
        for (int s2 = 0; s2 < 2; ++s2) {
          u32x4 pf;
#pragma unroll
          for (int e = 0; e < 4; ++e) pf[e] = pk2(__builtin_amdgcn_exp2f(s[8 * s2 + 2 * e]), __builtin_amdgcn_exp2f(s[8 * s2 + 2 * e + 1]));
          const u32 va = vb + (32 * t + 16 * s2) * 48;
          oacc = MFMA32(ldstr2(va, va + 384), pf, oacc);
        }
      }
    };
    const int ko = 40 * j + 20 * hh + 4 * h;
    u32 ob = WS_OAF + (u32)((mt * 4 + qb) * 40) * 1024 + c32 * 16 + (ko >> 3) * 512 + ((ko >> 2) & 1) * 8;
    PIN(ob);
    if (mt >= 2) attn(BoolC<true>{}); else attn(BoolC<false>{});
    FST(4, 10);
    if (__builtin_amdgcn_ballot_w64(h != 0 && !(oacc[8] < ATT_LIMIT)) != 0ull) exact();
    const float lx = xor32f(oacc[8]);
    const float inv = __builtin_amdgcn_rcpf(64.f * (h ? oacc[8] : lx));
#pragma unroll
    for (int g = 0; g < 3; ++g) {
      const u32x2 o = {pk2(oacc[4 * g] * inv, oacc[4 * g + 1] * inv), pk2(oacc[4 * g + 2] * inv, oacc[4 * g + 3] * inv)};
      if (g < 2 || h == 0) __builtin_amdgcn_raw_buffer_store_b64(o, rs, ob + g * 512, 0, 16);
    }
    STAMP(5); CST(1, 19); FST(4, 11);
    WAITVM(0);
    BAR();
    if (tid == 0) signal(ws, C_O + mt * 256);
    STAMP(6); FST(4, 13);
  }
  u32 wbase[5], avoff;
  u32x2 x1k[3];
  f32x4 c5[5];
  {
    PHASE();
    const char* sg1 = ws + WS_WGF + (size_t)j * WGB + 81920 + (kh * 40 + tt * 8) * 1024;
    auto stage1 = [&]() LAM { sfor<0, 8>([&](auto m_) LAM { constexpr int m = decltype(m_)::v; gld_sc1<(m & 3) * 1024>(st[10 + m], sg1 + (m >> 2) * 4096, l16); }); };
    if (w >= 4) { __builtin_amdgcn_s_sleep(2); Run<30, 4, false>(ws + WS_WOF + (size_t)n8 * WOB + 71680, wo_slot(7), w - 4).all(l16); }
    stage1();
#pragma unroll
    for (int m = 0; m < 5; ++m) wbase[m] = (kh * 5 + (m + 3 * kh) % 5) * 1024 + l16;
    avoff = (g4 >> 1) * 1024 + (g4 & 1) * 512 + (16 * (tt & 1) + c16) * 16;
    const int tt64 = mt * 4 + 2 * th + (tt >> 1);
    const char* ab = ws + WS_OAF + (size_t)(tt64 * 40 + 20 * kh) * 1024;
    const u32 ew = 73728 + (tt * 5) * 1024 + l16;
    u32 xb = WS_X1 + (u32)(tt64 * 40 + 5 * n8) * 1024 + (g4 >> 1) * 512 + (16 * (tt & 1) + c16) * 16 + (g4 & 1) * 8 + kh * 3072;
    u32 sta = WS_STAT + (u32)((tt64 * 8 + n8) * 32 + 16 * (tt & 1) + c16) * 16 + kh * 8;
    u32 mo = L_MODS + kh * 192 + g4 * 16;
    PIN(xb); PIN(sta); PIN(mo); PIN(avoff);
    auto iss = [&](auto s_) LAM { constexpr int s = decltype(s_)::v; gld_sc1<(s & 1) * 2048>(a[s], ab + (s >> 1) * 4096, avoff); };
    u32x4 wf[3][5];
#pragma unroll
    for (int s = 0; s < 2; ++s)
#pragma unroll
      for (int m = 0; m < 5; ++m) wf[s][m] = lds128(wbase[m] + wo_slot(s));
#pragma unroll
    for (int m = 0; m < 5; ++m) c5[m] = f32x4{0.f, 0.f, 0.f, 0.f};
    if (w == 0) poll_ge(ws, C_O + mt * 256, 16u);
    BAR();
    STAMP(7); FST(5, 1);
    sfor<0, 5>(iss);
    sfor<0, 10>([&](auto s_) LAM {
      constexpr int s = decltype(s_)::v;
      WAITV(s == 0 ? 4 : s == 1 ? 6 : 9 - s);
      FST(5, 2 + s);
      if (s == 4) BAR();
      PIN(a[s]);
      if constexpr (s + 2 < 10) {
#pragma unroll
        for (int m = 0; m < 5; ++m) wf[(s + 2) % 3][m] = lds128(wbase[m] + wo_slot(s + 2));
      }
#pragma unroll
      for (int m = 0; m < 5; ++m) c5[m] = MFMA16(wf[s % 3][m], a[s], c5[m]);
      SB();
      if constexpr (s == 0) sfor<5, 8>(iss);
      if constexpr (s == 1) sfor<8, 10>(iss);
      SB();
    });
    STAMP(8); FST(5, 12);
    stf4(ew + ((3 + 3 * kh) % 5) * 1024, c5[3]);
    stf4(ew + ((4 + 3 * kh) % 5) * 1024, c5[4]);
    if (kh) stf4(ew, c5[2]);
    BAR();
    FST(5, 13);
    float xs[12];
#pragma unroll
    for (int m = 0; m < 3; ++m) PIN(xr[m]);
#pragma unroll
    for (int m = 0; m < 3; ++m) {
      if (m < 2 || kh == 0) {
        const f32x4 o = ldsf4(ew + kh * 3072 + m * 1024), ga = ldsf4(mo + m * 64), g2 = ldsf4(mo + 320 + m * 64);
        const u32 y01 = pk2(c5[m][0] + o[0], c5[m][1] + o[1]), y23 = pk2(c5[m][2] + o[2], c5[m][3] + o[3]);
        const u32 t01 = pk2(ga[0] * lo16(y01), ga[1] * hi16(y01)), t23 = pk2(ga[2] * lo16(y23), ga[3] * hi16(y23));
        x1k[m] = u32x2{pk2(lo16(xr[m].x) + lo16(t01), hi16(xr[m].x) + hi16(t01)), pk2(lo16(xr[m].y) + lo16(t23), hi16(xr[m].y) + hi16(t23))};
        xs[4 * m] = lo16(x1k[m].x); xs[4 * m + 1] = hi16(x1k[m].x); xs[4 * m + 2] = lo16(x1k[m].y); xs[4 * m + 3] = hi16(x1k[m].y);
        __builtin_amdgcn_raw_buffer_store_b64(u32x2{pk2(xs[4 * m] * g2[0], xs[4 * m + 1] * g2[1]), pk2(xs[4 * m + 2] * g2[2], xs[4 * m + 3] * g2[3])}, rs, xb + m * 1024, 0, 16);
      } else {
        x1k[m] = u32x2{0u, 0u};
        xs[8] = xs[9] = xs[10] = xs[11] = 0.f;
      }
    }
    float s = ((xs[0] + xs[1]) + (xs[2] + xs[3])) + ((xs[4] + xs[5]) + (xs[6] + xs[7])) + ((xs[8] + xs[9]) + (xs[10] + xs[11]));
    s = sum32(sum16(s));
    const float mean = s * (kh ? 1.f / 32.f : 1.f / 48.f);
    float q0 = 0.f, q1 = 0.f;
#pragma unroll
    for (int e = 0; e < 4; ++e) { const float d0 = xs[e] - mean, d1 = xs[4 + e] - mean; q0 = fma1(d0, d0, q0); q1 = fma1(d1, d1, q1); }
    if (kh == 0) {
#pragma unroll
      for (int e = 8; e < 12; ++e) { const float d = xs[e] - mean; q0 = fma1(d, d, q0); }
    }
    const float q = sum32(sum16(q0 + q1));
    if (g4 == 0) __builtin_amdgcn_raw_buffer_store_b64(u32x2{f2u(mean), f2u(q)}, rs, sta, 0, 16);
    FST(5, 14);
    BAR();
    STAMP(9); FST(5, 16);
  }
  {
    PHASE();
    const bool old4 = w < 4;
    const char* wgb = ws + WS_WGF + (size_t)j * WGB;
    const char* wdb = ws + WS_WDF + (size_t)n8 * WDB;
    u32x4 stv[4], csv[2];
    const char* ab = ws + WS_X1 + (size_t)((mt * 4 + tt) * 40 + 20 * kh) * 1024;
    const char* sb = ws + WS_STAT + (size_t)(mt * 4 + tt) * 4096;
    auto iss = [&](auto s_) LAM { constexpr int s = decltype(s_)::v; gld_sc1<(s & 3) * 1024>(a[s], ab + (s >> 2) * 4096, l16); };
    {
      const char* cb = ws + WS_CSP + (size_t)(j * 16 + 4 * (w >> 1) + (w & 1)) * 768;
      const u32 co = lane < 48 ? l16 : 0u;
      gld_sc1<0>(csv[0], cb, co); gld_sc1<1536>(csv[1], cb, co);
    }
#pragma unroll
    for (int m = 0; m < 18; ++m) { PIN(st[m]); st128((m < 10 ? (kh * 40 + tt * 10 + m) : (80 + kh * 32 + tt * 8 + m - 10)) * 1024 + l16, st[m]); }
    float c1 = 0.f, d2 = 0.f;
    u32x4 bcor = {0u, 0u, 0u, 0u};
    u32 sw = kh * 40960 + tt * 4096 + l16, sr = (kh ^ 1) * 40960 + tt * 4096 + l16;
    u32 actb = WS_ACT + (u32)((mt * 4 + tt) * 96 + 6 * j + kh) * 1024 + l16;
    const u32 so = h * 2048 + c32 * 16;
    PIN(actb);
    const auto rC = gu_run<1, 16, 20, 4>(wgb, w & 3);
    const auto rA = gu_run<2, 0, 8, 4>(wgb, w & 3);
    const auto rB = gu_run<2, 8, 20, 4>(wgb, w & 3);
    const Run<60, 8, true> rD(wdb, 0, w);
    const Run<90, 8, true> rS(wdb + 61440, 61440, w);
    gate(sb, (c32 >> 2) * 512 + ((c32 >> 1) & 1) * 256 + (c32 & 1) * 8);
    STAMP(10); FST(2, 1); FST(5, 17);
    sfor<0, 2>(iss);
    gld_sc1<0>(stv[0], sb, so); gld_sc1<512>(stv[1], sb, so); gld_sc1<1024>(stv[2], sb, so); gld_sc1<1536>(stv[3], sb, so);
    sfor<2, 6>(iss);
    f32x16 ag[2], au[2];
    u32x4 fg[3], fu[3], outv;
    u32 ub = 0;
    auto ldw = [&](auto g_) LAM {
      constexpr int g = decltype(g_)::v, pp = g / 20, i = g % 20;
      constexpr bool first = i == 0 || gu_unit(pp, 0, i) != gu_unit(pp, 0, i - 1) + 1;
      constexpr int i0 = [] { int x = i; while (x > 0 && gu_unit(pp, 0, x) == gu_unit(pp, 0, x - 1) + 1) --x; return x; }();
      if constexpr (first) ub = frow + (kh ? gu_unit(pp, 1, i) : gu_unit(pp, 0, i)) * 2048;
      fg[g % 3] = lds128(ub + (i - i0) * 2048); fu[g % 3] = lds128(ub + (i - i0) * 2048 + 1024);
    };
    auto xwrite = [&](int pp) LAM {
      const f32x16 &G = ag[pp & 1], &U = au[pp & 1];
      stf4(sw, f32x4{G[8], G[9], G[10], G[11]}); stf4(sw + 1024, f32x4{G[12], G[13], G[14], G[15]});
      stf4(sw + 2048, f32x4{U[8], U[9], U[10], U[11]}); stf4(sw + 3072, f32x4{U[12], U[13], U[14], U[15]});
    };
    f32x4 xo[4];
    auto xread = [&]() LAM {
#pragma unroll
      for (int u = 0; u < 4; ++u) xo[u] = ldsf4(sr + u * 1024);
    };
    auto xadd = [&](int pp, int q) LAM {
#pragma unroll
      for (int i = 0; i < 4; ++i) { ag[pp & 1][4 * q + i] += xo[q][i]; au[pp & 1][4 * q + i] += xo[2 + q][i]; }
    };
    auto corr = [&](int pp) LAM {
      ag[pp & 1] = MFMA32(lds128(L_T2 + pp * 1024 + rsw * 16), bcor, ag[pp & 1]);
      au[pp & 1] = MFMA32(lds128(L_T2 + pp * 1024 + 512 + rsw * 16), bcor, au[pp & 1]);
    };
    float eu[2], ee[2];
    auto epi = [&](int pp, int o, int ph) LAM {
#pragma unroll
      for (int i = 0; i < 2; ++i) {
        if (ph == 0) { ee[i] = __builtin_amdgcn_exp2f(ag[pp & 1][2 * o + i] * c1); eu[i] = ag[pp & 1][2 * o + i] * au[pp & 1][2 * o + i]; }
        if (ph == 1) ee[i] = __builtin_amdgcn_rcpf(fma1(ee[i], d2, d2));
      }
      if (ph == 2) outv[o] = pk2(eu[0] * ee[0], eu[1] * ee[1]);
    };
    auto ostore = [&](int pp) LAM { __builtin_amdgcn_raw_buffer_store_b128(outv, rs, actb + pp * 2048, 0, 16); asm volatile("" ::: "memory"); };
    BAR();
    ldw(IC<0>{}); ldw(IC<1>{});
    sfor<0, 60>([&](auto g_) LAM {
      constexpr int g = decltype(g_)::v, pp = g / 20, i = g % 20;
      if constexpr (g < 20) {
        WAITV(g == 0 ? 9 : g == 1 ? 12 : g == 2 ? 11 : g == 3 ? 14 : 19 - g);
        PIN(a[g]);
        ensure<(g & 3) * 1024>(a[g], ab + (g >> 2) * 4096, l16);
      }
      if (g == 32) WAITV(0);
      if (g == 43) WAITV(4);
      if ((g & 3) == 0) FST(2, 2 + g / 4);
      if (g == 12 || g == 22 || g == 32 || g == 43 || g == 50) BAR();
      if (g == 22 && CSEL != 4) { STAMP(16); CST(0, 18); }
      if (g == 43 && CSEL != 4) { STAMP(17); CST(0, 19); }
      if constexpr (g + 2 < 60) ldw(IC<g + 2>{});
      if (g == 22 || g == 43) xread();
      if (i == 0) {
        f32x16 z;
#pragma unroll
        for (int e = 0; e < 16; ++e) z[e] = 0.f;
        ag[pp & 1] = MFMA32(fg[g % 3], a[i], z); au[pp & 1] = MFMA32(fu[g % 3], a[i], z);
      } else {
        ag[pp & 1] = MFMA32(fg[g % 3], a[i], ag[pp & 1]); au[pp & 1] = MFMA32(fu[g % 3], a[i], au[pp & 1]);
      }
      if (g == 4) {
#pragma unroll
        for (int u = 0; u < 4; ++u) PIN(stv[u]);
        ensure<0>(stv[0], sb, so); ensure<512>(stv[1], sb, so); ensure<1024>(stv[2], sb, so); ensure<1536>(stv[3], sb, so);
        PIN(csv[0]); PIN(csv[1]);
        f32x4 sm;
#pragma unroll
        for (int e = 0; e < 4; ++e) sm[e] = u2f(csv[0][e]) + u2f(csv[1][e]);
        if (lane < 48) stf4(L_GS + (w >> 1) * 1536 + (w & 1) * 768 + l16, sm);
        float s = 0.f;
#pragma unroll
        for (int u = 0; u < 4; ++u) s += 48.f * u2f(stv[u][0]) + 32.f * u2f(stv[u][2]);
        const float mean = sum32(s) * (1.f / 640.f);
        float q = 0.f;
#pragma unroll
        for (int u = 0; u < 4; ++u) {
          const float d0 = u2f(stv[u][0]) - mean, d1 = u2f(stv[u][2]) - mean;
          q += u2f(stv[u][1]) + u2f(stv[u][3]) + 48.f * d0 * d0 + 32.f * d1 * d1;
        }
        q = sum32(q);
        const float den = rbf(__builtin_amdgcn_sqrtf(rbf(rbf(q * (1.f / 640.f)) + 1e-5f)));
        c1 = -1.4426950408889634f * __builtin_amdgcn_rcpf(den); d2 = den * den;
        const u32 nm = pk2(-mean, -mean), dd = pk2(den, den);
        bcor = u32x4{h ? 0u : nm, h ? 0u : ((nm & 0xffffu) | (dd & 0xffff0000u)), h ? 0u : dd, 0u};
      }
      if (g == 12 && tid < 192) {
        const u32 t4 = L_GS + (tid >> 3) * 64 + (tid & 7) * 4;
        const float cs = (ldsf(t4) + ldsf(t4 + 1536)) + (ldsf(t4 + 3072) + ldsf(t4 + 4608));
        const float bi = (ldsf(t4 + 32) + ldsf(t4 + 1568)) + (ldsf(t4 + 3104) + ldsf(t4 + 4640));
        const u32 h0 = pk2(cs, bi);
        const float cr = cs - lo16(h0), br = bi - hi16(h0);
        const u32 h1 = pk2(cr, br), h2 = pk2(cr - lo16(h1), br - hi16(h1));
        const int gu = tid >= 96, nn = tid - 96 * gu;
        st128(L_T2 + (nn >> 5) * 1024 + gu * 512 + (nn & 31) * 16,
              u32x4{__builtin_amdgcn_perm(h1, h0, 0x05040100u), __builtin_amdgcn_perm(h0, h2, 0x07060100u), __builtin_amdgcn_perm(h2, h1, 0x07060302u), 0u});
      }
      if (g == 21 || g == 41) xwrite(pp - 1);
      if (g == 22 || g == 42) corr(pp - 1);
      if (g == 24 || g == 25) xadd(0, g - 24);
      if (g >= 28 && g <= 37 && (g - 28) % 3 == 0) epi(0, (g - 28) / 3, 2);
      if (g >= 26 && g < 37 && (g - 26) % 3 < 2) epi(0, (g - 26) / 3, (g - 26) % 3);
      if (g == 44 || g == 45) xadd(1, g - 44);
      if (g >= 48 && g <= 57 && (g - 48) % 3 == 0) epi(1, (g - 48) / 3, 2);
      if (g >= 46 && g < 57 && (g - 46) % 3 < 2) epi(1, (g - 46) / 3, (g - 46) % 3);
      SGB(0x100, 6, 0); SGB(0x008, 1, 0); SGB(0x002, 4, 0); SGB(0x008, 1, 0);
      SB();
      if constexpr (g < 3) sfor<6 + 4 * g, 10 + 4 * g>(iss);
      if constexpr (g == 3) sfor<18, 20>(iss);
      if (g == 40) ostore(0);
      if constexpr (g >= 40 && g < 52) gld_sc1<((g - 40) & 3) * 1024>(a[g - 40], rS.src + ((g - 40) >> 2) * 4096, l16);
      if constexpr (g >= 50 && g < 58) rD.template go<g - 50>(l16);
      if (g == 57) ostore(1);
      if constexpr ((g >= 16 && g < 18) || (g >= 22 && g < 26) || (g >= 32 && g < 38)) {
        if (old4) {
          if constexpr (g < 18) { rC.template go<2 * (g - 16)>(l16); rC.template go<2 * (g - 16) + 1>(l16); }
          else if constexpr (g < 26) { rA.template go<2 * (g - 22)>(l16); rA.template go<2 * (g - 22) + 1>(l16); }
          else { rB.template go<2 * (g - 32)>(l16); rB.template go<2 * (g - 32) + 1>(l16); }
        }
      }
      SB();
    });
    { const u32 lo_ = 65536 + tt * 4096, hi_ = (tt < 2 ? 106496 : 131072) + tt * 4096; sw = (kh ? hi_ : lo_) + l16; sr = (kh ? lo_ : hi_) + l16; }
    xwrite(2);
    corr(2);
    BAR();
    STAMP(11); FST(2, 17);
    xread(); xadd(2, 0); xadd(2, 1);
#pragma unroll
    for (int o = 0; o < 4; ++o)
#pragma unroll
      for (int ph = 0; ph < 3; ++ph) epi(2, o, ph);
    ostore(2);
    WAITV(1);
    BAR();
#pragma unroll
    for (int m = 0; m < 12; ++m) { PIN(a[m]); st128(rS.lds + m * 1024 + l16, a[m]); }
    STAMP(12);
  }
  {
    PHASE();
    const bool old4 = w < 4;
    const char* wdb = ws + WS_WDF + (size_t)n8 * WDB;
    const int tt64 = mt * 4 + 2 * th + (tt >> 1);
    const char* ab = ws + WS_ACT + (size_t)(tt64 * 96 + 48 * kh) * 1024;
    auto iss = [&](auto s_) LAM { constexpr int s = decltype(s_)::v; gld_sc1<(s & 1) * 2048>(a[s], ab + (s >> 1) * 4096, avoff); };
#pragma unroll
    for (int m = 0; m < 5; ++m) c5[m] = f32x4{0.f, 0.f, 0.f, 0.f};
    const Run<50, 4, true> rE(wdb + 153600, 0, w & 3);
    const Run<40, 4, true> rF(wdb + 204800, 51200, w & 3);
    const u32 ew = 92160 + (tt * 5) * 1024 + l16;
    u32 oo = (u32)(128 * mt + 64 * th + 16 * tt + c16) * 1280 + (80 * n8 + 48 * kh + 4 * g4) * 2;
    u32 mo = L_MODS + 640 + kh * 192 + g4 * 16;
    PIN(oo); PIN(mo);
    gate(ab, ((c16 >> 1) * 6 + 4 + (c16 & 1)) * 1024 + (tt & 1) * 256);
    STAMP(13); CST(2, 18); FST(1, 1);
    sfor<0, 6>(iss);
    BAR();
    u32x4 wf[3][5];
#pragma unroll
    for (int u = 0; u < 2; ++u)
#pragma unroll
      for (int m = 0; m < 5; ++m) wf[u][m] = lds128(wbase[m] + wd_slot(u));
    sfor<0, 24>([&](auto s_) LAM {
      constexpr int s = decltype(s_)::v;
      WAITV(s == 13 ? 8 : s == 18 ? 0 : s < 6 ? 5 + 2 * s : s > 18 ? 0 : 23 - s);
      if ((s & 1) == 0) FST(1, 2 + s / 2);
      if (s == 5 || s == 9 || s == 13 || s == 18) BAR();
      PIN(a[s]);
      ensure<(s & 1) * 2048>(a[s], ab + (s >> 1) * 4096, avoff);
      if constexpr (s + 2 < 24) {
#pragma unroll
        for (int m = 0; m < 5; ++m) wf[(s + 2) % 3][m] = lds128(wbase[m] + wd_slot(s + 2));
      }
#pragma unroll
      for (int m = 0; m < 5; ++m) c5[m] = MFMA16(wf[s % 3][m], a[s], c5[m]);
      SB();
      if constexpr (s < 6) sfor<6 + 3 * s, 9 + 3 * s>(iss);
      if constexpr (s >= 5 && s < 14) {
        if (old4) {
          if constexpr (s < 9) { sfor<0, 4>([&](auto u_) LAM { rE.template go<4 * (s - 5) + decltype(u_)::v>(l16); }); }
          else { rF.template go<2 * (s - 9)>(l16); rF.template go<2 * (s - 9) + 1>(l16); }
        }
      }
      SB();
    });
    STAMP(14); CST(2, 19); FST(1, 14);
    stf4(ew + ((3 + 3 * kh) % 5) * 1024, c5[3]);
    stf4(ew + ((4 + 3 * kh) % 5) * 1024, c5[4]);
    if (kh) stf4(ew, c5[2]);
    BAR();
#pragma unroll
    for (int m = 0; m < 3; ++m)
      if (m < 2 || kh == 0) {
        const f32x4 o = ldsf4(ew + kh * 3072 + m * 1024), gb = ldsf4(mo + m * 64);
        const u32 y01 = pk2(c5[m][0] + o[0], c5[m][1] + o[1]), y23 = pk2(c5[m][2] + o[2], c5[m][3] + o[3]);
        const u32 t01 = pk2(gb[0] * lo16(y01), gb[1] * hi16(y01)), t23 = pk2(gb[2] * lo16(y23), gb[3] * hi16(y23));
        const u32x2 r = {pk2(lo16(x1k[m].x) + lo16(t01), hi16(x1k[m].x) + hi16(t01)), pk2(lo16(x1k[m].y) + lo16(t23), hi16(x1k[m].y) + hi16(t23))};
        *(GAS u32x2*)((GAS char*)p.out + oo + m * 32) = r;
      }
  }
}
}

extern "C" void kernel_launch(void* const* d_in, const int* in_sizes, int n_in, void* d_out, int out_size, void* d_ws, size_t ws_size,
                              hipStream_t stream) {
  (void)in_sizes; (void)n_in; (void)out_size; (void)ws_size;
  (void)hipFuncSetAttribute((const void*)k2, hipFuncAttributeMaxDynamicSharedMemorySize, LDS_BYTES);
  P1 q;
  q.x = (const u16*)d_in[0]; q.wqkv = (const u16*)d_in[1]; q.wo = (const u16*)d_in[2];
  q.cvec = (const u16*)d_in[7]; q.wmod = (const u16*)d_in[8]; q.ws = (char*)d_ws + ((ws_size - WS_END) & ~(size_t)0x1fffff);
  k1<<<K1_BLOCKS, 256, 0, stream>>>(q);
  P2 p;
  p.x = (const u16*)d_in[0]; p.wg = (const u16*)d_in[4]; p.wu = (const u16*)d_in[5]; p.wd = (const u16*)d_in[6];
  p.out = (u16*)d_out; p.ws = (char*)d_ws + ((ws_size - WS_END) & ~(size_t)0x1fffff);
  k2<<<256, 512, LDS_BYTES, stream>>>(p);
}
```

```cpp
#include <hip/hip_runtime.h>
#include <hip/hip_bf16.h>
#include <stdint.h>

#define GAS __attribute__((address_space(1)))
#define LAS __attribute__((address_space(3)))
#if defined(__HIP_DEVICE_COMPILE__)
#define NOPK __attribute__((target("no-packed-fp32-ops")))
#else
#define NOPK
#endif
#define DI __device__ __forceinline__ NOPK
#define LAM __attribute__((always_inline)) NOPK
typedef unsigned int u32;
typedef unsigned short u16;
typedef u32 u32x4 __attribute__((ext_vector_type(4)));
typedef u32 u32x2 __attribute__((ext_vector_type(2)));
typedef float f32x2 __attribute__((ext_vector_type(2)));
typedef float f32x4 __attribute__((ext_vector_type(4)));
typedef float f32x16 __attribute__((ext_vector_type(16)));
typedef __bf16 bf16x2 __attribute__((ext_vector_type(2)));
typedef __bf16 bf16x4 __attribute__((ext_vector_type(4)));
typedef __bf16 bf16x8 __attribute__((ext_vector_type(8)));
typedef GAS u32 gu32;
typedef _Float16 f16x8 __attribute__((ext_vector_type(8)));
typedef _Float16 f16x2 __attribute__((ext_vector_type(2)));

#define STAMPS 0

namespace {
constexpr int S = 2048, D = 640, F = 1536;
constexpr u32 WS_KVF = 0;
constexpr u32 WS_CNT = 32768;
constexpr u32 WS_MODP = 65536;
constexpr u32 WS_CSP = WS_MODP + 61440;
constexpr u32 WS_STAT = WS_CSP + 16 * 16 * 1536;
constexpr u32 WS_N1 = WS_STAT + 64 * 8 * 32 * 16;
constexpr u32 WS_OAF = WS_N1 + 64 * 40 * 1024;
constexpr u32 WS_X1 = WS_OAF + 64 * 40 * 1024;
constexpr u32 WS_ACT = WS_X1 + 64 * 40 * 1024;
constexpr u32 KVB = 24576;
constexpr u32 WS_KVX = WS_ACT + 64 * 96 * 1024;
constexpr u32 WQB = 153600;
constexpr u32 WS_WQF = WS_KVX + 256 * KVB;
constexpr u32 WOB = 102400, WGB = 245760, WDB = 245760;
constexpr u32 WS_WOF = WS_WQF + 16 * WQB;
constexpr u32 WS_WGF = WS_WOF + 8 * WOB;
constexpr u32 WS_WDF = WS_WGF + 16 * WGB;
constexpr u32 WS_DBG = WS_WDF + 8 * WDB;
constexpr u32 WS_DBG1 = WS_DBG + 256 * 32 * 8;
constexpr u32 WS_END = WS_DBG1 + 1024 * 16;
constexpr u32 L_MISC = 153600;
constexpr u32 L_GS = L_MISC;
constexpr u32 L_MODS = L_MISC + 6144;
constexpr u32 L_T2 = L_MISC + 7168;
constexpr u32 L_FLAG = L_MISC + 9728;
constexpr u32 LDS_BYTES = 163840;
constexpr u32 L_K = 0, L_V = 30720;
constexpr float QSCALE = 0.22360679774997896f * 1.4426950408889634f;
constexpr int K1_BLOCKS = 488;
#define ATT_LIMIT 1.0e18f
constexpr u32 SPIN_CAP = 1u << 20;

DI float u2f(u32 v) { return __builtin_bit_cast(float, v); }
DI u32 f2u(float v) { return __builtin_bit_cast(u32, v); }
DI int tidx() { return (int)__builtin_amdgcn_workitem_id_x(); }
DI int bidx() { return (int)__builtin_amdgcn_workgroup_id_x(); }
DI float bf2f(u32 v) { return u2f(v << 16); }
DI float lo16(u32 v) { return u2f(v << 16); }
DI float hi16(u32 v) { return u2f(v & 0xffff0000u); }
DI u32 pk2(float a, float b) { f32x2 v = {a, b}; return __builtin_bit_cast(u32, __builtin_convertvector(v, bf16x2)); }
DI u32 pkh2(float a, float b) { f32x2 v = {a, b}; return __builtin_bit_cast(u32, __builtin_convertvector(v, f16x2)); }
DI float hlo(u32 v) { return (float)__builtin_bit_cast(f16x2, v).x; }
DI float hhi(u32 v) { return (float)__builtin_bit_cast(f16x2, v).y; }
DI float rbf(float a) { return lo16(pk2(a, a)); }
DI float fma1(float a, float b, float c) { return __builtin_fmaf(a, b, c); }
DI float mul1(float a, float b) { return a * b; }
DI float add1(float a, float b) { return a + b; }
DI float max3(float a, float b, float c) { return __builtin_fmaxf(__builtin_fmaxf(a, b), c); }
DI float xor32f(float v) {
  auto r = __builtin_amdgcn_permlane32_swap(f2u(v), f2u(v), false, false);
  return u2f((tidx() & 32) ? r[0] : r[1]);
}
DI float sum32(float v) { auto r = __builtin_amdgcn_permlane32_swap(f2u(v), f2u(v), false, false); return u2f(r[0]) + u2f(r[1]); }
DI float sum16(float v) { auto r = __builtin_amdgcn_permlane16_swap(f2u(v), f2u(v), false, false); return u2f(r[0]) + u2f(r[1]); }
DI float max32(float v) { auto r = __builtin_amdgcn_permlane32_swap(f2u(v), f2u(v), false, false); return __builtin_fmaxf(u2f(r[0]), u2f(r[1])); }
template <int CTRL> DI float dppf(float v) { return u2f(__builtin_amdgcn_update_dpp(0u, f2u(v), CTRL, 0xf, 0xf, true)); }

#define MFMA32(a, b, c) __builtin_amdgcn_mfma_f32_32x32x16_bf16(__builtin_bit_cast(bf16x8, a), __builtin_bit_cast(bf16x8, b), c, 0, 0, 0)
#define MFMA32H(a, b, c) __builtin_amdgcn_mfma_f32_32x32x16_f16(__builtin_bit_cast(f16x8, a), __builtin_bit_cast(f16x8, b), c, 0, 0, 0)
#define MFMA16(a, b, c) __builtin_amdgcn_mfma_f32_16x16x32_bf16(__builtin_bit_cast(bf16x8, a), __builtin_bit_cast(bf16x8, b), c, 0, 0, 0)
#define SGB(m, n, id) __builtin_amdgcn_sched_group_barrier(m, n, id)
#define SB() __builtin_amdgcn_sched_barrier(0)
template <bool B> struct BoolC { static constexpr bool v = B; };
template <int I> struct IC { static constexpr int v = I; };
template <int I, int N, class Fn> DI void sfor(Fn&& f) { if constexpr (I < N) { f(IC<I>{}); sfor<I + 1, N>(f); } }
#define PIN(x) asm volatile("" : "+v"(x))
#define WAITV(n) asm volatile("s_waitcnt vmcnt(%0)" ::"i"(n))
#define WAITVM(n) asm volatile("s_waitcnt vmcnt(%0)" ::"i"(n) : "memory")
#define BAR() asm volatile("s_waitcnt lgkmcnt(0)\n\ts_barrier" ::: "memory")
#define RLX_AGENT __ATOMIC_RELAXED, __HIP_MEMORY_SCOPE_AGENT

DI u32x4 lds128(u32 a) { return *(const LAS u32x4*)(size_t)a; }
DI f32x4 ldsf4(u32 a) { return *(const LAS f32x4*)(size_t)a; }
DI u32x2 lds64(u32 a) { return *(const LAS u32x2*)(size_t)a; }
DI float ldsf(u32 a) { return *(const LAS float*)(size_t)a; }
DI void st128(u32 a, u32x4 v) { *(LAS u32x4*)(size_t)a = v; }
DI void stf4(u32 a, f32x4 v) { *(LAS f32x4*)(size_t)a = v; }
DI void st64(u32 a, u32x2 v) { *(LAS u32x2*)(size_t)a = v; }
DI u32x2 ldstr(u32 a) { return __builtin_bit_cast(u32x2, __builtin_amdgcn_ds_read_tr16_b64_v4bf16((LAS bf16x4*)(size_t)a)); }
DI u32x4 ldstr2(u32 a, u32 b) { u32x2 lo = ldstr(a), hi = ldstr(b); return u32x4{lo.x, lo.y, hi.x, hi.y}; }

template <int IMM, bool SC1> DI void dmai(const char* sbase, u32 voff, u32 lds) {
  if constexpr (SC1) asm volatile("s_mov_b32 m0, %2\n\ts_nop 0\n\tglobal_load_lds_dwordx4 %0, %1 offset:%3 sc1" ::"v"(voff), "s"(sbase), "s"(lds), "i"(IMM));
  else asm volatile("s_mov_b32 m0, %2\n\ts_nop 0\n\tglobal_load_lds_dwordx4 %0, %1 offset:%3" ::"v"(voff), "s"(sbase), "s"(lds), "i"(IMM));
}
DI void dma(const char* sbase, u32 voff, u32 lds) { dmai<0, false>(sbase, voff, lds); }
DI void gld32(u32& r, const char* sbase, u32 voff) { asm volatile("global_load_dword %0, %1, %2" : "=v"(r) : "v"(voff), "s"(sbase)); }
DI void gld32_sc1(u32& r, const char* sbase, u32 voff) { asm volatile("global_load_dword %0, %1, %2 sc1" : "=v"(r) : "v"(voff), "s"(sbase)); }
template <int IMM> DI void gld_sc1(u32x4& r, const char* sbase, u32 voff) {
  asm volatile("global_load_dwordx4 %0, %1, %2 offset:%3 sc1" : "=v"(r) : "v"(voff), "s"(sbase), "i"(IMM));
}
template <int IMM> DI void gld(u32x4& r, const char* sbase, u32 voff) {
  asm volatile("global_load_dwordx4 %0, %1, %2 offset:%3" : "=v"(r) : "v"(voff), "s"(sbase), "i"(IMM));
}
DI void gld64(u32x2& r, const char* sbase, u32 voff) {
  asm volatile("global_load_dwordx2 %0, %1, %2" : "=v"(r) : "v"(voff), "s"(sbase));
}
DI void poll_ge(const char* ws, u32 off, u32 want) {
  gu32* p = (gu32*)(ws + off);
  for (u32 spins = 0; spins < SPIN_CAP; ++spins) {
    if ((u32)__builtin_amdgcn_readfirstlane(__hip_atomic_load(p, RLX_AGENT)) >= want) break;
    __builtin_amdgcn_s_sleep(1);
  }
  __builtin_amdgcn_fence(__ATOMIC_ACQUIRE, "wavefront");
}
DI void signal(const char* ws, u32 off) { __hip_atomic_fetch_add((gu32*)(ws + off), 1u, RLX_AGENT); }

struct P1 {
  const u16 *x, *wqkv, *wo, *cvec, *wmod;
  char* ws;
};
#define PFB 16
#define K1AUX 0
__global__ void __launch_bounds__(256) NOPK k1(P1 p) {
  __shared__ float red[32 * 64 + 4 * 64 + 320];
  const int b = bidx(), t = tidx();
  char* ws = p.ws;
  const __amdgpu_buffer_rsrc_t rs = __builtin_amdgcn_make_buffer_rsrc(p.ws, 0, WS_END, 0x00020000);
  if (b >= 408 && b < 408 + PFB) {
    unsigned long long pc;
    asm volatile("s_getpc_b64 s[40:41]\n\ts_add_u32 s40, s40, _ZN12_GLOBAL__N_12k2ENS_2P2E@rel32@lo+4\n\ts_addc_u32 s41, s41, _ZN12_GLOBAL__N_12k2ENS_2P2E@rel32@hi+12\n\ts_mov_b64 %0, s[40:41]"
                 : "=s"(pc) : : "scc", "s40", "s41");
    const u32 csz = 300 * 128;
    const int part = (b - 408) >> 3;
    u32 acc_ = 0;
    for (int o = part * 256 + t; o < (int)(csz >> 7); o += (PFB / 8) * 256) acc_ ^= *(const GAS u32*)(pc + (size_t)o * 128);
    asm volatile("" ::"v"(acc_));
  }
  if (b < 120) {
    const int ch = t & 7, r = t >> 3, col0 = 64 * (b >> 1), row0 = 320 * (b & 1);
    const u32x4 cv = *(const u32x4*)(p.cvec + row0 + 8 * (t < 40 ? t : 39));
    u32x4 wv[10];
#pragma unroll
    for (int i = 0; i < 10; ++i) wv[i] = *(const u32x4*)(p.wmod + (size_t)(row0 + r + 32 * i) * 3840 + col0 + 8 * ch);
    if (t < 40) {
#pragma unroll
      for (int e = 0; e < 8; ++e) {
        const float c = (e & 1) ? hi16(cv[e >> 1]) : lo16(cv[e >> 1]);
        red[2304 + 8 * t + e] = rbf(c * __builtin_amdgcn_rcpf(1.f + __builtin_amdgcn_exp2f(-1.4426950408889634f * c)));
      }
    }
    BAR();
    float acc[8] = {0, 0, 0, 0, 0, 0, 0, 0};
#pragma unroll
    for (int i = 0; i < 10; ++i) {
      const float s = red[2304 + r + 32 * i];
#pragma unroll
      for (int e = 0; e < 4; ++e) { acc[2 * e] = fma1(s, lo16(wv[i][e]), acc[2 * e]); acc[2 * e + 1] = fma1(s, hi16(wv[i][e]), acc[2 * e + 1]); }
    }
    *(f32x4*)&red[r * 64 + 8 * ch] = f32x4{acc[0], acc[1], acc[2], acc[3]};
    *(f32x4*)&red[r * 64 + 8 * ch + 4] = f32x4{acc[4], acc[5], acc[6], acc[7]};
    BAR();
    { const int c = t & 63, part = t >> 6; float s = 0;
#pragma unroll
      for (int i = 0; i < 8; ++i) s += red[(part * 8 + i) * 64 + c];
      red[2048 + part * 64 + c] = s; }
    BAR();
    if (t < 64) {
      const float s = (red[2048 + t] + red[2112 + t]) + (red[2176 + t] + red[2240 + t]);
      __builtin_amdgcn_raw_buffer_store_b32(f2u(s), rs, WS_MODP + ((b & 1) * 3840 + col0 + t) * 4, 0, K1AUX);
    }
    if (b == 0) {
      *(u32*)(ws + WS_KVF + t * 128) = 0u;
      if (t < 72) *(u32*)(ws + WS_CNT + t * 256) = 0u;
    }
  } else if (b < 248) {
    const int hb = b - 120, c = t & 15, tok = 16 * (hb & 1) + (t >> 4);
    const u16* row = p.x + (size_t)(32 * (hb >> 1) + tok) * D;
    u32x4 v[5];
#pragma unroll
    for (int i = 0; i < 5; ++i) v[i] = *(const u32x4*)(row + 8 * (c + 16 * i));
    float s = 0;
#pragma unroll
    for (int i = 0; i < 5; ++i)
#pragma unroll
      for (int e = 0; e < 4; ++e) s += lo16(v[i][e]) + hi16(v[i][e]);
    s += dppf<0xB1>(s); s += dppf<0x4E>(s); s += dppf<0x141>(s); s += dppf<0x140>(s);
    const float mean = s * (1.f / 640.f);
    float q = 0;
#pragma unroll
    for (int i = 0; i < 5; ++i)
#pragma unroll
      for (int e = 0; e < 4; ++e) { const float a = lo16(v[i][e]) - mean, bb = hi16(v[i][e]) - mean; q = fma1(a, a, q); q = fma1(bb, bb, q); }
    q += dppf<0xB1>(q); q += dppf<0x4E>(q); q += dppf<0x141>(q); q += dppf<0x140>(q);
    const float mb = rbf(mean), den = rbf(__builtin_amdgcn_sqrtf(rbf(rbf(q * (1.f / 640.f)) + 1e-5f))), rr = __builtin_amdgcn_rcpf(den);
    const u32 dst = WS_N1 + (u32)(hb >> 1) * 40960 + tok * 16;
#pragma unroll
    for (int i = 0; i < 5; ++i) {
      u32x4 o;
#pragma unroll
      for (int e = 0; e < 4; ++e) {
        const u32 d = pk2(lo16(v[i][e]) - mb, hi16(v[i][e]) - mb);
        const u32 n = pk2(lo16(d) * rr, hi16(d) * rr);
        o[e] = pkh2(lo16(n), hi16(n));
      }
      __builtin_amdgcn_raw_buffer_store_b128(o, rs, dst + (c + 16 * i) * 512, 0, K1AUX);
    }
  } else {
    const u16* src; int stride; u32 off0, off1; bool act = true;
    if (b < 408) {
      const int idx = b - 248, ks = idx >> 2, jq = idx & 3, tc = t < 240 ? t : 239;
      act = t < 240;
      const int seg = tc / 80, cp = tc - 80 * seg;
      src = p.wqkv + (size_t)(16 * ks) * 1920 + 640 * seg + 160 * jq + 2 * cp; stride = 1920;
      const int jj = (2 * cp) / 40, within = 2 * cp - 40 * jj, j = 4 * jq + jj;
      const int n = (seg == 1 ? 0 : seg == 2 ? 40 : 80) + within, nb = n >> 5, r = n & 31;
      off0 = WS_WQF + j * WQB + (nb < 3 ? ((nb * 40 + ks) * 64 + r) * 16 : 122880 + ks * 768 + r * 16);
      off1 = off0 + (nb < 3 ? 512 : 384);
    } else {
      const int idx = b - 408, k16 = idx >> 1, tc = t < 160 ? t : 159, n = (idx & 1) * 320 + 2 * tc;
      act = t < 160;
      src = p.wo + (size_t)(16 * k16) * 640 + n; stride = 640;
      const int kh = k16 >= 20, kk = k16 - 20 * kh, st = kk >> 1, H = kk & 1;
      const int ntg = n >> 4, n8 = ntg / 5, nt = ntg - 5 * n8;
      off0 = WS_WOF + n8 * WOB + (st * 10 + kh * 5 + nt) * 1024 + (2 * H * 16 + (n & 15)) * 16;
      off1 = off0 + 256;
    }
    u32 v[16];
#pragma unroll
    for (int i = 0; i < 16; ++i) v[i] = *(const u32*)(src + (size_t)i * stride);
    if (b < 408) {
#pragma unroll
      for (int i = 0; i < 16; ++i) v[i] = pkh2(lo16(v[i]) * 64.f, hi16(v[i]) * 64.f);
    }
    if (act) {
#pragma unroll
      for (int h = 0; h < 2; ++h) {
        u32x4 a, bq;
#pragma unroll
        for (int e = 0; e < 4; ++e) {
          a[e] = __builtin_amdgcn_perm(v[8 * h + 2 * e + 1], v[8 * h + 2 * e], 0x05040100u);
          bq[e] = __builtin_amdgcn_perm(v[8 * h + 2 * e + 1], v[8 * h + 2 * e], 0x07060302u);
        }
        __builtin_amdgcn_raw_buffer_store_b128(a, rs, h ? off1 : off0, 0, K1AUX);
        __builtin_amdgcn_raw_buffer_store_b128(bq, rs, (h ? off1 : off0) + 16, 0, K1AUX);
      }
    }
  }
}

struct P2 {
  const u16 *x, *wg, *wu, *wd;
  u16* out;
  char* ws;
};
#define MAP 0
constexpr u32 C_O = WS_CNT, C_RG = WS_CNT + 48 * 256, C_RD = WS_CNT + 64 * 256;
#define OPQ(x) asm volatile("" : "+s"(x))

DI constexpr int gu_unit(int p, int kh, int i) {
  return p == 0 ? 20 * kh + i
       : p == 1 ? (i < 16 ? 40 + 16 * kh + i : 8 + 20 * kh + (i - 16))
                : (i < 8 ? 12 + 20 * kh + i : 40 + 16 * kh + (i - 8));
}
DI constexpr u32 wo_slot(int s) { return s < 7 ? (72 + 10 * s) * 1024 : (s - 7) * 10240; }
DI constexpr u32 wd_slot(int s) { return (s < 15 ? s : s - 15) * 10240; }
template <int N, int NW, bool SC1> struct Run {
  static constexpr int C = (N + NW - 1) / NW;
  const char* src; u32 lds;
  DI Run(const char* s, u32 l, int x) { int o = x * C; o = (o < N - C ? o : N - C) * 1024; src = s + o; lds = l + o; }
  template <int M> DI void go(u32 l16) const { if constexpr (M < C) dmai<(M & 3) * 1024, SC1>(src + (M >> 2) * 4096, l16, lds + (M >> 2) * 4096); }
  DI void all(u32 l16) const { sfor<0, C>([&](auto m_) LAM { go<decltype(m_)::v>(l16); }); }
};
template <int P, int I0, int I1, int NW> DI auto gu_run(const char* blob, int x) {
  const int k = x >= NW / 2;
  return Run<2 * (I1 - I0), NW / 2, true>(blob + ((P * 2 + k) * 20 + I0) * 2048, (k ? gu_unit(P, 1, I0) : gu_unit(P, 0, I0)) * 2048, x - k * (NW / 2));
}
DI u32 pkfma16(u32 x, u32 y, u32 z) { return __builtin_bit_cast(u32, __builtin_elementwise_fma(__builtin_bit_cast(f16x2, x), __builtin_bit_cast(f16x2, y), __builtin_bit_cast(f16x2, z))); }
DI void gld4_if(int cond, u32x4& r0, u32x4& r1, u32x4& r2, u32x4& r3, const char* sbase, u32 voff) {
  asm volatile("s_cmp_eq_u32 %6, 0\n\ts_cbranch_scc1 .Lskip%=\n\tglobal_load_dwordx4 %0, %4, %5\n\tglobal_load_dwordx4 %1, %4, %5 offset:1024\n\t"
               "global_load_dwordx4 %2, %4, %5 offset:2048\n\tglobal_load_dwordx4 %3, %4, %5 offset:3072\n.Lskip%=:"
               : "+v"(r0), "+v"(r1), "+v"(r2), "+v"(r3) : "v"(voff), "s"(sbase), "s"(cond) : "scc");
}
DI bool has_sent(const u32x4& v) {
  const u32 m = __builtin_elementwise_max(__builtin_elementwise_max(v.x, v.y), __builtin_elementwise_max(v.z, v.w));
  return __builtin_amdgcn_ballot_w64(m == 0xffffffffu) != 0ull;
}
template <int IMM> DI void ensure(u32x4& v, const char* sbase, u32 voff) {
  if (__builtin_expect(has_sent(v), 0)) {
    for (u32 spins = 0; spins < SPIN_CAP; ++spins) {
      gld_sc1<IMM>(v, sbase, voff); WAITV(0); PIN(v);
      if (!has_sent(v)) break;
    }
  }
}
DI void gate(const char* sbase, u32 voff) {
  for (u32 spins = 0; spins < SPIN_CAP; ++spins) {
    u32 v;
    gld32_sc1(v, sbase, voff); WAITV(0); PIN(v);
    if (__builtin_amdgcn_ballot_w64(v == 0xffffffffu) == 0ull) break;
  }
}

__global__ void __launch_bounds__(512, 2) NOPK k2(P2 p) {
  const int tid = tidx(), lane = tid & 63;
  int w = __builtin_amdgcn_readfirstlane(tid >> 6);
  const int b = bidx();
  int mt = MAP == 0 ? 4 * ((b & 7) >> 1) + (b >> 6) : (b & 7) * 2 + (b >> 7), j = MAP == 0 ? 8 * (b & 1) + ((b >> 3) & 7) : (b >> 3) & 15;
  const int c32 = lane & 31, h = lane >> 5;
  const char* ws = p.ws;
  const __amdgpu_buffer_rsrc_t rs = __builtin_amdgcn_make_buffer_rsrc(p.ws, 0, WS_END, 0x00020000);
  const u32 l16 = lane * 16;
#define PHASE() OPQ(w); OPQ(mt); OPQ(j); OPQ(ws); const int tt = w & 3, kh = w >> 2, th = j & 1, n8 = j >> 1; (void)tt; (void)kh; (void)th; (void)n8
#define FINE 0
#define CSEL 0
#define FST(k, i) do {} while (0)
#define STAMP(i) do {} while (0)
#define CST(k, i) do {} while (0)
  STAMP(0); CST(3, 18);
  u32x4 a[24];
  u32x4 st[18];
  const int rsw = c32 ^ (16 * (w >> 2));
  const u32 frow = (h * 32 + rsw) * 16;
  u32 imga[8];
  {
    PHASE();
    const char* wq = ws + WS_WQF + (size_t)j * WQB;
    const char* wqw = wq + (kh * 20 + tt) * 1024;
    const u32 wql = (kh * 20 + tt) * 1024;
    const char* n1 = ws + WS_N1 + (size_t)((mt * 4 + tt) * 40 + kh * 20) * 1024;
    auto issA = [&](auto k_) LAM { constexpr int k = decltype(k_)::v; gld<(k & 3) * 1024>(a[k], n1 + (k >> 2) * 4096, l16); };
    auto issW = [&](int s, int nb) LAM { dma(wqw + nb * 40960 + s * 4096, l16, wql + nb * 40960 + s * 4096); };
    u32x4 mp[2];
    u32 mdst; bool plus1;
    {
      const int m1 = tid >= 160, k1 = 4 * (tid - 160 * m1);
      const int u2 = tid - 320, m2 = (u2 * 205) >> 12, e2 = u2 - 20 * m2;
      const int col2 = (m2 == 0 ? 1280 : m2 == 2 ? 3200 : m2 == 4 ? 1920 : 2560) + 80 * (m2 < 3 ? n8 : (mt >> 1)) + 4 * (m2 < 5 ? e2 : 0);
      const int col = tid < 320 ? 640 * m1 + k1 : col2;
#pragma unroll
      for (int q = 0; q < 2; ++q) gld<0>(mp[q], ws + WS_MODP + q * 15360, (u32)col * 4);
      sfor<0, 4>(issA);
      issW(0, 0); issW(0, 1); issW(0, 2);
      mdst = tid < 320 ? L_GS + (k1 >> 3) * 32 + (m1 ? 0 : 16) + (k1 & 4) * 2 : L_MODS + u2 * 16;
      plus1 = tid < 320 ? m1 != 0 : (m2 & 1) != 0;
    }
    if (tid == 0) *(LAS u32*)(size_t)L_FLAG = 0u;
    const int rk16 = w < 5 ? 5 * (mt >> 1) + w : (mt * 2 + th) * 3 + (w - 5);
    const char* rsrc = w < 5 ? (const char*)((mt & 1) ? p.wu : p.wg) + (size_t)rk16 * 49152 + 192 * j : (const char*)p.wd + (size_t)rk16 * 20480 + 160 * n8;
    const u32 rimg = 122880 + w * 3072;
    auto issR = [&]() LAM {
#pragma unroll
      for (int m = 0; m < 3; ++m) {
        const u32 x = 64 * m + lane, i5 = x < 160 ? x : 159, r12 = (x * 171) >> 11, r10 = (i5 * 205) >> 11;
        dma(rsrc, w < 5 ? r12 * 2880 + x * 16 : r10 * 1120 + i5 * 16, rimg + m * 1024);
      }
    };
    f32x16 acc[3];
    const u32 wb = kh * 20480 + frow, gb_ = L_GS + kh * 1280 + h * 32;
    u32x4 gs[3][2], wf[2][3], ro[3];
    auto ldgs = [&](int k) LAM { gs[k % 3][0] = lds128(gb_ + k * 64); gs[k % 3][1] = lds128(gb_ + k * 64 + 16); };
    auto modulate = [&](int k) LAM {
      PIN(a[k]);
#pragma unroll
      for (int e = 0; e < 4; ++e)
        a[k][e] = pkfma16(a[k][e], gs[k % 3][0][e], gs[k % 3][1][e]);
    };
    FST(3, 1);
    WAITV(7);
    FST(3, 2);
    f32x4 mv;
    {
#pragma unroll
      for (int q = 0; q < 2; ++q) PIN(mp[q]);
      float mxv = 0.f;
#pragma unroll
      for (int e = 0; e < 4; ++e) {
        const float s = rbf(u2f(mp[0][e]) + u2f(mp[1][e]));
        mv[e] = plus1 ? rbf(1.f + s) : s;
        mxv = __builtin_fmaxf(mxv, __builtin_fabsf(mv[e]));
      }
      if (tid < 320) st64(mdst, u32x2{pkh2(mv[0], mv[1]), pkh2(mv[2], mv[3])}); else if (tid < 420) stf4(mdst, mv);
      const bool big = __builtin_amdgcn_ballot_w64(tid < 320 && !(mxv < 2000.f)) != 0ull;
      if (lane == 0) *(LAS u32*)(size_t)(L_FLAG + 16 + w * 4) = big;
    }
    WAITV(0);
    BAR();
    FST(3, 3);
    auto grpA = [&](int cond, int s) LAM { gld4_if(cond, a[4 * s], a[4 * s + 1], a[4 * s + 2], a[4 * s + 3], n1 + s * 4096, l16); };
    const int kl = kh ^ 1;
    auto grpW = [&](int s) LAM { issW(s, 0); issW(s, 1); issW(s, 2); };
    grpA(kh, 1); if (kh) { grpW(1); issR(); }
    bool scaled;
    {
      const u32x4 f0 = lds128(L_FLAG + 16);
      const u32 f1 = *(const LAS u32*)(size_t)(L_FLAG + 32);
      ldgs(0); ldgs(1);
      scaled = __builtin_amdgcn_readfirstlane(f0.x | f0.y | f0.z | f0.w | f1) != 0;
      if (scaled) {
        BAR();
        const float sc = 9.765625e-4f;
        if (tid < 320) st64(mdst, u32x2{pkh2(mv[0] * sc, mv[1] * sc), pkh2(mv[2] * sc, mv[3] * sc)});
        BAR();
        ldgs(0); ldgs(1);
      }
    }
    modulate(0);
    SB();
    const Run<30, 8, false> rb3(wq + 122880, 122880, w);
    sfor<0, 21>([&](auto k_) LAM {
      constexpr int k = decltype(k_)::v, s = k >> 2, i = k & 3;
      if constexpr (i == 3 && k < 16) WAITV(k == 3 || k == 15 ? 13 : 10);
      if constexpr (i == 0 && k > 0 && k < 20) {
        FST(3, 3 + 3 * s); WAITV(k == 4 || k == 12 ? 10 : 7); FST(3, 4 + 3 * s); BAR(); FST(3, 5 + 3 * s);
        if (s == 2 && CSEL == 4) { CST(4, 18); STAMP(16); } if (s == 3 && CSEL == 4) { CST(4, 19); STAMP(17); }
      }
      if constexpr (k < 20) {
#pragma unroll
        for (int nb = 0; nb < 3; ++nb) wf[k & 1][nb] = lds128(wb + nb * 40960 + k * 1024);
      }
      if constexpr (k == 1) {
        f32x16 z;
#pragma unroll
        for (int e = 0; e < 16; ++e) z[e] = 0.f;
#pragma unroll
        for (int nb = 0; nb < 3; ++nb) acc[nb] = MFMA32H(wf[0][nb], a[0], z);
      } else if constexpr (k > 1) {
#pragma unroll
        for (int nb = 0; nb < 3; ++nb) acc[nb] = MFMA32H(wf[(k - 1) & 1][nb], a[k - 1], acc[nb]);
      }
      if constexpr (k + 1 < 20) modulate(k + 1);
      if constexpr (k + 2 < 20) ldgs(k + 2);
      if constexpr (k == 8) {
        const u32 q4 = (lane >> 2) & 3, nh = (lane >> 4) & 1;
        const u32 r0 = rimg + (w < 5 ? (8 * h + q4) * 192 + 32 * nh : (4 * nh + q4) * 160 + 32 * h) + (lane & 3) * 8, r1 = r0 + (w < 5 ? 768 : 1280);
#pragma unroll
        for (int u = 0; u < 3; ++u) ro[u] = ldstr2(r0 + 64 * u, r1 + 64 * u);
      }
      if constexpr (k == 9 || k == 10) {
        if (w < 5) {
          const u32 ga_ = L_MODS + 960 + w * 64 + h * 32;
          const f32x4 g0 = ldsf4(ga_), g1 = ldsf4(ga_ + 16), s0 = ldsf4(ga_ + 320), s1 = ldsf4(ga_ + 336);
#pragma unroll
          for (int u = (k == 9 ? 0 : 2); u < (k == 9 ? 2 : 3); ++u) {
            float c = 0.f, bi = 0.f;
#pragma unroll
            for (int e = 0; e < 4; ++e) {
              const u32 v = ro[u][e];
              const float x0 = lo16(v), x1 = hi16(v), ge = e < 2 ? g0[2 * e] : g1[2 * e - 4], go = e < 2 ? g0[2 * e + 1] : g1[2 * e - 3];
              const float se = e < 2 ? s0[2 * e] : s1[2 * e - 4], so_ = e < 2 ? s0[2 * e + 1] : s1[2 * e - 3];
              c = fma1(ge, x0, c); c = fma1(go, x1, c); bi = fma1(se, x0, bi); bi = fma1(so_, x1, bi);
            }
            auto r = __builtin_amdgcn_permlane32_swap(f2u(c), f2u(bi), false, false);
            *(LAS float*)(size_t)(w * 768 + u * 256 + (c32 >> 3) * 64 + h * 32 + (c32 & 7) * 4) = u2f(r[0]) + u2f(r[1]);
          }
        }
      }
      if constexpr (k == 13) {
        if (w == 7) {
          f32x4 csum = {0.f, 0.f, 0.f, 0.f};
#pragma unroll
          for (int u = 0; u < 5; ++u) csum += ldsf4(u * 768 + (lane < 48 ? l16 : 0u));
          if (lane < 48) __builtin_amdgcn_raw_buffer_store_b128(__builtin_bit_cast(u32x4, csum), rs, WS_CSP + (u32)(j * 16 + mt) * 768 + l16, 0, 16);
        }
      }
      if constexpr (k == 16) {
        const u32 x2 = 16 * kh + 4 * h, tb = 12288 + (32 * tt + c32) * 48 + 2 * x2;
#pragma unroll
        for (int u = 0; u < 8; ++u) {
          const u32 part = ((x2 + 32 * (u >> 1) + 8 * (u & 1)) * 205) >> 12;
          imga[u] = part * 18392 + tb + 64 * (u >> 1) + 16 * (u & 1); PIN(imga[u]);
        }
      }
      SB();
      if constexpr (k == 0) { grpA(kl, 1); if (!kh) { grpW(1); issR(); } }
      if constexpr (k == 1) grpA(kl, 2);
      if constexpr (k == 2) { if (!kh) grpW(2); grpA(kh, 2); }
      if constexpr (k == 3) { if (kh) grpW(2); }
      if constexpr (s == 1 || s == 2) {
        if constexpr (i == 0) grpA(kl, s + 2);
        if constexpr (i == 1) { if (!kh) grpW(s + 2); }
        if constexpr (i == 2) grpA(kh, s + 2);
        if constexpr (i == 3) { if (kh) grpW(s + 2); }
      }
      if constexpr (k == 11) {
        u32 off0, du;
        if (w < 5) { const int k2 = rk16 >= 20, i2 = rk16 - 20 * k2; off0 = WS_WGF + j * WGB + (k2 * 20 + i2) * 2048 + (mt & 1) * 1024 + l16; du = 81920; }
        else { const int k2 = rk16 >= 48, kk = rk16 - 48 * k2; off0 = WS_WDF + n8 * WDB + ((kk >> 1) * 10 + k2 * 5 + h) * 1024 + (kk & 1) * 512 + c32 * 16; du = 2048; }
        asm volatile("" ::: "memory");
        __builtin_amdgcn_raw_buffer_store_b128(ro[0], rs, off0, 0, 16);
        __builtin_amdgcn_raw_buffer_store_b128(ro[1], rs, off0 + du, 0, 16);
        __builtin_amdgcn_raw_buffer_store_b128(ro[2], rs, (w < 5 || h == 0) ? off0 + 2 * du : 0xfffffff0u, 0, 16);
        asm volatile("" ::: "memory");
      }
      if constexpr (k == 12) { if (!kh) rb3.all(l16); }
      if constexpr (k == 14) { if (kh) rb3.all(l16); }
      SB();
    });
    STAMP(1); CST(3, 19); FST(6, 1);
    WAITV(0);
    BAR();
    FST(6, 2);
    const u32 exw = (w >> 1) * 18432 + (w & 1) * 6144 + l16, exr = ((w ^ 4) >> 1) * 18432 + (w & 1) * 6144 + l16;
#pragma unroll
    for (int nb = 0; nb < 3; ++nb) {
      stf4(exw + nb * 2048, f32x4{acc[nb][8], acc[nb][9], acc[nb][10], acc[nb][11]});
      stf4(exw + nb * 2048 + 1024, f32x4{acc[nb][12], acc[nb][13], acc[nb][14], acc[nb][15]});
    }
    if (kh) st64((tid >> 7) * 18432 + 12288 + (tid & 127) * 48 + 40, u32x2{0x3f803f80u, 0x3f803f80u});
    BAR();
    FST(6, 3);
    const float osc = scaled ? 1024.f : 1.f, qs = QSCALE * (1.f / 4096.f) * osc, sc2 = kh ? qs : osc;
#pragma unroll
    for (int nb = 0; nb < 3; ++nb)
#pragma unroll
      for (int q = 0; q < 2; ++q) {
        const f32x4 o = ldsf4(exr + nb * 2048 + q * 1024);
        float v0 = acc[nb][4 * q] + o[0], v1 = acc[nb][4 * q + 1] + o[1], v2 = acc[nb][4 * q + 2] + o[2], v3 = acc[nb][4 * q + 3] + o[3];
        if (nb == 2) { v0 *= sc2; v1 *= sc2; v2 *= sc2; v3 *= sc2; }
        else if (scaled) { v0 *= osc; v1 *= osc; v2 *= osc; v3 *= osc; }
        st64(imga[2 * nb + q], u32x2{pk2(v0, v1), pk2(v2, v3)});
      }
    BAR();
    FST(6, 4);
    {
      const u32 dst = WS_KVX + (u32)(mt * 16 + j) * KVB + w * 3072 + l16, src = (w >> 1) * 18432 + 12288 + (w & 1) * 3072 + l16;
#pragma unroll
      for (int i = 0; i < 3; ++i) __builtin_amdgcn_raw_buffer_store_b128(lds128(src + i * 1024), rs, dst + i * 1024, 0, 16);
    }
    f32x16 acc3;
#pragma unroll
    for (int e = 0; e < 16; ++e) acc3[e] = 0.f;
    {
      const int r3 = rsw < 24 ? rsw : (rsw < 28 ? rsw - 4 : rsw - 12);
      const u32 wb3 = 122880 + kh * 15360 + (h * 24 + r3) * 16;
      u32x4 w3[4];
#pragma unroll
      for (int k = 0; k < 3; ++k) w3[k] = lds128(wb3 + k * 768);
#pragma unroll
      for (int k = 0; k < 20; ++k) {
        if (k + 3 < 20) w3[(k + 3) & 3] = lds128(wb3 + (k + 3) * 768);
        acc3 = MFMA32H(w3[k & 3], a[k], acc3);
        if (k == 9) {
          FST(6, 5);
          WAITVM(0);
          FST(6, 6);
          if (lane == 0) {
            const u32 old = __hip_atomic_fetch_add((LAS u32*)(size_t)L_FLAG, 1u, __ATOMIC_RELAXED, __HIP_MEMORY_SCOPE_WORKGROUP);
            if (old == 7u) {
              __hip_atomic_store((gu32*)(ws + WS_KVF + (mt * 16 + j) * 128), 1u, RLX_AGENT);
              signal(ws, C_RG + j * 256); signal(ws, C_RD + n8 * 256);
            }
          }
        }
        SB();
      }
    }
    const u32 ex3w = (w < 4 ? 73728 : 84992) + w * 2048 + l16, ex3r = (w < 4 ? 93184 : 65536) + w * 2048 + l16;
    stf4(ex3w, f32x4{acc3[8], acc3[9], acc3[10], acc3[11]});
    stf4(ex3w + 1024, f32x4{acc3[12], acc3[13], acc3[14], acc3[15]});
    FST(6, 7);
    BAR();
    FST(6, 8);
    STAMP(2);
#pragma unroll
    for (int q = 0; q < 2; ++q) {
      const f32x4 o = ldsf4(ex3r + q * 1024);
      st64(imga[6 + q], u32x2{pk2((acc3[4 * q] + o[0]) * qs, (acc3[4 * q + 1] + o[1]) * qs), pk2((acc3[4 * q + 2] + o[2]) * qs, (acc3[4 * q + 3] + o[3]) * qs)});
    }
    BAR();
    FST(6, 9);
  }
  u32x2 xr[3];
  const int c16 = lane & 15, g4 = lane >> 4;
  {
    PHASE();
    const int hh = w >> 2, qb = w & 3;
    u32x4 q0f, q1f;
    {
      const u32 qa = (4 + hh) * 18432 + 12288 + (32 * qb + c32) * 48;
      const u32x2 x2 = lds64(qa + 32);
      q0f = lds128(qa + 16 * h);
      q1f = u32x4{h ? 0u : x2.x, h ? 0u : x2.y, 0u, 0u};
    }
    if (w == 0) {
      const int l3 = lane & 3, back = l3 + 1 <= mt ? l3 + 1 : 0;
      const u32 fo = l3 == 3 ? C_RD + n8 * 256 : (l3 == 2 || back == 0) ? C_RG + j * 256 : WS_KVF + ((mt - back) * 16 + j) * 128;
      const u32 want = l3 == 3 ? 32u : l3 == 2 ? 16u : 1u;
      for (u32 spins = 0; spins < SPIN_CAP; ++spins) {
        u32 v;
        gld32_sc1(v, ws, fo); WAITV(0); PIN(v);
        if (__builtin_amdgcn_ballot_w64(v < want) == 0ull) break;
      }
    }
    BAR();
    STAMP(3); FST(6, 10);
    {
      const u32 l0 = (w >> 1) * 18432 + (w & 1) * 3072;
#pragma unroll
      for (int sl = 1; sl >= 0; --sl) {
        if (mt + sl >= 2) {
          const char* src = ws + WS_KVX + (size_t)((mt - 2 + sl) * 16 + j) * KVB + w * 3072;
          dmai<0, true>(src, l16, l0 + sl * 6144); dmai<1024, true>(src, l16, l0 + sl * 6144); dmai<2048, true>(src, l16, l0 + sl * 6144);
        } else {
          const u32x4 z = {0u, 0u, 0u, 0u};
          st128(l0 + sl * 6144 + l16, z); st128(l0 + sl * 6144 + 1024 + l16, z); st128(l0 + sl * 6144 + 2048 + l16, z);
        }
      }
    }
    FST(6, 11);
    {
      const int tok64 = 128 * mt + 64 * th + 16 * tt + c16;
#pragma unroll
      for (int m = 0; m < 3; ++m) gld64(xr[m], (const char*)p.x, (u32)(tok64 * 1280 + (80 * n8 + 16 * ((m + 3 * kh) % 5) + 4 * g4) * 2));
    }
    FST(6, 12);
    WAITV(3);
    FST(6, 13);
    BAR();
    STAMP(4); CST(1, 18); FST(4, 12); FST(6, 14);
    const char* sg0 = ws + WS_WGF + (size_t)j * WGB + (kh * 40 + tt * 10) * 1024;
    const int tmin = mt >= 2 ? 0 : 8 - 4 * mt - qb;
    const u32 kb = hh * 18432 + (32 * qb + c32) * 48, kb2 = kb + 16 * h;
    const u32 vb = (2 + hh) * 18432 + (32 * qb + 4 * h + ((lane >> 2) & 3)) * 48 + ((lane >> 4) & 1) * 32 + (lane & 3) * 8;
    const int qlim = c32 - 4 * h;
    const Run<70, 4, false> rwo(ws + WS_WOF + (size_t)n8 * WOB, wo_slot(0), w & 3);
    const u32 fa = WS_ACT + (u32)((mt * 4 + (w & 3)) * 96 + 6 * j) * 1024 + l16;
    const u32 fx = w < 2 ? WS_X1 + (u32)((mt * 4 + 2 * th + w) * 40 + 5 * n8) * 1024 + l16 : WS_STAT + (u32)((mt * 4 + 2 * th + h) * 8 + n8) * 512 + c32 * 16;
    f32x16 oacc;
    auto attn = [&](auto full_) LAM {
      constexpr bool FULL = decltype(full_)::v;
      u32x4 ka[2], kq[2], vf[2][2];
      kq[0] = u32x4{0u, 0u, 0u, 0u}; kq[1] = kq[0];
      auto ldk = [&](int t, int sl) LAM {
        const u32x2 x2 = lds64(kb + t * 1536 + 32);
        ka[sl] = lds128(kb2 + t * 1536); kq[sl].x = x2.x; kq[sl].y = x2.y;
      };
      auto ldv = [&](int t, int sl) LAM {
#pragma unroll
        for (int s2 = 0; s2 < 2; ++s2) { const u32 va = vb + (32 * t + 16 * s2) * 48; vf[sl][s2] = ldstr2(va, va + 384); }
      };
      auto splat = [&](float v) LAM -> f32x16 {
        f32x16 c;
#pragma unroll
        for (int e = 0; e < 16; ++e) c[e] = v;
        return c;
      };
      auto pre = [&](auto m_) LAM {
        constexpr int m = decltype(m_)::v;
        gld_sc1<(m & 3) * 1024>(st[m], sg0 + (m >> 2) * 4096, l16);
        if (w < 4) {
          rwo.go<2 * m>(l16); rwo.go<2 * m + 1>(l16);
          const u32x4 f = {~0u, ~0u, ~0u, ~0u};
          if constexpr (m < 6) __builtin_amdgcn_raw_buffer_store_b128(f, rs, fa + m * 1024, 0, 16);
          if constexpr (m >= 5) { if (w < 2 || (w == 2 && m == 5)) __builtin_amdgcn_raw_buffer_store_b128(f, rs, fx + (m - 5) * 1024, 0, 16); }
        }
        SB();
      };
      f32x16 sb[2], cm;
      float pe[16];
      FST(4, 1);
      ldk(8, 0); ldk(0, 1); ldv(0, 0); ldv(8, 1);
      sb[0] = MFMA32(ka[0], q0f, splat(0.f));
      sb[1] = MFMA32(ka[1], q0f, splat(FULL ? 0.f : (tmin == 0 ? 0.f : -1.0e30f)));
      sb[0] = MFMA32(kq[0], q1f, sb[0]);
      sb[1] = MFMA32(kq[1], q1f, sb[1]);
      ldk(1, 0); ldk(2, 1);
      SB();
      pre(IC<0>{});
#pragma unroll
      for (int e = 0; e < 16; ++e) pe[e] = ((e & 3) + 8 * (e >> 2) > qlim) ? sb[1][e] : sb[0][e];
      float mx = __builtin_fmaxf(pe[0], pe[1]);
#pragma unroll
      for (int e = 2; e < 16; e += 2) mx = max3(mx, pe[e], pe[e + 1]);
      mx = -max32(mx);
      cm = splat(mx);
      auto cin = [&](int t) LAM -> f32x16 { if constexpr (FULL) return cm; else return splat(t >= tmin ? mx : -1.0e30f); };
      sb[0] = MFMA32(ka[0], q0f, cin(1));
      FST(4, 2);
      SB();
#pragma unroll
      for (int e = 0; e < 8; ++e) pe[e] = __builtin_amdgcn_exp2f(pe[e] + mx);
      SB();
      sb[0] = MFMA32(kq[0], q1f, sb[0]);
      SB();
#pragma unroll
      for (int e = 8; e < 16; ++e) pe[e] = __builtin_amdgcn_exp2f(pe[e] + mx);
      SB();
      sfor<0, 4>([&](auto u_) LAM {
        constexpr int u = decltype(u_)::v, s2 = u & 1, t8 = u >> 1;
        u32x4 pf;
#pragma unroll
        for (int e = 0; e < 4; ++e) {
          const int e0 = 8 * s2 + 2 * e, e1 = e0 + 1;
          const bool m0 = ((e0 & 3) + 8 * (e0 >> 2) > qlim) != (t8 != 0), m1 = ((e1 & 3) + 8 * (e1 >> 2) > qlim) != (t8 != 0);
          pf[e] = pk2(m0 ? pe[e0] : 0.f, m1 ? pe[e1] : 0.f);
        }
        if constexpr (u == 0) oacc = MFMA32(vf[0][0], pf, splat(0.f)); else oacc = MFMA32(vf[t8][s2], pf, oacc);
        if constexpr (u == 1) ldv(1, 0);
        SB();
        if constexpr (u == 1) pre(IC<1>{});
        if constexpr (u == 3) pre(IC<2>{});
      });
      sfor<1, 8>([&](auto t_) LAM {
        constexpr int t = decltype(t_)::v, c = (t + 1) & 1, n = t & 1;
        u32x4 pf0, pf1;
        FST(4, 2 + t);
        if constexpr (t < 7) { sb[n] = MFMA32(ka[n], q0f, cin(t + 1)); SB(); ldv(t + 1, n); }
#pragma unroll
        for (int e = 0; e < 8; ++e) pe[e] = __builtin_amdgcn_exp2f(sb[c][e]);
        SB();
        if constexpr (t < 7) { sb[n] = MFMA32(kq[n], q1f, sb[n]); SB(); }
#pragma unroll
        for (int e = 8; e < 12; ++e) pe[e] = __builtin_amdgcn_exp2f(sb[c][e]);
#pragma unroll
        for (int e = 0; e < 4; ++e) pf0[e] = pk2(pe[2 * e], pe[2 * e + 1]);
        SB();
        oacc = MFMA32(vf[c][0], pf0, oacc);
        SB();
        if constexpr (t + 2 < 8) ldk(t + 2, c);
#pragma unroll
        for (int e = 12; e < 16; ++e) pe[e] = __builtin_amdgcn_exp2f(sb[c][e]);
#pragma unroll
        for (int e = 0; e < 4; ++e) pf1[e] = pk2(pe[8 + 2 * e], pe[8 + 2 * e + 1]);
        SB();
        oacc = MFMA32(vf[c][1], pf1, oacc);
        SB();
        pre(IC<2 + t>{});
      });
    };
    auto exact = [&]() LAM {
      auto sc = [&](int t, float c0, f32x16& s) LAM {
        const u32x2 x2 = lds64(kb + t * 1536 + 32);
#pragma unroll
        for (int e = 0; e < 16; ++e) s[e] = c0;
        s = MFMA32(lds128(kb2 + t * 1536), q0f, s);
        s = MFMA32((u32x4{x2.x, x2.y, 0u, 0u}), q1f, s);
#pragma unroll
        for (int e = 0; e < 16; ++e) s[e] = (u32)(32 * t + (e & 3) + 8 * (e >> 2) - qlim - 1) < 256u ? s[e] : -1.0e30f;
      };
      float mx = -1.0e30f;
      f32x16 s;
#pragma nounroll
      for (int t = tmin; t < 9; ++t) {
        sc(t, 0.f, s);
#pragma unroll
        for (int e = 0; e < 16; e += 2) mx = max3(mx, s[e], s[e + 1]);
      }
      mx = -max32(mx);
#pragma unroll
      for (int e = 0; e < 16; ++e) oacc[e] = 0.f;
#pragma nounroll
      for (int t = tmin; t < 9; ++t) {
        sc(t, mx, s);
#pragma unroll
        for (int s2 = 0; s2 < 2; ++s2) {
          u32x4 pf;
#pragma unroll
          for (int e = 0; e < 4; ++e) pf[e] = pk2(__builtin_amdgcn_exp2f(s[8 * s2 + 2 * e]), __builtin_amdgcn_exp2f(s[8 * s2 + 2 * e + 1]));
          const u32 va = vb + (32 * t + 16 * s2) * 48;
          oacc = MFMA32(ldstr2(va, va + 384), pf, oacc);
        }
      }
    };
    const int ko = 40 * j + 20 * hh + 4 * h;
    u32 ob = WS_OAF + (u32)((mt * 4 + qb) * 40) * 1024 + c32 * 16 + (ko >> 3) * 512 + ((ko >> 2) & 1) * 8;
    PIN(ob);
    if (mt >= 2) attn(BoolC<true>{}); else attn(BoolC<false>{});
    FST(4, 10);
    if (__builtin_amdgcn_ballot_w64(h != 0 && !(oacc[8] < ATT_LIMIT)) != 0ull) exact();
    const float lx = xor32f(oacc[8]);
    const float inv = __builtin_amdgcn_rcpf(64.f * (h ? oacc[8] : lx));
#pragma unroll
    for (int g = 0; g < 3; ++g) {
      const u32x2 o = {pk2(oacc[4 * g] * inv, oacc[4 * g + 1] * inv), pk2(oacc[4 * g + 2] * inv, oacc[4 * g + 3] * inv)};
      if (g < 2 || h == 0) __builtin_amdgcn_raw_buffer_store_b64(o, rs, ob + g * 512, 0, 16);
    }
    STAMP(5); CST(1, 19); FST(4, 11);
    WAITVM(0);
    BAR();
    if (tid == 0) signal(ws, C_O + mt * 256);
    STAMP(6); FST(4, 13);
  }
  u32 wbase[5], avoff;
  u32x2 x1k[3];
  f32x4 c5[5];
  {
    PHASE();
    const char* sg1 = ws + WS_WGF + (size_t)j * WGB + 81920 + (kh * 40 + tt * 8) * 1024;
    auto stage1 = [&]() LAM { sfor<0, 8>([&](auto m_) LAM { constexpr int m = decltype(m_)::v; gld_sc1<(m & 3) * 1024>(st[10 + m], sg1 + (m >> 2) * 4096, l16); }); };
    if (w >= 4) { __builtin_amdgcn_s_sleep(2); Run<30, 4, false>(ws + WS_WOF + (size_t)n8 * WOB + 71680, wo_slot(7), w - 4).all(l16); }
    stage1();
#pragma unroll
    for (int m = 0; m < 5; ++m) wbase[m] = (kh * 5 + (m + 3 * kh) % 5) * 1024 + l16;
    avoff = (g4 >> 1) * 1024 + (g4 & 1) * 512 + (16 * (tt & 1) + c16) * 16;
    const int tt64 = mt * 4 + 2 * th + (tt >> 1);
    const char* ab = ws + WS_OAF + (size_t)(tt64 * 40 + 20 * kh) * 1024;
    const u32 ew = 73728 + (tt * 5) * 1024 + l16;
    u32 xb = WS_X1 + (u32)(tt64 * 40 + 5 * n8) * 1024 + (g4 >> 1) * 512 + (16 * (tt & 1) + c16) * 16 + (g4 & 1) * 8 + kh * 3072;
    u32 sta = WS_STAT + (u32)((tt64 * 8 + n8) * 32 + 16 * (tt & 1) + c16) * 16 + kh * 8;
    u32 mo = L_MODS + kh * 192 + g4 * 16;
    PIN(xb); PIN(sta); PIN(mo); PIN(avoff);
    auto iss = [&](auto s_) LAM { constexpr int s = decltype(s_)::v; gld_sc1<(s & 1) * 2048>(a[s], ab + (s >> 1) * 4096, avoff); };
    u32x4 wf[3][5];
#pragma unroll
    for (int s = 0; s < 2; ++s)
#pragma unroll
      for (int m = 0; m < 5; ++m) wf[s][m] = lds128(wbase[m] + wo_slot(s));
#pragma unroll
    for (int m = 0; m < 5; ++m) c5[m] = f32x4{0.f, 0.f, 0.f, 0.f};
    if (w == 0) poll_ge(ws, C_O + mt * 256, 16u);
    BAR();
    STAMP(7); FST(5, 1);
    sfor<0, 5>(iss);
    sfor<0, 10>([&](auto s_) LAM {
      constexpr int s = decltype(s_)::v;
      WAITV(s == 0 ? 4 : s == 1 ? 6 : 9 - s);
      FST(5, 2 + s);
      if (s == 4) BAR();
      PIN(a[s]);
      if constexpr (s + 2 < 10) {
#pragma unroll
        for (int m = 0; m < 5; ++m) wf[(s + 2) % 3][m] = lds128(wbase[m] + wo_slot(s + 2));
      }
#pragma unroll
      for (int m = 0; m < 5; ++m) c5[m] = MFMA16(wf[s % 3][m], a[s], c5[m]);
      SB();
      if constexpr (s == 0) sfor<5, 8>(iss);
      if constexpr (s == 1) sfor<8, 10>(iss);
      SB();
    });
    STAMP(8); FST(5, 12);
    stf4(ew + ((3 + 3 * kh) % 5) * 1024, c5[3]);
    stf4(ew + ((4 + 3 * kh) % 5) * 1024, c5[4]);
    if (kh) stf4(ew, c5[2]);
    BAR();
    FST(5, 13);
    float xs[12];
#pragma unroll
    for (int m = 0; m < 3; ++m) PIN(xr[m]);
#pragma unroll
    for (int m = 0; m < 3; ++m) {
      if (m < 2 || kh == 0) {
        const f32x4 o = ldsf4(ew + kh * 3072 + m * 1024), ga = ldsf4(mo + m * 64), g2 = ldsf4(mo + 320 + m * 64);
        const u32 y01 = pk2(c5[m][0] + o[0], c5[m][1] + o[1]), y23 = pk2(c5[m][2] + o[2], c5[m][3] + o[3]);
        const u32 t01 = pk2(ga[0] * lo16(y01), ga[1] * hi16(y01)), t23 = pk2(ga[2] * lo16(y23), ga[3] * hi16(y23));
        x1k[m] = u32x2{pk2(lo16(xr[m].x) + lo16(t01), hi16(xr[m].x) + hi16(t01)), pk2(lo16(xr[m].y) + lo16(t23), hi16(xr[m].y) + hi16(t23))};
        xs[4 * m] = lo16(x1k[m].x); xs[4 * m + 1] = hi16(x1k[m].x); xs[4 * m + 2] = lo16(x1k[m].y); xs[4 * m + 3] = hi16(x1k[m].y);
        __builtin_amdgcn_raw_buffer_store_b64(u32x2{pk2(xs[4 * m] * g2[0], xs[4 * m + 1] * g2[1]), pk2(xs[4 * m + 2] * g2[2], xs[4 * m + 3] * g2[3])}, rs, xb + m * 1024, 0, 16);
      } else {
        x1k[m] = u32x2{0u, 0u};
        xs[8] = xs[9] = xs[10] = xs[11] = 0.f;
      }
    }
    float s = ((xs[0] + xs[1]) + (xs[2] + xs[3])) + ((xs[4] + xs[5]) + (xs[6] + xs[7])) + ((xs[8] + xs[9]) + (xs[10] + xs[11]));
    s = sum32(sum16(s));
    const float mean = s * (kh ? 1.f / 32.f : 1.f / 48.f);
    float q0 = 0.f, q1 = 0.f;
#pragma unroll
    for (int e = 0; e < 4; ++e) { const float d0 = xs[e] - mean, d1 = xs[4 + e] - mean; q0 = fma1(d0, d0, q0); q1 = fma1(d1, d1, q1); }
    if (kh == 0) {
#pragma unroll
      for (int e = 8; e < 12; ++e) { const float d = xs[e] - mean; q0 = fma1(d, d, q0); }
    }
    const float q = sum32(sum16(q0 + q1));
    if (g4 == 0) __builtin_amdgcn_raw_buffer_store_b64(u32x2{f2u(mean), f2u(q)}, rs, sta, 0, 16);
    FST(5, 14);
    BAR();
    STAMP(9); FST(5, 16);
  }
  {
    PHASE();
    const bool old4 = w < 4;
    const char* wgb = ws + WS_WGF + (size_t)j * WGB;
    const char* wdb = ws + WS_WDF + (size_t)n8 * WDB;
    u32x4 stv[4], csv[2];
    const char* ab = ws + WS_X1 + (size_t)((mt * 4 + tt) * 40 + 20 * kh) * 1024;
    const char* sb = ws + WS_STAT + (size_t)(mt * 4 + tt) * 4096;
    auto iss = [&](auto s_) LAM { constexpr int s = decltype(s_)::v; gld_sc1<(s & 3) * 1024>(a[s], ab + (s >> 2) * 4096, l16); };
    {
      const char* cb = ws + WS_CSP + (size_t)(j * 16 + 4 * (w >> 1) + (w & 1)) * 768;
      const u32 co = lane < 48 ? l16 : 0u;
      gld_sc1<0>(csv[0], cb, co); gld_sc1<1536>(csv[1], cb, co);
    }
#pragma unroll
    for (int m = 0; m < 18; ++m) { PIN(st[m]); st128((m < 10 ? (kh * 40 + tt * 10 + m) : (80 + kh * 32 + tt * 8 + m - 10)) * 1024 + l16, st[m]); }
    float c1 = 0.f, d2 = 0.f;
    u32x4 bcor = {0u, 0u, 0u, 0u};
    u32 sw = kh * 40960 + tt * 4096 + l16, sr = (kh ^ 1) * 40960 + tt * 4096 + l16;
    u32 actb = WS_ACT + (u32)((mt * 4 + tt) * 96 + 6 * j + kh) * 1024 + l16;
    const u32 so = h * 2048 + c32 * 16;
    PIN(actb);
    const auto rC = gu_run<1, 16, 20, 4>(wgb, w & 3);
    const auto rA = gu_run<2, 0, 8, 4>(wgb, w & 3);
    const auto rB = gu_run<2, 8, 20, 4>(wgb, w & 3);
    const Run<60, 8, true> rD(wdb, 0, w);
    const Run<90, 8, true> rS(wdb + 61440, 61440, w);
    gate(sb, (c32 >> 2) * 512 + ((c32 >> 1) & 1) * 256 + (c32 & 1) * 8);
    STAMP(10); FST(2, 1); FST(5, 17);
    sfor<0, 2>(iss);
    gld_sc1<0>(stv[0], sb, so); gld_sc1<512>(stv[1], sb, so); gld_sc1<1024>(stv[2], sb, so); gld_sc1<1536>(stv[3], sb, so);
    sfor<2, 6>(iss);
    f32x16 ag[2], au[2];
    u32x4 fg[3], fu[3], outv;
    u32 ub = 0;
    auto ldw = [&](auto g_) LAM {
      constexpr int g = decltype(g_)::v, pp = g / 20, i = g % 20;
      constexpr bool first = i == 0 || gu_unit(pp, 0, i) != gu_unit(pp, 0, i - 1) + 1;
      constexpr int i0 = [] { int x = i; while (x > 0 && gu_unit(pp, 0, x) == gu_unit(pp, 0, x - 1) + 1) --x; return x; }();
      if constexpr (first) ub = frow + (kh ? gu_unit(pp, 1, i) : gu_unit(pp, 0, i)) * 2048;
      fg[g % 3] = lds128(ub + (i - i0) * 2048); fu[g % 3] = lds128(ub + (i - i0) * 2048 + 1024);
    };
    auto xwrite = [&](int pp) LAM {
      const f32x16 &G = ag[pp & 1], &U = au[pp & 1];
      stf4(sw, f32x4{G[8], G[9], G[10], G[11]}); stf4(sw + 1024, f32x4{G[12], G[13], G[14], G[15]});
      stf4(sw + 2048, f32x4{U[8], U[9], U[10], U[11]}); stf4(sw + 3072, f32x4{U[12], U[13], U[14], U[15]});
    };
    f32x4 xo[4];
    auto xread = [&]() LAM {
#pragma unroll
      for (int u = 0; u < 4; ++u) xo[u] = ldsf4(sr + u * 1024);
    };
    auto xadd = [&](int pp, int q) LAM {
#pragma unroll
      for (int i = 0; i < 4; ++i) { ag[pp & 1][4 * q + i] += xo[q][i]; au[pp & 1][4 * q + i] += xo[2 + q][i]; }
    };
    auto corr = [&](int pp) LAM {
      ag[pp & 1] = MFMA32(lds128(L_T2 + pp * 1024 + rsw * 16), bcor, ag[pp & 1]);
      au[pp & 1] = MFMA32(lds128(L_T2 + pp * 1024 + 512 + rsw * 16), bcor, au[pp & 1]);
    };
    float eu[2], ee[2];
    auto epi = [&](int pp, int o, int ph) LAM {
#pragma unroll
      for (int i = 0; i < 2; ++i) {
        if (ph == 0) { ee[i] = __builtin_amdgcn_exp2f(ag[pp & 1][2 * o + i] * c1); eu[i] = ag[pp & 1][2 * o + i] * au[pp & 1][2 * o + i]; }
        if (ph == 1) ee[i] = __builtin_amdgcn_rcpf(fma1(ee[i], d2, d2));
      }
      if (ph == 2) outv[o] = pk2(eu[0] * ee[0], eu[1] * ee[1]);
    };
    auto ostore = [&](int pp) LAM { __builtin_amdgcn_raw_buffer_store_b128(outv, rs, actb + pp * 2048, 0, 16); asm volatile("" ::: "memory"); };
    BAR();
    ldw(IC<0>{}); ldw(IC<1>{});
    sfor<0, 60>([&](auto g_) LAM {
      constexpr int g = decltype(g_)::v, pp = g / 20, i = g % 20;
      if constexpr (g < 20) {
        WAITV(g == 0 ? 9 : g == 1 ? 12 : g == 2 ? 11 : g == 3 ? 14 : 19 - g);
        PIN(a[g]);
        ensure<(g & 3) * 1024>(a[g], ab + (g >> 2) * 4096, l16);
      }
      if (g == 32) WAITV(0);
      if (g == 43) WAITV(4);
      if ((g & 3) == 0) FST(2, 2 + g / 4);
      if (g == 12 || g == 22 || g == 32 || g == 43 || g == 50) BAR();
      if (g == 22 && CSEL != 4) { STAMP(16); CST(0, 18); }
      if (g == 43 && CSEL != 4) { STAMP(17); CST(0, 19); }
      if constexpr (g + 2 < 60) ldw(IC<g + 2>{});
      if (g == 22 || g == 43) xread();
      if (i == 0) {
        f32x16 z;
#pragma unroll
        for (int e = 0; e < 16; ++e) z[e] = 0.f;
        ag[pp & 1] = MFMA32(fg[g % 3], a[i], z); au[pp & 1] = MFMA32(fu[g % 3], a[i], z);
      } else {
        ag[pp & 1] = MFMA32(fg[g % 3], a[i], ag[pp & 1]); au[pp & 1] = MFMA32(fu[g % 3], a[i], au[pp & 1]);
      }
      if (g == 4) {
#pragma unroll
        for (int u = 0; u < 4; ++u) PIN(stv[u]);
        ensure<0>(stv[0], sb, so); ensure<512>(stv[1], sb, so); ensure<1024>(stv[2], sb, so); ensure<1536>(stv[3], sb, so);
        PIN(csv[0]); PIN(csv[1]);
        f32x4 sm;
#pragma unroll
        for (int e = 0; e < 4; ++e) sm[e] = u2f(csv[0][e]) + u2f(csv[1][e]);
        if (lane < 48) stf4(L_GS + (w >> 1) * 1536 + (w & 1) * 768 + l16, sm);
        float s = 0.f;
#pragma unroll
        for (int u = 0; u < 4; ++u) s += 48.f * u2f(stv[u][0]) + 32.f * u2f(stv[u][2]);
        const float mean = sum32(s) * (1.f / 640.f);
        float q = 0.f;
#pragma unroll
        for (int u = 0; u < 4; ++u) {
          const float d0 = u2f(stv[u][0]) - mean, d1 = u2f(stv[u][2]) - mean;
          q += u2f(stv[u][1]) + u2f(stv[u][3]) + 48.f * d0 * d0 + 32.f * d1 * d1;
        }
        q = sum32(q);
        const float den = rbf(__builtin_amdgcn_sqrtf(rbf(rbf(q * (1.f / 640.f)) + 1e-5f)));
        c1 = -1.4426950408889634f * __builtin_amdgcn_rcpf(den); d2 = den * den;
        const u32 nm = pk2(-mean, -mean), dd = pk2(den, den);
        bcor = u32x4{h ? 0u : nm, h ? 0u : ((nm & 0xffffu) | (dd & 0xffff0000u)), h ? 0u : dd, 0u};
      }
      if (g == 12 && tid < 192) {
        const u32 t4 = L_GS + (tid >> 3) * 64 + (tid & 7) * 4;
        const float cs = (ldsf(t4) + ldsf(t4 + 1536)) + (ldsf(t4 + 3072) + ldsf(t4 + 4608));
        const float bi = (ldsf(t4 + 32) + ldsf(t4 + 1568)) + (ldsf(t4 + 3104) + ldsf(t4 + 4640));
        const u32 h0 = pk2(cs, bi);
        const float cr = cs - lo16(h0), br = bi - hi16(h0);
        const u32 h1 = pk2(cr, br), h2 = pk2(cr - lo16(h1), br - hi16(h1));
        const int gu = tid >= 96, nn = tid - 96 * gu;
        st128(L_T2 + (nn >> 5) * 1024 + gu * 512 + (nn & 31) * 16,
              u32x4{__builtin_amdgcn_perm(h1, h0, 0x05040100u), __builtin_amdgcn_perm(h0, h2, 0x07060100u), __builtin_amdgcn_perm(h2, h1, 0x07060302u), 0u});
      }
      if (g == 21 || g == 41) xwrite(pp - 1);
      if (g == 22 || g == 42) corr(pp - 1);
      if (g == 24 || g == 25) xadd(0, g - 24);
      if (g >= 28 && g <= 37 && (g - 28) % 3 == 0) epi(0, (g - 28) / 3, 2);
      if (g >= 26 && g < 37 && (g - 26) % 3 < 2) epi(0, (g - 26) / 3, (g - 26) % 3);
      if (g == 44 || g == 45) xadd(1, g - 44);
      if (g >= 48 && g <= 57 && (g - 48) % 3 == 0) epi(1, (g - 48) / 3, 2);
      if (g >= 46 && g < 57 && (g - 46) % 3 < 2) epi(1, (g - 46) / 3, (g - 46) % 3);
      SGB(0x100, 6, 0); SGB(0x008, 1, 0); SGB(0x002, 4, 0); SGB(0x008, 1, 0);
      SB();
      if constexpr (g < 3) sfor<6 + 4 * g, 10 + 4 * g>(iss);
      if constexpr (g == 3) sfor<18, 20>(iss);
      if (g == 40) ostore(0);
      if constexpr (g >= 40 && g < 52) gld_sc1<((g - 40) & 3) * 1024>(a[g - 40], rS.src + ((g - 40) >> 2) * 4096, l16);
      if constexpr (g >= 50 && g < 58) rD.template go<g - 50>(l16);
      if (g == 57) ostore(1);
      if constexpr ((g >= 16 && g < 18) || (g >= 22 && g < 26) || (g >= 32 && g < 38)) {
        if (old4) {
          if constexpr (g < 18) { rC.template go<2 * (g - 16)>(l16); rC.template go<2 * (g - 16) + 1>(l16); }
          else if constexpr (g < 26) { rA.template go<2 * (g - 22)>(l16); rA.template go<2 * (g - 22) + 1>(l16); }
          else { rB.template go<2 * (g - 32)>(l16); rB.template go<2 * (g - 32) + 1>(l16); }
        }
      }
      SB();
    });
    { const u32 lo_ = 65536 + tt * 4096, hi_ = (tt < 2 ? 106496 : 131072) + tt * 4096; sw = (kh ? hi_ : lo_) + l16; sr = (kh ? lo_ : hi_) + l16; }
    xwrite(2);
    corr(2);
    BAR();
    STAMP(11); FST(2, 17);
    xread(); xadd(2, 0); xadd(2, 1);
#pragma unroll
    for (int o = 0; o < 4; ++o)
#pragma unroll
      for (int ph = 0; ph < 3; ++ph) epi(2, o, ph);
    ostore(2);
    WAITV(1);
    BAR();
#pragma unroll
    for (int m = 0; m < 12; ++m) { PIN(a[m]); st128(rS.lds + m * 1024 + l16, a[m]); }
    STAMP(12);
  }
  {
    PHASE();
    const bool old4 = w < 4;
    const char* wdb = ws + WS_WDF + (size_t)n8 * WDB;
    const int tt64 = mt * 4 + 2 * th + (tt >> 1);
    const char* ab = ws + WS_ACT + (size_t)(tt64 * 96 + 48 * kh) * 1024;
    auto iss = [&](auto s_) LAM { constexpr int s = decltype(s_)::v; gld_sc1<(s & 1) * 2048>(a[s], ab + (s >> 1) * 4096, avoff); };
#pragma unroll
    for (int m = 0; m < 5; ++m) c5[m] = f32x4{0.f, 0.f, 0.f, 0.f};
    const Run<50, 4, true> rE(wdb + 153600, 0, w & 3);
    const Run<40, 4, true> rF(wdb + 204800, 51200, w & 3);
    const u32 ew = 92160 + (tt * 5) * 1024 + l16;
    u32 oo = (u32)(128 * mt + 64 * th + 16 * tt + c16) * 1280 + (80 * n8 + 48 * kh + 4 * g4) * 2;
    u32 mo = L_MODS + 640 + kh * 192 + g4 * 16;
    PIN(oo); PIN(mo);
    gate(ab, ((c16 >> 1) * 6 + 4 + (c16 & 1)) * 1024 + (tt & 1) * 256);
    STAMP(13); CST(2, 18); FST(1, 1);
    sfor<0, 6>(iss);
    BAR();
    u32x4 wf[3][5];
#pragma unroll
    for (int u = 0; u < 2; ++u)
#pragma unroll
      for (int m = 0; m < 5; ++m) wf[u][m] = lds128(wbase[m] + wd_slot(u));
    sfor<0, 24>([&](auto s_) LAM {
      constexpr int s = decltype(s_)::v;
      WAITV(s == 13 ? 8 : s == 18 ? 0 : s < 6 ? 5 + 2 * s : s > 18 ? 0 : 23 - s);
      if ((s & 1) == 0) FST(1, 2 + s / 2);
      if (s == 5 || s == 9 || s == 13 || s == 18) BAR();
      PIN(a[s]);
      ensure<(s & 1) * 2048>(a[s], ab + (s >> 1) * 4096, avoff);
      if constexpr (s + 2 < 24) {
#pragma unroll
        for (int m = 0; m < 5; ++m) wf[(s + 2) % 3][m] = lds128(wbase[m] + wd_slot(s + 2));
      }
#pragma unroll
      for (int m = 0; m < 5; ++m) c5[m] = MFMA16(wf[s % 3][m], a[s], c5[m]);
      SB();
      if constexpr (s < 6) sfor<6 + 3 * s, 9 + 3 * s>(iss);
      if constexpr (s >= 5 && s < 14) {
        if (old4) {
          if constexpr (s < 9) { sfor<0, 4>([&](auto u_) LAM { rE.template go<4 * (s - 5) + decltype(u_)::v>(l16); }); }
          else { rF.template go<2 * (s - 9)>(l16); rF.template go<2 * (s - 9) + 1>(l16); }
        }
      }
      SB();
    });
    STAMP(14); CST(2, 19); FST(1, 14);
    stf4(ew + ((3 + 3 * kh) % 5) * 1024, c5[3]);
    stf4(ew + ((4 + 3 * kh) % 5) * 1024, c5[4]);
    if (kh) stf4(ew, c5[2]);
    BAR();
#pragma unroll
    for (int m = 0; m < 3; ++m)
      if (m < 2 || kh == 0) {
        const f32x4 o = ldsf4(ew + kh * 3072 + m * 1024), gb = ldsf4(mo + m * 64);
        const u32 y01 = pk2(c5[m][0] + o[0], c5[m][1] + o[1]), y23 = pk2(c5[m][2] + o[2], c5[m][3] + o[3]);
        const u32 t01 = pk2(gb[0] * lo16(y01), gb[1] * hi16(y01)), t23 = pk2(gb[2] * lo16(y23), gb[3] * hi16(y23));
        const u32x2 r = {pk2(lo16(x1k[m].x) + lo16(t01), hi16(x1k[m].x) + hi16(t01)), pk2(lo16(x1k[m].y) + lo16(t23), hi16(x1k[m].y) + hi16(t23))};
        *(GAS u32x2*)((GAS char*)p.out + oo + m * 32) = r;
      }
  }
}
}

extern "C" void kernel_launch(void* const* d_in, const int* in_sizes, int n_in, void* d_out, int out_size, void* d_ws, size_t ws_size,
                              hipStream_t stream) {
  (void)in_sizes; (void)n_in; (void)out_size; (void)ws_size;
  (void)hipFuncSetAttribute((const void*)k2, hipFuncAttributeMaxDynamicSharedMemorySize, LDS_BYTES);
  P1 q;
  q.x = (const u16*)d_in[0]; q.wqkv = (const u16*)d_in[1]; q.wo = (const u16*)d_in[2];
  q.cvec = (const u16*)d_in[7]; q.wmod = (const u16*)d_in[8]; q.ws = (char*)d_ws;
  k1<<<K1_BLOCKS, 256, 0, stream>>>(q);
  P2 p;
  p.x = (const u16*)d_in[0]; p.wg = (const u16*)d_in[4]; p.wu = (const u16*)d_in[5]; p.wd = (const u16*)d_in[6];
  p.out = (u16*)d_out; p.ws = (char*)d_ws;
  k2<<<256, 512, LDS_BYTES, stream>>>(p);
}
```

```cpp
#include <hip/hip_runtime.h>
#include <hip/hip_bf16.h>
#include <stdint.h>

#define GAS __attribute__((address_space(1)))
#define LAS __attribute__((address_space(3)))
#if defined(__HIP_DEVICE_COMPILE__)
#define NOPK __attribute__((target("no-packed-fp32-ops")))
#else
#define NOPK
#endif
#define DI __device__ __forceinline__ NOPK
#define LAM __attribute__((always_inline)) NOPK
typedef unsigned int u32;
typedef unsigned short u16;
typedef u32 u32x4 __attribute__((ext_vector_type(4)));
typedef u32 u32x2 __attribute__((ext_vector_type(2)));
typedef float f32x2 __attribute__((ext_vector_type(2)));
typedef float f32x4 __attribute__((ext_vector_type(4)));
typedef float f32x16 __attribute__((ext_vector_type(16)));
typedef __bf16 bf16x2 __attribute__((ext_vector_type(2)));
typedef __bf16 bf16x4 __attribute__((ext_vector_type(4)));
typedef __bf16 bf16x8 __attribute__((ext_vector_type(8)));
typedef GAS u32 gu32;
typedef _Float16 f16x8 __attribute__((ext_vector_type(8)));
typedef _Float16 f16x2 __attribute__((ext_vector_type(2)));

#define STAMPS 0

namespace {
constexpr int S = 2048, D = 640, F = 1536;
constexpr u32 WS_KVF = 0;
constexpr u32 WS_CNT = 32768;
constexpr u32 WS_MODP = 65536;
constexpr u32 WS_CSP = WS_MODP + 61440;
constexpr u32 WS_STAT = WS_CSP + 16 * 16 * 1536;
constexpr u32 WS_N1 = WS_STAT + 64 * 8 * 32 * 16;
constexpr u32 WS_OAF = WS_N1 + 64 * 40 * 1024;
constexpr u32 WS_X1 = WS_OAF + 64 * 40 * 1024;
constexpr u32 WS_ACT = WS_X1 + 64 * 40 * 1024;
constexpr u32 KVB = 24576;
constexpr u32 WS_KVX = WS_ACT + 64 * 96 * 1024;
constexpr u32 WQB = 153600;
constexpr u32 WS_WQF = WS_KVX + 256 * KVB;
constexpr u32 WOB = 102400, WGB = 245760, WDB = 245760;
constexpr u32 WS_WOF = WS_WQF + 16 * WQB;
constexpr u32 WS_WGF = WS_WOF + 8 * WOB;
constexpr u32 WS_WDF = WS_WGF + 16 * WGB;
constexpr u32 WS_DBG = WS_WDF + 8 * WDB;
constexpr u32 WS_DBG1 = WS_DBG + 256 * 32 * 8;
constexpr u32 WS_END = WS_DBG1 + 1024 * 16;
constexpr u32 L_MISC = 153600;
constexpr u32 L_GS = L_MISC;
constexpr u32 L_MODS = L_MISC + 6144;
constexpr u32 L_T2 = L_MISC + 7168;
constexpr u32 L_FLAG = L_MISC + 9728;
constexpr u32 LDS_BYTES = 163840;
constexpr u32 L_K = 0, L_V = 30720;
constexpr float QSCALE = 0.22360679774997896f * 1.4426950408889634f;
constexpr int K1_BLOCKS = 488;
#define ATT_LIMIT 1.0e18f
constexpr int K2_LINES = 300;
constexpr u32 SPIN_CAP = 1u << 20;

DI float u2f(u32 v) { return __builtin_bit_cast(float, v); }
DI u32 f2u(float v) { return __builtin_bit_cast(u32, v); }
DI int tidx() { return (int)__builtin_amdgcn_workitem_id_x(); }
DI int bidx() { return (int)__builtin_amdgcn_workgroup_id_x(); }
DI float bf2f(u32 v) { return u2f(v << 16); }
DI float lo16(u32 v) { return u2f(v << 16); }
DI float hi16(u32 v) { return u2f(v & 0xffff0000u); }
DI u32 pk2(float a, float b) { f32x2 v = {a, b}; return __builtin_bit_cast(u32, __builtin_convertvector(v, bf16x2)); }
DI u32 pkh2(float a, float b) { f32x2 v = {a, b}; return __builtin_bit_cast(u32, __builtin_convertvector(v, f16x2)); }
DI float hlo(u32 v) { return (float)__builtin_bit_cast(f16x2, v).x; }
DI float hhi(u32 v) { return (float)__builtin_bit_cast(f16x2, v).y; }
DI float rbf(float a) { return lo16(pk2(a, a)); }
DI float fma1(float a, float b, float c) { return __builtin_fmaf(a, b, c); }
DI float mul1(float a, float b) { return a * b; }
DI float add1(float a, float b) { return a + b; }
DI float max3(float a, float b, float c) { return __builtin_fmaxf(__builtin_fmaxf(a, b), c); }
DI float xor32f(float v) {
  auto r = __builtin_amdgcn_permlane32_swap(f2u(v), f2u(v), false, false);
  return u2f((tidx() & 32) ? r[0] : r[1]);
}
DI float sum32(float v) { auto r = __builtin_amdgcn_permlane32_swap(f2u(v), f2u(v), false, false); return u2f(r[0]) + u2f(r[1]); }
DI float sum16(float v) { auto r = __builtin_amdgcn_permlane16_swap(f2u(v), f2u(v), false, false); return u2f(r[0]) + u2f(r[1]); }
DI float max32(float v) { auto r = __builtin_amdgcn_permlane32_swap(f2u(v), f2u(v), false, false); return __builtin_fmaxf(u2f(r[0]), u2f(r[1])); }
template <int CTRL> DI float dppf(float v) { return u2f(__builtin_amdgcn_update_dpp(0u, f2u(v), CTRL, 0xf, 0xf, true)); }

#define MFMA32(a, b, c) __builtin_amdgcn_mfma_f32_32x32x16_bf16(__builtin_bit_cast(bf16x8, a), __builtin_bit_cast(bf16x8, b), c, 0, 0, 0)
#define MFMA32H(a, b, c) __builtin_amdgcn_mfma_f32_32x32x16_f16(__builtin_bit_cast(f16x8, a), __builtin_bit_cast(f16x8, b), c, 0, 0, 0)
#define MFMA16(a, b, c) __builtin_amdgcn_mfma_f32_16x16x32_bf16(__builtin_bit_cast(bf16x8, a), __builtin_bit_cast(bf16x8, b), c, 0, 0, 0)
#define SGB(m, n, id) __builtin_amdgcn_sched_group_barrier(m, n, id)
#define SB() __builtin_amdgcn_sched_barrier(0)
template <bool B> struct BoolC { static constexpr bool v = B; };
template <int I> struct IC { static constexpr int v = I; };
template <int I, int N, class Fn> DI void sfor(Fn&& f) { if constexpr (I < N) { f(IC<I>{}); sfor<I + 1, N>(f); } }
#define PIN(x) asm volatile("" : "+v"(x))
#define WAITV(n) asm volatile("s_waitcnt vmcnt(%0)" ::"i"(n))
#define WAITVM(n) asm volatile("s_waitcnt vmcnt(%0)" ::"i"(n) : "memory")
#define BAR() asm volatile("s_waitcnt lgkmcnt(0)\n\ts_barrier" ::: "memory")
#define RLX_AGENT __ATOMIC_RELAXED, __HIP_MEMORY_SCOPE_AGENT

DI u32x4 lds128(u32 a) { return *(const LAS u32x4*)(size_t)a; }
DI f32x4 ldsf4(u32 a) { return *(const LAS f32x4*)(size_t)a; }
DI u32x2 lds64(u32 a) { return *(const LAS u32x2*)(size_t)a; }
DI float ldsf(u32 a) { return *(const LAS float*)(size_t)a; }
DI void st128(u32 a, u32x4 v) { *(LAS u32x4*)(size_t)a = v; }
DI void stf4(u32 a, f32x4 v) { *(LAS f32x4*)(size_t)a = v; }
DI void st64(u32 a, u32x2 v) { *(LAS u32x2*)(size_t)a = v; }
DI u32x2 ldstr(u32 a) { return __builtin_bit_cast(u32x2, __builtin_amdgcn_ds_read_tr16_b64_v4bf16((LAS bf16x4*)(size_t)a)); }
DI u32x4 ldstr2(u32 a, u32 b) { u32x2 lo = ldstr(a), hi = ldstr(b); return u32x4{lo.x, lo.y, hi.x, hi.y}; }

template <int IMM, bool SC1> DI void dmai(const char* sbase, u32 voff, u32 lds) {
  if constexpr (SC1) asm volatile("s_mov_b32 m0, %2\n\ts_nop 0\n\tglobal_load_lds_dwordx4 %0, %1 offset:%3 sc1" ::"v"(voff), "s"(sbase), "s"(lds), "i"(IMM));
  else asm volatile("s_mov_b32 m0, %2\n\ts_nop 0\n\tglobal_load_lds_dwordx4 %0, %1 offset:%3" ::"v"(voff), "s"(sbase), "s"(lds), "i"(IMM));
}
DI void dma(const char* sbase, u32 voff, u32 lds) { dmai<0, false>(sbase, voff, lds); }
DI void gld32(u32& r, const char* sbase, u32 voff) { asm volatile("global_load_dword %0, %1, %2" : "=v"(r) : "v"(voff), "s"(sbase)); }
DI void gld32_sc1(u32& r, const char* sbase, u32 voff) { asm volatile("global_load_dword %0, %1, %2 sc1" : "=v"(r) : "v"(voff), "s"(sbase)); }
template <int IMM> DI void gld_sc1(u32x4& r, const char* sbase, u32 voff) {
  asm volatile("global_load_dwordx4 %0, %1, %2 offset:%3 sc1" : "=v"(r) : "v"(voff), "s"(sbase), "i"(IMM));
}
template <int IMM> DI void gld(u32x4& r, const char* sbase, u32 voff) {
  asm volatile("global_load_dwordx4 %0, %1, %2 offset:%3" : "=v"(r) : "v"(voff), "s"(sbase), "i"(IMM));
}
DI void gld64(u32x2& r, const char* sbase, u32 voff) {
  asm volatile("global_load_dwordx2 %0, %1, %2" : "=v"(r) : "v"(voff), "s"(sbase));
}
DI void poll_ge(const char* ws, u32 off, u32 want) {
  gu32* p = (gu32*)(ws + off);
  for (u32 spins = 0; spins < SPIN_CAP; ++spins) {
    if ((u32)__builtin_amdgcn_readfirstlane(__hip_atomic_load(p, RLX_AGENT)) >= want) break;
    __builtin_amdgcn_s_sleep(1);
  }
  __builtin_amdgcn_fence(__ATOMIC_ACQUIRE, "wavefront");
}
DI void signal(const char* ws, u32 off) { __hip_atomic_fetch_add((gu32*)(ws + off), 1u, RLX_AGENT); }

struct P1 {
  const u16 *x, *wqkv, *wo, *cvec, *wmod;
  char* ws;
};
#define K1AUX 0
__global__ void __launch_bounds__(256) NOPK k1(P1 p) {
  __shared__ float red[32 * 64 + 4 * 64 + 320];
  const int b = bidx(), t = tidx();
  char* ws = p.ws;
  const __amdgpu_buffer_rsrc_t rs = __builtin_amdgcn_make_buffer_rsrc(p.ws, 0, WS_END, 0x00020000);
  if (b >= 408 && b < 424) {
    unsigned long long pc;
    asm volatile("s_getpc_b64 s[40:41]\n\ts_add_u32 s40, s40, _ZN12_GLOBAL__N_12k2ENS_2P2E@rel32@lo+4\n\ts_addc_u32 s41, s41, _ZN12_GLOBAL__N_12k2ENS_2P2E@rel32@hi+12\n\ts_mov_b64 %0, s[40:41]"
                 : "=s"(pc) : : "scc", "s40", "s41");
    const int o = ((b - 408) >> 3) * 256 + t;
    if (o < K2_LINES) { u32 v_ = *(const GAS u32*)(pc + (size_t)o * 128); asm volatile("" ::"v"(v_)); }
  }
  if (b < 120) {
    const int ch = t & 7, r = t >> 3, col0 = 64 * (b >> 1), row0 = 320 * (b & 1);
    const u32x4 cv = *(const u32x4*)(p.cvec + row0 + 8 * (t < 40 ? t : 39));
    u32x4 wv[10];
#pragma unroll
    for (int i = 0; i < 10; ++i) wv[i] = *(const u32x4*)(p.wmod + (size_t)(row0 + r + 32 * i) * 3840 + col0 + 8 * ch);
    if (t < 40) {
#pragma unroll
      for (int e = 0; e < 8; ++e) {
        const float c = (e & 1) ? hi16(cv[e >> 1]) : lo16(cv[e >> 1]);
        red[2304 + 8 * t + e] = rbf(c * __builtin_amdgcn_rcpf(1.f + __builtin_amdgcn_exp2f(-1.4426950408889634f * c)));
      }
    }
    BAR();
    float acc[8] = {0, 0, 0, 0, 0, 0, 0, 0};
#pragma unroll
    for (int i = 0; i < 10; ++i) {
      const float s = red[2304 + r + 32 * i];
#pragma unroll
      for (int e = 0; e < 4; ++e) { acc[2 * e] = fma1(s, lo16(wv[i][e]), acc[2 * e]); acc[2 * e + 1] = fma1(s, hi16(wv[i][e]), acc[2 * e + 1]); }
    }
    *(f32x4*)&red[r * 64 + 8 * ch] = f32x4{acc[0], acc[1], acc[2], acc[3]};
    *(f32x4*)&red[r * 64 + 8 * ch + 4] = f32x4{acc[4], acc[5], acc[6], acc[7]};
    BAR();
    { const int c = t & 63, part = t >> 6; float s = 0;
#pragma unroll
      for (int i = 0; i < 8; ++i) s += red[(part * 8 + i) * 64 + c];
      red[2048 + part * 64 + c] = s; }
    BAR();
    if (t < 64) {
      const float s = (red[2048 + t] + red[2112 + t]) + (red[2176 + t] + red[2240 + t]);
      __builtin_amdgcn_raw_buffer_store_b32(f2u(s), rs, WS_MODP + ((b & 1) * 3840 + col0 + t) * 4, 0, K1AUX);
    }
    if (b == 0) {
      *(u32*)(ws + WS_KVF + t * 128) = 0u;
      if (t < 72) *(u32*)(ws + WS_CNT + t * 256) = 0u;
    }
  } else if (b < 248) {
    const int hb = b - 120, c = t & 15, tok = 16 * (hb & 1) + (t >> 4);
    const u16* row = p.x + (size_t)(32 * (hb >> 1) + tok) * D;
    u32x4 v[5];
#pragma unroll
    for (int i = 0; i < 5; ++i) v[i] = *(const u32x4*)(row + 8 * (c + 16 * i));
    float s = 0;
#pragma unroll
    for (int i = 0; i < 5; ++i)
#pragma unroll
      for (int e = 0; e < 4; ++e) s += lo16(v[i][e]) + hi16(v[i][e]);
    s += dppf<0xB1>(s); s += dppf<0x4E>(s); s += dppf<0x141>(s); s += dppf<0x140>(s);
    const float mean = s * (1.f / 640.f);
    float q = 0;
#pragma unroll
    for (int i = 0; i < 5; ++i)
#pragma unroll
      for (int e = 0; e < 4; ++e) { const float a = lo16(v[i][e]) - mean, bb = hi16(v[i][e]) - mean; q = fma1(a, a, q); q = fma1(bb, bb, q); }
    q += dppf<0xB1>(q); q += dppf<0x4E>(q); q += dppf<0x141>(q); q += dppf<0x140>(q);
    const float mb = rbf(mean), den = rbf(__builtin_amdgcn_sqrtf(rbf(rbf(q * (1.f / 640.f)) + 1e-5f))), rr = __builtin_amdgcn_rcpf(den);
    const u32 dst = WS_N1 + (u32)(hb >> 1) * 40960 + tok * 16;
#pragma unroll
    for (int i = 0; i < 5; ++i) {
      u32x4 o;
#pragma unroll
      for (int e = 0; e < 4; ++e) {
        const u32 d = pk2(lo16(v[i][e]) - mb, hi16(v[i][e]) - mb);
        const u32 n = pk2(lo16(d) * rr, hi16(d) * rr);
        o[e] = pkh2(lo16(n), hi16(n));
      }
      __builtin_amdgcn_raw_buffer_store_b128(o, rs, dst + (c + 16 * i) * 512, 0, K1AUX);
    }
  } else {
    const u16* src; int stride; u32 off0, off1; bool act = true;
    if (b < 408) {
      const int idx = b - 248, ks = idx >> 2, jq = idx & 3, tc = t < 240 ? t : 239;
      act = t < 240;
      const int seg = tc / 80, cp = tc - 80 * seg;
      src = p.wqkv + (size_t)(16 * ks) * 1920 + 640 * seg + 160 * jq + 2 * cp; stride = 1920;
      const int jj = (2 * cp) / 40, within = 2 * cp - 40 * jj, j = 4 * jq + jj;
      const int n = (seg == 1 ? 0 : seg == 2 ? 40 : 80) + within, nb = n >> 5, r = n & 31;
      off0 = WS_WQF + j * WQB + (nb < 3 ? ((nb * 40 + ks) * 64 + r) * 16 : 122880 + ks * 768 + r * 16);
      off1 = off0 + (nb < 3 ? 512 : 384);
    } else {
      const int idx = b - 408, k16 = idx >> 1, tc = t < 160 ? t : 159, n = (idx & 1) * 320 + 2 * tc;
      act = t < 160;
      src = p.wo + (size_t)(16 * k16) * 640 + n; stride = 640;
      const int kh = k16 >= 20, kk = k16 - 20 * kh, st = kk >> 1, H = kk & 1;
      const int ntg = n >> 4, n8 = ntg / 5, nt = ntg - 5 * n8;
      off0 = WS_WOF + n8 * WOB + (st * 10 + kh * 5 + nt) * 1024 + (2 * H * 16 + (n & 15)) * 16;
      off1 = off0 + 256;
    }
    u32 v[16];
#pragma unroll
    for (int i = 0; i < 16; ++i) v[i] = *(const u32*)(src + (size_t)i * stride);
    if (b < 408) {
#pragma unroll
      for (int i = 0; i < 16; ++i) v[i] = pkh2(lo16(v[i]) * 64.f, hi16(v[i]) * 64.f);
    }
    if (act) {
#pragma unroll
      for (int h = 0; h < 2; ++h) {
        u32x4 a, bq;
#pragma unroll
        for (int e = 0; e < 4; ++e) {
          a[e] = __builtin_amdgcn_perm(v[8 * h + 2 * e + 1], v[8 * h + 2 * e], 0x05040100u);
          bq[e] = __builtin_amdgcn_perm(v[8 * h + 2 * e + 1], v[8 * h + 2 * e], 0x07060302u);
        }
        __builtin_amdgcn_raw_buffer_store_b128(a, rs, h ? off1 : off0, 0, K1AUX);
        __builtin_amdgcn_raw_buffer_store_b128(bq, rs, (h ? off1 : off0) + 16, 0, K1AUX);
      }
    }
  }
}

struct P2 {
  const u16 *x, *wg, *wu, *wd;
  u16* out;
  char* ws;
};
#define MAP 0
constexpr u32 C_O = WS_CNT, C_RG = WS_CNT + 48 * 256, C_RD = WS_CNT + 64 * 256;
#define OPQ(x) asm volatile("" : "+s"(x))

DI constexpr int gu_unit(int p, int kh, int i) {
  return p == 0 ? 20 * kh + i
       : p == 1 ? (i < 16 ? 40 + 16 * kh + i : 8 + 20 * kh + (i - 16))
                : (i < 8 ? 12 + 20 * kh + i : 40 + 16 * kh + (i - 8));
}
DI constexpr u32 wo_slot(int s) { return s < 7 ? (72 + 10 * s) * 1024 : (s - 7) * 10240; }
DI constexpr u32 wd_slot(int s) { return (s < 15 ? s : s - 15) * 10240; }
template <int N, int NW, bool SC1> struct Run {
  static constexpr int C = (N + NW - 1) / NW;
  const char* src; u32 lds;
  DI Run(const char* s, u32 l, int x) { int o = x * C; o = (o < N - C ? o : N - C) * 1024; src = s + o; lds = l + o; }
  template <int M> DI void go(u32 l16) const { if constexpr (M < C) dmai<(M & 3) * 1024, SC1>(src + (M >> 2) * 4096, l16, lds + (M >> 2) * 4096); }
  DI void all(u32 l16) const { sfor<0, C>([&](auto m_) LAM { go<decltype(m_)::v>(l16); }); }
};
template <int P, int I0, int I1, int NW> DI auto gu_run(const char* blob, int x) {
  const int k = x >= NW / 2;
  return Run<2 * (I1 - I0), NW / 2, true>(blob + ((P * 2 + k) * 20 + I0) * 2048, (k ? gu_unit(P, 1, I0) : gu_unit(P, 0, I0)) * 2048, x - k * (NW / 2));
}
DI u32 pkfma16(u32 x, u32 y, u32 z) { return __builtin_bit_cast(u32, __builtin_elementwise_fma(__builtin_bit_cast(f16x2, x), __builtin_bit_cast(f16x2, y), __builtin_bit_cast(f16x2, z))); }
DI void gld4_if(int cond, u32x4& r0, u32x4& r1, u32x4& r2, u32x4& r3, const char* sbase, u32 voff) {
  asm volatile("s_cmp_eq_u32 %6, 0\n\ts_cbranch_scc1 .Lskip%=\n\tglobal_load_dwordx4 %0, %4, %5\n\tglobal_load_dwordx4 %1, %4, %5 offset:1024\n\t"
               "global_load_dwordx4 %2, %4, %5 offset:2048\n\tglobal_load_dwordx4 %3, %4, %5 offset:3072\n.Lskip%=:"
               : "+v"(r0), "+v"(r1), "+v"(r2), "+v"(r3) : "v"(voff), "s"(sbase), "s"(cond) : "scc");
}
#define NRS 12
DI constexpr int dn_nA(int s) { return s < 3 ? 3 : s == 8 || s == 10 || s == 13 ? 3 : 0; }
DI constexpr int dn_nD(int s) { return s >= 5 && s < 8 ? 4 : s == 8 ? 1 : s >= 9 && s < 14 ? 2 : 0; }
DI constexpr int dn_cumA(int s) { int c = 6; for (int u = 0; u < s; ++u) c += dn_nA(u); return c; }
DI constexpr int dn_young(int s, bool old, int a_idx, int d_step) {
  int pos = 0, tgt = -1, na = 6;
  if (a_idx >= 0 && a_idx < 6) tgt = a_idx;
  pos = 6;
  for (int u = 0; u < s; ++u) {
    if (old) { pos += dn_nD(u); if (u == d_step) tgt = pos - 1; }
    for (int q = 0; q < dn_nA(u); ++q) { if (na == a_idx) tgt = pos; ++na; ++pos; }
  }
  return tgt < 0 ? 63 : pos - 1 - tgt;
}
DI constexpr int dn_wait(int s, bool old) {
  const int x = dn_young(s, old, s, -1), y = s == 13 ? dn_young(s, old, -1, 8) : s == 18 ? dn_young(s, old, -1, 13) : 63;
  return x < y ? x : y;
}
#define OWARM 0
DI bool has_sent(const u32x4& v) {
  const u32 m = __builtin_elementwise_max(__builtin_elementwise_max(v.x, v.y), __builtin_elementwise_max(v.z, v.w));
  return __builtin_amdgcn_ballot_w64(m == 0xffffffffu) != 0ull;
}
template <int IMM> DI void ensure(u32x4& v, const char* sbase, u32 voff) {
  if (__builtin_expect(has_sent(v), 0)) {
    for (u32 spins = 0; spins < SPIN_CAP; ++spins) {
      gld_sc1<IMM>(v, sbase, voff); WAITV(0); PIN(v);
      if (!has_sent(v)) break;
    }
  }
}
DI void gate(const char* sbase, u32 voff) {
  for (u32 spins = 0; spins < SPIN_CAP; ++spins) {
    u32 v;
    gld32_sc1(v, sbase, voff); WAITV(0); PIN(v);
    if (__builtin_amdgcn_ballot_w64(v == 0xffffffffu) == 0ull) break;
  }
}

__global__ void __launch_bounds__(512, 2) NOPK k2(P2 p) {
  const int tid = tidx(), lane = tid & 63;
  int w = __builtin_amdgcn_readfirstlane(tid >> 6);
  const int b = bidx();
  int mt = MAP == 0 ? 4 * ((b & 7) >> 1) + (b >> 6) : (b & 7) * 2 + (b >> 7), j = MAP == 0 ? 8 * (b & 1) + ((b >> 3) & 7) : (b >> 3) & 15;
  const int c32 = lane & 31, h = lane >> 5;
  const char* ws = p.ws;
  const __amdgpu_buffer_rsrc_t rs = __builtin_amdgcn_make_buffer_rsrc(p.ws, 0, WS_END, 0x00020000);
  const u32 l16 = lane * 16;
#define PHASE() OPQ(w); OPQ(mt); OPQ(j); OPQ(ws); const int tt = w & 3, kh = w >> 2, th = j & 1, n8 = j >> 1; (void)tt; (void)kh; (void)th; (void)n8
#define FINE 0
#define CSEL 0
#define FST(k, i) do {} while (0)
#define STAMP(i) do {} while (0)
#define CST(k, i) do {} while (0)
#define PRIO 0
  if (PRIO == 1 && w >= 4) __builtin_amdgcn_s_setprio(1);
  STAMP(0); CST(3, 18);
  u32x4 a[24];
  u32x4 st[18];
  const int rsw = c32 ^ (16 * (w >> 2));
  const u32 frow = (h * 32 + rsw) * 16;
  u32 imga[8];
  {
    PHASE();
    const char* wq = ws + WS_WQF + (size_t)j * WQB;
    const char* wqw = wq + (kh * 20 + tt) * 1024;
    const u32 wql = (kh * 20 + tt) * 1024;
    const char* n1 = ws + WS_N1 + (size_t)((mt * 4 + tt) * 40 + kh * 20) * 1024;
    auto issA = [&](auto k_) LAM { constexpr int k = decltype(k_)::v; gld<(k & 3) * 1024>(a[k], n1 + (k >> 2) * 4096, l16); };
    auto issW = [&](int s, int nb) LAM { dma(wqw + nb * 40960 + s * 4096, l16, wql + nb * 40960 + s * 4096); };
    u32x4 mp[2];
    u32 mdst; bool plus1;
    {
      const int m1 = tid >= 160, k1 = 4 * (tid - 160 * m1);
      const int u2 = tid - 320, m2 = (u2 * 205) >> 12, e2 = u2 - 20 * m2;
      const int col2 = (m2 == 0 ? 1280 : m2 == 2 ? 3200 : m2 == 4 ? 1920 : 2560) + 80 * (m2 < 3 ? n8 : (mt >> 1)) + 4 * (m2 < 5 ? e2 : 0);
      const int col = tid < 320 ? 640 * m1 + k1 : col2;
#pragma unroll
      for (int q = 0; q < 2; ++q) gld<0>(mp[q], ws + WS_MODP + q * 15360, (u32)col * 4);
      sfor<0, 4>(issA);
      issW(0, 0); issW(0, 1); issW(0, 2);
      mdst = tid < 320 ? L_GS + (k1 >> 3) * 32 + (m1 ? 0 : 16) + (k1 & 4) * 2 : L_MODS + u2 * 16;
      plus1 = tid < 320 ? m1 != 0 : (m2 & 1) != 0;
    }
    if (tid == 0) st128(L_FLAG, u32x4{0u, 0u, 0u, 0u});
    const int rk16 = w < 5 ? 5 * (mt >> 1) + w : (mt * 2 + th) * 3 + (w - 5);
    const char* rsrc = w < 5 ? (const char*)((mt & 1) ? p.wu : p.wg) + (size_t)rk16 * 49152 + 192 * j : (const char*)p.wd + (size_t)rk16 * 20480 + 160 * n8;
    const u32 rimg = 122880 + w * 3072;
    auto issR = [&]() LAM {
#pragma unroll
      for (int m = 0; m < 3; ++m) {
        const u32 x = 64 * m + lane, i5 = x < 160 ? x : 159, r12 = (x * 171) >> 11, r10 = (i5 * 205) >> 11;
        dma(rsrc, w < 5 ? r12 * 2880 + x * 16 : r10 * 1120 + i5 * 16, rimg + m * 1024);
      }
    };
    f32x16 acc[3];
    const u32 wb = kh * 20480 + frow, gb_ = L_GS + kh * 1280 + h * 32;
    u32x4 gs[3][2], wf[2][3], ro[3];
    f32x4 csum = {0.f, 0.f, 0.f, 0.f};
    auto ldgs = [&](int k) LAM { gs[k % 3][0] = lds128(gb_ + k * 64); gs[k % 3][1] = lds128(gb_ + k * 64 + 16); };
    auto modulate = [&](int k) LAM {
      PIN(a[k]);
#pragma unroll
      for (int e = 0; e < 4; ++e)
        a[k][e] = pkfma16(a[k][e], gs[k % 3][0][e], gs[k % 3][1][e]);
    };
    FST(3, 1);
    WAITV(7);
    FST(3, 2);
    f32x4 mv;
    {
#pragma unroll
      for (int q = 0; q < 2; ++q) PIN(mp[q]);
      float mxv = 0.f;
#pragma unroll
      for (int e = 0; e < 4; ++e) {
        const float s = rbf(u2f(mp[0][e]) + u2f(mp[1][e]));
        mv[e] = plus1 ? rbf(1.f + s) : s;
        mxv = __builtin_fmaxf(mxv, __builtin_fabsf(mv[e]));
      }
      if (tid < 320) st64(mdst, u32x2{pkh2(mv[0], mv[1]), pkh2(mv[2], mv[3])}); else if (tid < 420) stf4(mdst, mv);
      const bool big = __builtin_amdgcn_ballot_w64(tid < 320 && !(mxv < 2000.f)) != 0ull;
      if (lane == 0) *(LAS u32*)(size_t)(L_FLAG + 16 + w * 4) = big;
    }
    WAITV(0);
    BAR();
    FST(3, 3);
    auto grpA = [&](int cond, int s) LAM { gld4_if(cond, a[4 * s], a[4 * s + 1], a[4 * s + 2], a[4 * s + 3], n1 + s * 4096, l16); };
    const int kl = kh ^ 1;
    auto grpW = [&](int s) LAM { issW(s, 0); issW(s, 1); issW(s, 2); };
    grpA(kh, 1); if (kh) { grpW(1); issR(); }
    bool scaled;
    {
      const u32x4 f0 = lds128(L_FLAG + 16);
      const u32 f1 = *(const LAS u32*)(size_t)(L_FLAG + 32);
      ldgs(0); ldgs(1);
      scaled = __builtin_amdgcn_readfirstlane(f0.x | f0.y | f0.z | f0.w | f1) != 0;
      if (scaled) {
        BAR();
        const float sc = 9.765625e-4f;
        if (tid < 320) st64(mdst, u32x2{pkh2(mv[0] * sc, mv[1] * sc), pkh2(mv[2] * sc, mv[3] * sc)});
        BAR();
        ldgs(0); ldgs(1);
      }
    }
    modulate(0);
    SB();
    const Run<30, 8, false> rb3(wq + 122880, 122880, w);
    sfor<0, 21>([&](auto k_) LAM {
      constexpr int k = decltype(k_)::v, s = k >> 2, i = k & 3;
      if constexpr (i == 3 && k < 16) WAITV(k == 3 ? 13 : 10);
      if constexpr (i == 0 && k > 0 && k < 20) {
        FST(3, 3 + 3 * s); WAITV(k == 4 ? 10 : k == 16 ? 4 : 7); FST(3, 4 + 3 * s); BAR(); FST(3, 5 + 3 * s);
        if (s == 2 && CSEL == 4) { CST(4, 18); STAMP(16); } if (s == 3 && CSEL == 4) { CST(4, 19); STAMP(17); }
      }
      if constexpr (k < 20) {
#pragma unroll
        for (int nb = 0; nb < 3; ++nb) wf[k & 1][nb] = lds128(wb + nb * 40960 + k * 1024);
      }
      if constexpr (k == 1) {
        f32x16 z;
#pragma unroll
        for (int e = 0; e < 16; ++e) z[e] = 0.f;
#pragma unroll
        for (int nb = 0; nb < 3; ++nb) acc[nb] = MFMA32H(wf[0][nb], a[0], z);
      } else if constexpr (k > 1) {
#pragma unroll
        for (int nb = 0; nb < 3; ++nb) acc[nb] = MFMA32H(wf[(k - 1) & 1][nb], a[k - 1], acc[nb]);
      }
      if constexpr (k + 1 < 20) modulate(k + 1);
      if constexpr (k + 2 < 20) ldgs(k + 2);
      if constexpr (k == 8) {
        const u32 q4 = (lane >> 2) & 3, nh = (lane >> 4) & 1;
        const u32 r0 = rimg + (w < 5 ? (8 * h + q4) * 192 + 32 * nh : (4 * nh + q4) * 160 + 32 * h) + (lane & 3) * 8, r1 = r0 + (w < 5 ? 768 : 1280);
#pragma unroll
        for (int u = 0; u < 3; ++u) ro[u] = ldstr2(r0 + 64 * u, r1 + 64 * u);
      }
      if constexpr (k == 9 || k == 10) {
        if (w < 5) {
          const u32 ga_ = L_MODS + 960 + w * 64 + h * 32;
          const f32x4 g0 = ldsf4(ga_), g1 = ldsf4(ga_ + 16), s0 = ldsf4(ga_ + 320), s1 = ldsf4(ga_ + 336);
#pragma unroll
          for (int u = (k == 9 ? 0 : 2); u < (k == 9 ? 2 : 3); ++u) {
            float c = 0.f, bi = 0.f;
#pragma unroll
            for (int e = 0; e < 4; ++e) {
              const u32 v = ro[u][e];
              const float x0 = lo16(v), x1 = hi16(v), ge = e < 2 ? g0[2 * e] : g1[2 * e - 4], go = e < 2 ? g0[2 * e + 1] : g1[2 * e - 3];
              const float se = e < 2 ? s0[2 * e] : s1[2 * e - 4], so_ = e < 2 ? s0[2 * e + 1] : s1[2 * e - 3];
              c = fma1(ge, x0, c); c = fma1(go, x1, c); bi = fma1(se, x0, bi); bi = fma1(so_, x1, bi);
            }
            auto r = __builtin_amdgcn_permlane32_swap(f2u(c), f2u(bi), false, false);
            *(LAS float*)(size_t)(w * 768 + u * 256 + (c32 >> 3) * 64 + h * 32 + (c32 & 7) * 4) = u2f(r[0]) + u2f(r[1]);
          }
        }
      }
      if constexpr (k == 17) {
        if (w == 7) {
#pragma unroll
          for (int u = 0; u < 5; ++u) csum += ldsf4(u * 768 + (lane < 48 ? l16 : 0u));
        }
      }
      if constexpr (k == 16) {
        const u32 x2 = 16 * kh + 4 * h, tb = 12288 + (32 * tt + c32) * 48 + 2 * x2;
#pragma unroll
        for (int u = 0; u < 8; ++u) {
          const u32 part = ((x2 + 32 * (u >> 1) + 8 * (u & 1)) * 205) >> 12;
          imga[u] = part * 18392 + tb + 64 * (u >> 1) + 16 * (u & 1); PIN(imga[u]);
        }
      }
      SB();
      if constexpr (k == 0) { grpA(kl, 1); if (!kh) { grpW(1); issR(); } }
      if constexpr (k == 1) grpA(kl, 2);
      if constexpr (k == 2) { if (!kh) grpW(2); grpA(kh, 2); }
      if constexpr (k == 3) { if (kh) grpW(2); }
      if constexpr (s == 1 || s == 2) {
        if constexpr (i == 0) grpA(kl, s + 2);
        if constexpr (i == 1) { if (!kh) grpW(s + 2); }
        if constexpr (i == 2) grpA(kh, s + 2);
        if constexpr (i == 3) { if (kh) grpW(s + 2); }
      }
      if constexpr (k == 12) { if (!kh) rb3.all(l16); }
      if constexpr (k == 14) { if (kh) rb3.all(l16); }
      if constexpr (k == 16) {
        u32 off0, du;
        if (w < 5) { const int k2 = rk16 >= 20, i2 = rk16 - 20 * k2; off0 = WS_WGF + j * WGB + (k2 * 20 + i2) * 2048 + (mt & 1) * 1024 + l16; du = 81920; }
        else { const int k2 = rk16 >= 48, kk = rk16 - 48 * k2; off0 = WS_WDF + n8 * WDB + ((kk >> 1) * 10 + k2 * 5 + h) * 1024 + (kk & 1) * 512 + c32 * 16; du = 2048; }
        asm volatile("" ::: "memory");
        __builtin_amdgcn_raw_buffer_store_b128(ro[0], rs, off0, 0, 16);
        __builtin_amdgcn_raw_buffer_store_b128(ro[1], rs, off0 + du, 0, 16);
        __builtin_amdgcn_raw_buffer_store_b128(ro[2], rs, (w < 5 || h == 0) ? off0 + 2 * du : 0xfffffff0u, 0, 16);
        asm volatile("" ::: "memory");
      }
      SB();
    });
    STAMP(1); CST(3, 19); FST(6, 1);
    WAITV(3);
    BAR();
    FST(6, 2);
    if (w == 7 && lane < 48) __builtin_amdgcn_raw_buffer_store_b128(__builtin_bit_cast(u32x4, csum), rs, WS_CSP + (u32)(j * 16 + mt) * 768 + l16, 0, 16);
    const u32 exw = (w >> 1) * 18432 + (w & 1) * 6144 + l16, exr = ((w ^ 4) >> 1) * 18432 + (w & 1) * 6144 + l16;
#pragma unroll
    for (int nb = 0; nb < 3; ++nb) {
      stf4(exw + nb * 2048, f32x4{acc[nb][8], acc[nb][9], acc[nb][10], acc[nb][11]});
      stf4(exw + nb * 2048 + 1024, f32x4{acc[nb][12], acc[nb][13], acc[nb][14], acc[nb][15]});
    }
    if (kh) st64((tid >> 7) * 18432 + 12288 + (tid & 127) * 48 + 40, u32x2{0x3f803f80u, 0x3f803f80u});
    BAR();
    FST(6, 3);
    const float osc = scaled ? 1024.f : 1.f, qs = QSCALE * (1.f / 4096.f) * osc, sc2 = kh ? qs : osc;
#pragma unroll
    for (int nb = 0; nb < 3; ++nb)
#pragma unroll
      for (int q = 0; q < 2; ++q) {
        const f32x4 o = ldsf4(exr + nb * 2048 + q * 1024);
        float v0 = acc[nb][4 * q] + o[0], v1 = acc[nb][4 * q + 1] + o[1], v2 = acc[nb][4 * q + 2] + o[2], v3 = acc[nb][4 * q + 3] + o[3];
        if (nb == 2) { v0 *= sc2; v1 *= sc2; v2 *= sc2; v3 *= sc2; }
        else if (scaled) { v0 *= osc; v1 *= osc; v2 *= osc; v3 *= osc; }
        st64(imga[2 * nb + q], u32x2{pk2(v0, v1), pk2(v2, v3)});
      }
    BAR();
    FST(6, 4);
    {
      const u32 dst = WS_KVX + (u32)(mt * 16 + j) * KVB + w * 3072 + l16, src = (w >> 1) * 18432 + 12288 + (w & 1) * 3072 + l16;
#pragma unroll
      for (int i = 0; i < 3; ++i) __builtin_amdgcn_raw_buffer_store_b128(lds128(src + i * 1024), rs, dst + i * 1024, 0, 16);
    }
    f32x16 acc3;
#pragma unroll
    for (int e = 0; e < 16; ++e) acc3[e] = 0.f;
    {
      const int r3 = rsw < 24 ? rsw : (rsw < 28 ? rsw - 4 : rsw - 12);
      const u32 wb3 = 122880 + kh * 15360 + (h * 24 + r3) * 16;
      u32x4 w3[4];
#pragma unroll
      for (int k = 0; k < 3; ++k) w3[k] = lds128(wb3 + k * 768);
#pragma unroll
      for (int k = 0; k < 20; ++k) {
        if (k + 3 < 20) w3[(k + 3) & 3] = lds128(wb3 + (k + 3) * 768);
        acc3 = MFMA32H(w3[k & 3], a[k], acc3);
        if (k == 9) {
          FST(6, 5);
          WAITVM(0);
          FST(6, 6);
          if (lane == 0) {
            const u32 old = __hip_atomic_fetch_add((LAS u32*)(size_t)L_FLAG, 1u, __ATOMIC_RELAXED, __HIP_MEMORY_SCOPE_WORKGROUP);
            if (old == 7u) {
              __hip_atomic_store((gu32*)(ws + WS_KVF + (mt * 16 + j) * 128), 1u, RLX_AGENT);
              signal(ws, C_RG + j * 256); signal(ws, C_RD + n8 * 256);
            }
          }
        }
        SB();
      }
    }
    const u32 ex3w = (w < 4 ? 73728 : 84992) + w * 2048 + l16, ex3r = (w < 4 ? 93184 : 65536) + w * 2048 + l16;
    stf4(ex3w, f32x4{acc3[8], acc3[9], acc3[10], acc3[11]});
    stf4(ex3w + 1024, f32x4{acc3[12], acc3[13], acc3[14], acc3[15]});
    FST(6, 7);
    BAR();
    FST(6, 8);
    STAMP(2);
#pragma unroll
    for (int q = 0; q < 2; ++q) {
      const f32x4 o = ldsf4(ex3r + q * 1024);
      st64(imga[6 + q], u32x2{pk2((acc3[4 * q] + o[0]) * qs, (acc3[4 * q + 1] + o[1]) * qs), pk2((acc3[4 * q + 2] + o[2]) * qs, (acc3[4 * q + 3] + o[3]) * qs)});
    }
    BAR();
    FST(6, 9);
  }
  u32x2 xr[3];
  const int c16 = lane & 15, g4 = lane >> 4;
  {
    PHASE();
    const int hh = w >> 2, qb = w < 4 ? w : 7 - w;
    u32x4 q0f, q1f;
    {
      const u32 qa = (4 + hh) * 18432 + 12288 + (32 * qb + c32) * 48;
      const u32x2 x2 = lds64(qa + 32);
      q0f = lds128(qa + 16 * h);
      q1f = u32x4{h ? 0u : x2.x, h ? 0u : x2.y, 0u, 0u};
      const int tok64 = 128 * mt + 64 * th + 16 * tt + c16;
#pragma unroll
      for (int m = 0; m < 3; ++m) gld64(xr[m], (const char*)p.x, (u32)(tok64 * 1280 + (80 * n8 + 16 * ((m + 3 * kh) % 5) + 4 * g4) * 2));
    }
    BAR();
    STAMP(3); STAMP(4); CST(1, 18); FST(4, 12);
    const char* sg0 = ws + WS_WGF + (size_t)j * WGB + (kh * 40 + tt * 10) * 1024;
    const int tmin = mt >= 2 ? 0 : 8 - 4 * mt - qb;
    const u32 kb = hh * 18432 + (32 * qb + c32) * 48, kb2 = kb + 16 * h;
    const u32 vb = (2 + hh) * 18432 + (32 * qb + 4 * h + ((lane >> 2) & 3)) * 48 + ((lane >> 4) & 1) * 32 + (lane & 3) * 8;
    const int qlim = c32 - 4 * h;
    const int dw = (w - 2) & 3;
    const bool duty = w >= 2 && w <= 5;
    const Run<70, 4, false> rwo(ws + WS_WOF + (size_t)n8 * WOB, wo_slot(0), dw);
    const u32 fa = WS_ACT + (u32)((mt * 4 + dw) * 96 + 6 * j) * 1024 + l16;
    const u32 fx = dw < 2 ? WS_X1 + (u32)((mt * 4 + 2 * th + dw) * 40 + 5 * n8) * 1024 + l16 : WS_STAT + (u32)((mt * 4 + 2 * th + h) * 8 + n8) * 512 + c32 * 16;
    const int ko = 40 * j + 20 * hh + 4 * h;
    u32 ob = WS_OAF + (u32)((mt * 4 + qb) * 40) * 1024 + c32 * 16 + (ko >> 3) * 512 + ((ko >> 2) & 1) * 8;
    PIN(ob);
    auto lds_wait = [&](u32 addr, u32 want) LAM {
      for (u32 spins = 0; spins < SPIN_CAP * 8; ++spins) {
        if ((u32)__builtin_amdgcn_readfirstlane(__hip_atomic_load((LAS u32*)(size_t)addr, __ATOMIC_RELAXED, __HIP_MEMORY_SCOPE_WORKGROUP)) >= want) break;
        __builtin_amdgcn_s_sleep(1);
      }
      asm volatile("" ::: "memory");
    };
    {
      if (!duty) {
        const int sl = w >= 6;
        const u32 l0 = (w & 1) * 36864 + sl * 6144;
        if (mt + sl >= 2) {
          poll_ge(ws, WS_KVF + ((mt - 2 + sl) * 16 + j) * 128, 1u);
          const char* src = ws + WS_KVX + (size_t)((mt - 2 + sl) * 16 + j) * KVB + (w & 1) * 12288;
#pragma unroll
          for (int u = 0; u < 4; ++u) {
            const u32 l1 = l0 + (u >> 1) * 18432 + (u & 1) * 3072;
            dmai<0, true>(src + u * 3072, l16, l1); dmai<1024, true>(src + u * 3072, l16, l1); dmai<2048, true>(src + u * 3072, l16, l1);
          }
        } else {
#pragma unroll
          for (int u = 0; u < 12; ++u) st128(l0 + (u / 6) * 18432 + (u % 6) * 1024 + l16, u32x4{0u, 0u, 0u, 0u});
        }
      }
    }
    auto halo = [&]() LAM {
      if (!duty) {
        asm volatile("s_waitcnt vmcnt(0) lgkmcnt(0)" ::: "memory");
        if (lane == 0) __hip_atomic_fetch_add((LAS u32*)(size_t)(L_FLAG + (w >= 6 ? 8 : 4)), 1u, __ATOMIC_RELAXED, __HIP_MEMORY_SCOPE_WORKGROUP);
      }
    };
    f32x16 oacc;
    u32 rcnt;
    auto attn = [&](auto full_) LAM {
      constexpr bool FULL = decltype(full_)::v;
      u32x4 ka, kq, vf[2][2];
      kq = u32x4{0u, 0u, 0u, 0u};
      auto ldkv = [&](int t) LAM {
        const u32x2 x2 = lds64(kb + t * 1536 + 32);
        ka = lds128(kb2 + t * 1536); kq.x = x2.x; kq.y = x2.y;
#pragma unroll
        for (int s2 = 0; s2 < 2; ++s2) { const u32 va = vb + (32 * t + 16 * s2) * 48; vf[t & 1][s2] = ldstr2(va, va + 384); }
      };
      auto splat = [&](float v) LAM -> f32x16 {
        f32x16 c;
#pragma unroll
        for (int e = 0; e < 16; ++e) c[e] = v;
        return c;
      };
      auto pre = [&](auto m_) LAM {
        constexpr int m = decltype(m_)::v;
        if constexpr (m == 2) gld32_sc1(rcnt, ws, lane < 32 ? C_RG + j * 256 : C_RD + n8 * 256);
        if constexpr (m == 4) {
          if (duty) WAITV(6); else WAITV(0);
          PIN(rcnt);
          if (__builtin_amdgcn_ballot_w64(rcnt < (lane < 32 ? 16u : 32u)) != 0ull) { poll_ge(ws, C_RG + j * 256, 16u); poll_ge(ws, C_RD + n8 * 256, 32u); }
        }
        if constexpr (m >= 4) {
          gld_sc1<((2 * m - 8) & 3) * 1024>(st[2 * m - 8], sg0 + ((2 * m - 8) >> 2) * 4096, l16);
          gld_sc1<((2 * m - 7) & 3) * 1024>(st[2 * m - 7], sg0 + ((2 * m - 7) >> 2) * 4096, l16);
        }
        if constexpr (m == 1 && OWARM) {
#pragma unroll
          for (int g = 0; g < 3; ++g)
            if (g < 2 || h == 0) __builtin_amdgcn_raw_buffer_store_b64(u32x2{0u, 0u}, rs, ob + g * 512, 0, 16);
          asm volatile("" ::: "memory");
        }
        if (duty) {
          rwo.go<2 * m>(l16); rwo.go<2 * m + 1>(l16);
          const u32x4 f = {~0u, ~0u, ~0u, ~0u};
          if constexpr (m < 6) __builtin_amdgcn_raw_buffer_store_b128(f, rs, fa + m * 1024, 0, 16);
          if constexpr (m >= 4) { if (dw < 2 || (dw == 2 && m == 4)) __builtin_amdgcn_raw_buffer_store_b128(f, rs, fx + (m - 4) * 1024, 0, 16); }
          asm volatile("" ::: "memory");
        }
        SB();
      };
      auto sync = [&](int t) LAM {
        if (t >= 4) { if (qb == 7 - t) lds_wait(L_FLAG + 8, 2u); } else { if (qb == 3 - t) lds_wait(L_FLAG + 4, 2u); }
      };
      f32x16 sb[2], cm;
      float pe[16];
      FST(4, 1);
      ldkv(8);
      sb[0] = MFMA32(ka, q0f, splat(0.f));
      sb[0] = MFMA32(kq, q1f, sb[0]);
      SB();
      pre(IC<0>{});
#pragma unroll
      for (int e = 0; e < 16; ++e) pe[e] = ((e & 3) + 8 * (e >> 2) > qlim) ? -1.0e30f : sb[0][e];
      float mx = __builtin_fmaxf(pe[0], pe[1]);
#pragma unroll
      for (int e = 2; e < 16; e += 2) mx = max3(mx, pe[e], pe[e + 1]);
      mx = -max32(mx);
      cm = splat(mx);
      auto cin = [&](int t) LAM -> f32x16 { if constexpr (FULL) return cm; else return splat(t >= tmin ? mx : -1.0e30f); };
#pragma unroll
      for (int s2 = 0; s2 < 2; ++s2) {
        u32x4 pf;
#pragma unroll
        for (int e = 0; e < 4; ++e) pf[e] = pk2(__builtin_amdgcn_exp2f(pe[8 * s2 + 2 * e] + mx), __builtin_amdgcn_exp2f(pe[8 * s2 + 2 * e + 1] + mx));
        if (s2 == 0) oacc = MFMA32(vf[0][0], pf, splat(0.f)); else oacc = MFMA32(vf[0][1], pf, oacc);
      }
      SB();
      FST(4, 2);
      halo();
      pre(IC<1>{});
      sync(7);
      ldkv(7);
      sb[1] = MFMA32(ka, q0f, cin(7));
      sb[1] = MFMA32(kq, q1f, sb[1]);
      SB();
      sfor<0, 8>([&](auto u_) LAM {
        constexpr int t = 7 - decltype(u_)::v, c = t & 1, n = c ^ 1;
        u32x4 pf0, pf1;
        FST(4, 3 + decltype(u_)::v);
        if constexpr (t > 0) { if constexpr (t - 1 != 7) sync(t - 1); ldkv(t - 1); }
        if constexpr (t == 0) {
#pragma unroll
          for (int e = 0; e < 16; ++e) sb[c][e] = ((e & 3) + 8 * (e >> 2) > qlim) ? sb[c][e] : -1.0e30f;
        }
#pragma unroll
        for (int e = 0; e < 8; ++e) pe[e] = __builtin_amdgcn_exp2f(sb[c][e]);
        SB();
        if constexpr (t > 0) { sb[n] = MFMA32(ka, q0f, cin(t - 1)); SB(); }
#pragma unroll
        for (int e = 8; e < 12; ++e) pe[e] = __builtin_amdgcn_exp2f(sb[c][e]);
#pragma unroll
        for (int e = 0; e < 4; ++e) pf0[e] = pk2(pe[2 * e], pe[2 * e + 1]);
        SB();
        if constexpr (t > 0) { sb[n] = MFMA32(kq, q1f, sb[n]); SB(); }
#pragma unroll
        for (int e = 12; e < 16; ++e) pe[e] = __builtin_amdgcn_exp2f(sb[c][e]);
        SB();
        oacc = MFMA32(vf[c][0], pf0, oacc);
        SB();
#pragma unroll
        for (int e = 0; e < 4; ++e) pf1[e] = pk2(pe[8 + 2 * e], pe[8 + 2 * e + 1]);
        SB();
        if constexpr (t > 0) pre(IC<9 - t>{});
        oacc = MFMA32(vf[c][1], pf1, oacc);
        SB();
      });
    };
    auto exact = [&]() LAM {
      auto sc = [&](int t, float c0, f32x16& s) LAM {
        const u32x2 x2 = lds64(kb + t * 1536 + 32);
#pragma unroll
        for (int e = 0; e < 16; ++e) s[e] = c0;
        s = MFMA32(lds128(kb2 + t * 1536), q0f, s);
        s = MFMA32((u32x4{x2.x, x2.y, 0u, 0u}), q1f, s);
#pragma unroll
        for (int e = 0; e < 16; ++e) s[e] = (u32)(32 * t + (e & 3) + 8 * (e >> 2) - qlim - 1) < 256u ? s[e] : -1.0e30f;
      };
      float mx = -1.0e30f;
      f32x16 s;
#pragma nounroll
      for (int t = tmin; t < 9; ++t) {
        sc(t, 0.f, s);
#pragma unroll
        for (int e = 0; e < 16; e += 2) mx = max3(mx, s[e], s[e + 1]);
      }
      mx = -max32(mx);
#pragma unroll
      for (int e = 0; e < 16; ++e) oacc[e] = 0.f;
#pragma nounroll
      for (int t = tmin; t < 9; ++t) {
        sc(t, mx, s);
#pragma unroll
        for (int s2 = 0; s2 < 2; ++s2) {
          u32x4 pf;
#pragma unroll
          for (int e = 0; e < 4; ++e) pf[e] = pk2(__builtin_amdgcn_exp2f(s[8 * s2 + 2 * e]), __builtin_amdgcn_exp2f(s[8 * s2 + 2 * e + 1]));
          const u32 va = vb + (32 * t + 16 * s2) * 48;
          oacc = MFMA32(ldstr2(va, va + 384), pf, oacc);
        }
      }
    };
    if (mt >= 2) attn(BoolC<true>{}); else attn(BoolC<false>{});
    if (__builtin_amdgcn_ballot_w64(h != 0 && !(oacc[8] < ATT_LIMIT)) != 0ull) exact();
    const float lx = xor32f(oacc[8]);
    const float inv = __builtin_amdgcn_rcpf(64.f * (h ? oacc[8] : lx));
#pragma unroll
    for (int g = 0; g < 3; ++g) {
      const u32x2 o = {pk2(oacc[4 * g] * inv, oacc[4 * g + 1] * inv), pk2(oacc[4 * g + 2] * inv, oacc[4 * g + 3] * inv)};
      if (g < 2 || h == 0) __builtin_amdgcn_raw_buffer_store_b64(o, rs, ob + g * 512, 0, 16);
    }
    STAMP(5); CST(1, 19); FST(4, 11);
    WAITVM(0);
    BAR();
    if (tid == 0) signal(ws, C_O + mt * 256);
    STAMP(6); FST(4, 13);
  }
  u32 wbase[5], avoff;
  u32x2 x1k[3];
  f32x4 c5[5];
  {
    PHASE();
    const char* sg1 = ws + WS_WGF + (size_t)j * WGB + 81920 + (kh * 40 + tt * 8) * 1024;
    auto stage1 = [&]() LAM { sfor<0, 8>([&](auto m_) LAM { constexpr int m = decltype(m_)::v; gld_sc1<(m & 3) * 1024>(st[10 + m], sg1 + (m >> 2) * 4096, l16); }); };
    if (w >= 4) { __builtin_amdgcn_s_sleep(2); Run<30, 4, false>(ws + WS_WOF + (size_t)n8 * WOB + 71680, wo_slot(7), w - 4).all(l16); }
    stage1();
#pragma unroll
    for (int m = 0; m < 5; ++m) wbase[m] = (kh * 5 + (m + 3 * kh) % 5) * 1024 + l16;
    avoff = (g4 >> 1) * 1024 + (g4 & 1) * 512 + (16 * (tt & 1) + c16) * 16;
    const int tt64 = mt * 4 + 2 * th + (tt >> 1);
    const char* ab = ws + WS_OAF + (size_t)(tt64 * 40 + 20 * kh) * 1024;
    const u32 ew = 73728 + (tt * 5) * 1024 + l16;
    u32 xb = WS_X1 + (u32)(tt64 * 40 + 5 * n8) * 1024 + (g4 >> 1) * 512 + (16 * (tt & 1) + c16) * 16 + (g4 & 1) * 8 + kh * 3072;
    u32 sta = WS_STAT + (u32)((tt64 * 8 + n8) * 32 + 16 * (tt & 1) + c16) * 16 + kh * 8;
    u32 mo = L_MODS + kh * 192 + g4 * 16;
    PIN(xb); PIN(sta); PIN(mo); PIN(avoff);
    auto iss = [&](auto s_) LAM { constexpr int s = decltype(s_)::v; gld_sc1<(s & 1) * 2048>(a[s], ab + (s >> 1) * 4096, avoff); };
    u32x4 wf[3][5];
#pragma unroll
    for (int s = 0; s < 2; ++s)
#pragma unroll
      for (int m = 0; m < 5; ++m) wf[s][m] = lds128(wbase[m] + wo_slot(s));
#pragma unroll
    for (int m = 0; m < 5; ++m) c5[m] = f32x4{0.f, 0.f, 0.f, 0.f};
    if (w == 0) poll_ge(ws, C_O + mt * 256, 16u);
    BAR();
    STAMP(7); FST(5, 1);
    sfor<0, 5>(iss);
    sfor<0, 10>([&](auto s_) LAM {
      constexpr int s = decltype(s_)::v;
      WAITV(s == 0 ? 4 : s == 1 ? 6 : 9 - s);
      FST(5, 2 + s);
      if (s == 4) BAR();
      PIN(a[s]);
      if constexpr (s + 2 < 10) {
#pragma unroll
        for (int m = 0; m < 5; ++m) wf[(s + 2) % 3][m] = lds128(wbase[m] + wo_slot(s + 2));
      }
#pragma unroll
      for (int m = 0; m < 5; ++m) c5[m] = MFMA16(wf[s % 3][m], a[s], c5[m]);
      SB();
      if constexpr (s == 0) sfor<5, 8>(iss);
      if constexpr (s == 1) sfor<8, 10>(iss);
      SB();
    });
    STAMP(8); FST(5, 12);
    stf4(ew + ((3 + 3 * kh) % 5) * 1024, c5[3]);
    stf4(ew + ((4 + 3 * kh) % 5) * 1024, c5[4]);
    if (kh) stf4(ew, c5[2]);
    BAR();
    FST(5, 13);
    float xs[12];
#pragma unroll
    for (int m = 0; m < 3; ++m) PIN(xr[m]);
#pragma unroll
    for (int m = 0; m < 3; ++m) {
      if (m < 2 || kh == 0) {
        const f32x4 o = ldsf4(ew + kh * 3072 + m * 1024), ga = ldsf4(mo + m * 64), g2 = ldsf4(mo + 320 + m * 64);
        const u32 y01 = pk2(c5[m][0] + o[0], c5[m][1] + o[1]), y23 = pk2(c5[m][2] + o[2], c5[m][3] + o[3]);
        const u32 t01 = pk2(ga[0] * lo16(y01), ga[1] * hi16(y01)), t23 = pk2(ga[2] * lo16(y23), ga[3] * hi16(y23));
        x1k[m] = u32x2{pk2(lo16(xr[m].x) + lo16(t01), hi16(xr[m].x) + hi16(t01)), pk2(lo16(xr[m].y) + lo16(t23), hi16(xr[m].y) + hi16(t23))};
        xs[4 * m] = lo16(x1k[m].x); xs[4 * m + 1] = hi16(x1k[m].x); xs[4 * m + 2] = lo16(x1k[m].y); xs[4 * m + 3] = hi16(x1k[m].y);
        __builtin_amdgcn_raw_buffer_store_b64(u32x2{pk2(xs[4 * m] * g2[0], xs[4 * m + 1] * g2[1]), pk2(xs[4 * m + 2] * g2[2], xs[4 * m + 3] * g2[3])}, rs, xb + m * 1024, 0, 16);
      } else {
        x1k[m] = u32x2{0u, 0u};
        xs[8] = xs[9] = xs[10] = xs[11] = 0.f;
      }
    }
    float s = ((xs[0] + xs[1]) + (xs[2] + xs[3])) + ((xs[4] + xs[5]) + (xs[6] + xs[7])) + ((xs[8] + xs[9]) + (xs[10] + xs[11]));
    s = sum32(sum16(s));
    const float mean = s * (kh ? 1.f / 32.f : 1.f / 48.f);
    float q0 = 0.f, q1 = 0.f;
#pragma unroll
    for (int e = 0; e < 4; ++e) { const float d0 = xs[e] - mean, d1 = xs[4 + e] - mean; q0 = fma1(d0, d0, q0); q1 = fma1(d1, d1, q1); }
    if (kh == 0) {
#pragma unroll
      for (int e = 8; e < 12; ++e) { const float d = xs[e] - mean; q0 = fma1(d, d, q0); }
    }
    const float q = sum32(sum16(q0 + q1));
    if (g4 == 0) __builtin_amdgcn_raw_buffer_store_b64(u32x2{f2u(mean), f2u(q)}, rs, sta, 0, 16);
    FST(5, 14);
    BAR();
    STAMP(9); FST(5, 16);
  }
  {
    PHASE();
    const bool old4 = w < 4;
    const char* wgb = ws + WS_WGF + (size_t)j * WGB;
    const char* wdb = ws + WS_WDF + (size_t)n8 * WDB;
    u32x4 stv[4], csv[2];
    const char* ab = ws + WS_X1 + (size_t)((mt * 4 + tt) * 40 + 20 * kh) * 1024;
    const char* sb = ws + WS_STAT + (size_t)(mt * 4 + tt) * 4096;
    auto iss = [&](auto s_) LAM { constexpr int s = decltype(s_)::v; gld_sc1<(s & 3) * 1024>(a[s], ab + (s >> 2) * 4096, l16); };
    {
      const char* cb = ws + WS_CSP + (size_t)(j * 16 + 4 * (w >> 1) + (w & 1)) * 768;
      const u32 co = lane < 48 ? l16 : 0u;
      gld_sc1<0>(csv[0], cb, co); gld_sc1<1536>(csv[1], cb, co);
    }
#pragma unroll
    for (int m = 0; m < 18; ++m) { PIN(st[m]); st128((m < 10 ? (kh * 40 + tt * 10 + m) : (80 + kh * 32 + tt * 8 + m - 10)) * 1024 + l16, st[m]); }
    float c1 = 0.f, d2 = 0.f;
    u32x4 bcor = {0u, 0u, 0u, 0u};
    u32 sw = kh * 40960 + tt * 4096 + l16, sr = (kh ^ 1) * 40960 + tt * 4096 + l16;
    u32 actb = WS_ACT + (u32)((mt * 4 + tt) * 96 + 6 * j + kh) * 1024 + l16;
    const u32 so = h * 2048 + c32 * 16;
    PIN(actb);
    const auto rC = gu_run<1, 16, 20, 4>(wgb, w & 3);
    const auto rA = gu_run<2, 0, 8, 4>(wgb, w & 3);
    const auto rB = gu_run<2, 8, 20, 4>(wgb, w & 3);
    const Run<60, 8, true> rD(wdb, 0, w);
    const Run<90, 8, true> rS(wdb + 61440, 61440, w);
    gate(sb, (c32 >> 2) * 512 + ((c32 >> 1) & 1) * 256 + (c32 & 1) * 8);
    STAMP(10); FST(2, 1); FST(5, 17);
    sfor<0, 2>(iss);
    gld_sc1<0>(stv[0], sb, so); gld_sc1<512>(stv[1], sb, so); gld_sc1<1024>(stv[2], sb, so); gld_sc1<1536>(stv[3], sb, so);
    sfor<2, 6>(iss);
    f32x16 ag[2], au[2];
    u32x4 fg[3], fu[3], outv;
    u32 ub = 0;
    auto ldw = [&](auto g_) LAM {
      constexpr int g = decltype(g_)::v, pp = g / 20, i = g % 20;
      constexpr bool first = i == 0 || gu_unit(pp, 0, i) != gu_unit(pp, 0, i - 1) + 1;
      constexpr int i0 = [] { int x = i; while (x > 0 && gu_unit(pp, 0, x) == gu_unit(pp, 0, x - 1) + 1) --x; return x; }();
      if constexpr (first) ub = frow + (kh ? gu_unit(pp, 1, i) : gu_unit(pp, 0, i)) * 2048;
      fg[g % 3] = lds128(ub + (i - i0) * 2048); fu[g % 3] = lds128(ub + (i - i0) * 2048 + 1024);
    };
    auto xwrite = [&](int pp) LAM {
      const f32x16 &G = ag[pp & 1], &U = au[pp & 1];
      stf4(sw, f32x4{G[8], G[9], G[10], G[11]}); stf4(sw + 1024, f32x4{G[12], G[13], G[14], G[15]});
      stf4(sw + 2048, f32x4{U[8], U[9], U[10], U[11]}); stf4(sw + 3072, f32x4{U[12], U[13], U[14], U[15]});
    };
    f32x4 xo[4];
    auto xread = [&]() LAM {
#pragma unroll
      for (int u = 0; u < 4; ++u) xo[u] = ldsf4(sr + u * 1024);
    };
    auto xadd = [&](int pp, int q) LAM {
#pragma unroll
      for (int i = 0; i < 4; ++i) { ag[pp & 1][4 * q + i] += xo[q][i]; au[pp & 1][4 * q + i] += xo[2 + q][i]; }
    };
    auto corr = [&](int pp) LAM {
      ag[pp & 1] = MFMA32(lds128(L_T2 + pp * 1024 + rsw * 16), bcor, ag[pp & 1]);
      au[pp & 1] = MFMA32(lds128(L_T2 + pp * 1024 + 512 + rsw * 16), bcor, au[pp & 1]);
    };
    float eu[2], ee[2];
    auto epi = [&](int pp, int o, int ph) LAM {
#pragma unroll
      for (int i = 0; i < 2; ++i) {
        if (ph == 0) { ee[i] = __builtin_amdgcn_exp2f(ag[pp & 1][2 * o + i] * c1); eu[i] = ag[pp & 1][2 * o + i] * au[pp & 1][2 * o + i]; }
        if (ph == 1) ee[i] = __builtin_amdgcn_rcpf(fma1(ee[i], d2, d2));
      }
      if (ph == 2) outv[o] = pk2(eu[0] * ee[0], eu[1] * ee[1]);
    };
    auto ostore = [&](int pp) LAM { __builtin_amdgcn_raw_buffer_store_b128(outv, rs, actb + pp * 2048, 0, 16); asm volatile("" ::: "memory"); };
    BAR();
    ldw(IC<0>{}); ldw(IC<1>{});
    auto nextA = [&](auto g_) LAM {
      constexpr int g = decltype(g_)::v;
      WAITV(g == 0 ? 9 : g == 1 ? 12 : g == 2 ? 11 : g == 3 ? 14 : 19 - g);
      PIN(a[g]);
      ensure<(g & 3) * 1024>(a[g], ab + (g >> 2) * 4096, l16);
    };
    nextA(IC<0>{});
    sfor<0, 60>([&](auto g_) LAM {
      constexpr int g = decltype(g_)::v, pp = g / 20, i = g % 20;
      if (g == 32) WAITV(0);
      if (g == 43) WAITV(NRS >= 3 ? 4 : 1 + NRS);
      if ((g & 3) == 0) FST(2, 2 + g / 4);
      if (g == 12 || g == 22 || g == 32 || g == 43 || g == 50) BAR();
      if (g == 22 && CSEL != 4) { STAMP(16); CST(0, 18); }
      if (g == 43 && CSEL != 4) { STAMP(17); CST(0, 19); }
      if constexpr (g + 2 < 60) ldw(IC<g + 2>{});
      if (g == 22 || g == 43) xread();
      if (i == 0) {
        f32x16 z;
#pragma unroll
        for (int e = 0; e < 16; ++e) z[e] = 0.f;
        ag[pp & 1] = MFMA32(fg[g % 3], a[i], z); au[pp & 1] = MFMA32(fu[g % 3], a[i], z);
      } else {
        ag[pp & 1] = MFMA32(fg[g % 3], a[i], ag[pp & 1]); au[pp & 1] = MFMA32(fu[g % 3], a[i], au[pp & 1]);
      }
      if (g == 4) {
#pragma unroll
        for (int u = 0; u < 4; ++u) PIN(stv[u]);
        ensure<0>(stv[0], sb, so); ensure<512>(stv[1], sb, so); ensure<1024>(stv[2], sb, so); ensure<1536>(stv[3], sb, so);
        PIN(csv[0]); PIN(csv[1]);
        f32x4 sm;
#pragma unroll
        for (int e = 0; e < 4; ++e) sm[e] = u2f(csv[0][e]) + u2f(csv[1][e]);
        if (lane < 48) stf4(L_GS + (w >> 1) * 1536 + (w & 1) * 768 + l16, sm);
        float s = 0.f;
#pragma unroll
        for (int u = 0; u < 4; ++u) s += 48.f * u2f(stv[u][0]) + 32.f * u2f(stv[u][2]);
        const float mean = sum32(s) * (1.f / 640.f);
        float q = 0.f;
#pragma unroll
        for (int u = 0; u < 4; ++u) {
          const float d0 = u2f(stv[u][0]) - mean, d1 = u2f(stv[u][2]) - mean;
          q += u2f(stv[u][1]) + u2f(stv[u][3]) + 48.f * d0 * d0 + 32.f * d1 * d1;
        }
        q = sum32(q);
        const float den = rbf(__builtin_amdgcn_sqrtf(rbf(rbf(q * (1.f / 640.f)) + 1e-5f)));
        c1 = -1.4426950408889634f * __builtin_amdgcn_rcpf(den); d2 = den * den;
        const u32 nm = pk2(-mean, -mean), dd = pk2(den, den);
        bcor = u32x4{h ? 0u : nm, h ? 0u : ((nm & 0xffffu) | (dd & 0xffff0000u)), h ? 0u : dd, 0u};
      }
      if (g == 12 && tid < 192) {
        const u32 t4 = L_GS + (tid >> 3) * 64 + (tid & 7) * 4;
        const float cs = (ldsf(t4) + ldsf(t4 + 1536)) + (ldsf(t4 + 3072) + ldsf(t4 + 4608));
        const float bi = (ldsf(t4 + 32) + ldsf(t4 + 1568)) + (ldsf(t4 + 3104) + ldsf(t4 + 4640));
        const u32 h0 = pk2(cs, bi);
        const float cr = cs - lo16(h0), br = bi - hi16(h0);
        const u32 h1 = pk2(cr, br), h2 = pk2(cr - lo16(h1), br - hi16(h1));
        const int gu = tid >= 96, nn = tid - 96 * gu;
        st128(L_T2 + (nn >> 5) * 1024 + gu * 512 + (nn & 31) * 16,
              u32x4{__builtin_amdgcn_perm(h1, h0, 0x05040100u), __builtin_amdgcn_perm(h0, h2, 0x07060100u), __builtin_amdgcn_perm(h2, h1, 0x07060302u), 0u});
      }
      if (g == 21 || g == 41) xwrite(pp - 1);
      if (g == 22 || g == 42) corr(pp - 1);
      if (g == 24 || g == 25) xadd(0, g - 24);
      if (g >= 28 && g <= 37 && (g - 28) % 3 == 0) epi(0, (g - 28) / 3, 2);
      if (g >= 26 && g < 37 && (g - 26) % 3 < 2) epi(0, (g - 26) / 3, (g - 26) % 3);
      if (g == 44 || g == 45) xadd(1, g - 44);
      if (g >= 48 && g <= 57 && (g - 48) % 3 == 0) epi(1, (g - 48) / 3, 2);
      if (g >= 46 && g < 57 && (g - 46) % 3 < 2) epi(1, (g - 46) / 3, (g - 46) % 3);
      SGB(0x100, 6, 0); SGB(0x008, 1, 0); SGB(0x002, 4, 0); SGB(0x008, 1, 0);
      SB();
      if constexpr (g < 3) sfor<6 + 4 * g, 10 + 4 * g>(iss);
      if constexpr (g == 3) sfor<18, 20>(iss);
      if constexpr (g + 1 < 20) nextA(IC<g + 1>{});
      if (g == 40) ostore(0);
      if constexpr (g >= 40 && g < 40 + NRS) gld_sc1<((g - 40) & 3) * 1024>(a[g - 40], rS.src + ((g - 40) >> 2) * 4096, l16);
      if constexpr (g >= 50 && g < 58) rD.template go<g - 50>(l16);
      if (g == 57) ostore(1);
      if constexpr ((g >= 16 && g < 18) || (g >= 22 && g < 26) || (g >= 32 && g < 38)) {
        if (old4) {
          if constexpr (g < 18) { rC.template go<2 * (g - 16)>(l16); rC.template go<2 * (g - 16) + 1>(l16); }
          else if constexpr (g < 26) { rA.template go<2 * (g - 22)>(l16); rA.template go<2 * (g - 22) + 1>(l16); }
          else { rB.template go<2 * (g - 32)>(l16); rB.template go<2 * (g - 32) + 1>(l16); }
        }
      }
      SB();
    });
    { const u32 lo_ = 65536 + tt * 4096, hi_ = (tt < 2 ? 106496 : 131072) + tt * 4096; sw = (kh ? hi_ : lo_) + l16; sr = (kh ? lo_ : hi_) + l16; }
    xwrite(2);
    corr(2);
    BAR();
    STAMP(11); FST(2, 17);
    xread(); xadd(2, 0); xadd(2, 1);
#pragma unroll
    for (int o = 0; o < 4; ++o)
#pragma unroll
      for (int ph = 0; ph < 3; ++ph) epi(2, o, ph);
    ostore(2);
    WAITV(8);
    BAR();
#pragma unroll
    for (int m = 0; m < NRS; ++m) { PIN(a[m]); st128(rS.lds + m * 1024 + l16, a[m]); }
    sfor<NRS, 12>([&](auto m_) LAM { rS.template go<decltype(m_)::v>(l16); });
    STAMP(12);
  }
  {
    PHASE();
    const bool old4 = w < 4;
    const char* wdb = ws + WS_WDF + (size_t)n8 * WDB;
    const int tt64 = mt * 4 + 2 * th + (tt >> 1);
    const char* ab = ws + WS_ACT + (size_t)(tt64 * 96 + 48 * kh) * 1024;
    auto iss = [&](auto s_) LAM { constexpr int s = decltype(s_)::v; gld_sc1<(s & 1) * 2048>(a[s], ab + (s >> 1) * 4096, avoff); };
#pragma unroll
    for (int m = 0; m < 5; ++m) c5[m] = f32x4{0.f, 0.f, 0.f, 0.f};
    const Run<50, 4, true> rE(wdb + 153600, 0, w & 3);
    const Run<40, 4, true> rF(wdb + 204800, 51200, w & 3);
    const u32 ew = 92160 + (tt * 5) * 1024 + l16;
    u32 oo = (u32)(128 * mt + 64 * th + 16 * tt + c16) * 1280 + (80 * n8 + 48 * kh + 4 * g4) * 2;
    u32 mo = L_MODS + 640 + kh * 192 + g4 * 16;
    PIN(oo); PIN(mo);
    gate(ab, ((c16 >> 1) * 6 + 4 + (c16 & 1)) * 1024 + (tt & 1) * 256);
    STAMP(13); CST(2, 18); FST(1, 1);
    sfor<0, 6>(iss);
    u32 wb2[5], wb3[5];
#pragma unroll
    for (int m = 0; m < 5; ++m) { wb2[m] = wbase[m] + 61440; wb3[m] = wbase[m] + 122880; PIN(wb2[m]); PIN(wb3[m]); }
    auto nextA = [&](auto s_) LAM {
      constexpr int s = decltype(s_)::v;
      if constexpr (dn_wait(s, true) == dn_wait(s, false)) WAITV(dn_wait(s, true)); else { if (old4) WAITV(dn_wait(s, true)); else WAITV(dn_wait(s, false)); }
      PIN(a[s]);
      ensure<(s & 1) * 2048>(a[s], ab + (s >> 1) * 4096, avoff);
    };
    BAR();
    u32x4 wf[3][5];
#pragma unroll
    for (int u = 0; u < 2; ++u)
#pragma unroll
      for (int m = 0; m < 5; ++m) wf[u][m] = lds128(wbase[m] + wd_slot(u));
    nextA(IC<0>{});
    sfor<0, 24>([&](auto s_) LAM {
      constexpr int s = decltype(s_)::v;
      if ((s & 1) == 0) FST(1, 2 + s / 2);
      if (s == 5 || s == 9 || s == 13 || s == 18) BAR();
      if constexpr (s + 2 < 24) {
        constexpr u32 so = wd_slot(s + 2);
#pragma unroll
        for (int m = 0; m < 5; ++m) wf[(s + 2) % 3][m] = lds128((so < 61440 ? wbase[m] : so < 122880 ? wb2[m] : wb3[m]) + so % 61440);
      }
#pragma unroll
      for (int m = 0; m < 5; ++m) c5[m] = MFMA16(wf[s % 3][m], a[s], c5[m]);
      SB();
      if constexpr (s >= 5 && s < 14) {
        if (old4) {
          if constexpr (s < 9) { sfor<0, 4>([&](auto u_) LAM { rE.template go<4 * (s - 5) + decltype(u_)::v>(l16); }); }
          else { rF.template go<2 * (s - 9)>(l16); rF.template go<2 * (s - 9) + 1>(l16); }
        }
      }
      sfor<dn_cumA(s), dn_cumA(s + 1)>(iss);
      if constexpr (s + 1 < 24) nextA(IC<s + 1>{});
      SB();
    });
    STAMP(14); CST(2, 19); FST(1, 14);
    stf4(ew + ((3 + 3 * kh) % 5) * 1024, c5[3]);
    stf4(ew + ((4 + 3 * kh) % 5) * 1024, c5[4]);
    if (kh) stf4(ew, c5[2]);
    BAR();
#pragma unroll
    for (int m = 0; m < 3; ++m)
      if (m < 2 || kh == 0) {
        const f32x4 o = ldsf4(ew + kh * 3072 + m * 1024), gb = ldsf4(mo + m * 64);
        const u32 y01 = pk2(c5[m][0] + o[0], c5[m][1] + o[1]), y23 = pk2(c5[m][2] + o[2], c5[m][3] + o[3]);
        const u32 t01 = pk2(gb[0] * lo16(y01), gb[1] * hi16(y01)), t23 = pk2(gb[2] * lo16(y23), gb[3] * hi16(y23));
        const u32x2 r = {pk2(lo16(x1k[m].x) + lo16(t01), hi16(x1k[m].x) + hi16(t01)), pk2(lo16(x1k[m].y) + lo16(t23), hi16(x1k[m].y) + hi16(t23))};
        *(GAS u32x2*)((GAS char*)p.out + oo + m * 32) = r;
      }
  }
}
}

extern "C" void kernel_launch(void* const* d_in, const int* in_sizes, int n_in, void* d_out, int out_size, void* d_ws, size_t ws_size,
                              hipStream_t stream) {
  (void)in_sizes; (void)n_in; (void)out_size; (void)ws_size;
  (void)hipFuncSetAttribute((const void*)k2, hipFuncAttributeMaxDynamicSharedMemorySize, LDS_BYTES);
  P1 q;
  q.x = (const u16*)d_in[0]; q.wqkv = (const u16*)d_in[1]; q.wo = (const u16*)d_in[2];
  q.cvec = (const u16*)d_in[7]; q.wmod = (const u16*)d_in[8]; q.ws = (char*)d_ws;
  k1<<<K1_BLOCKS, 256, 0, stream>>>(q);
  P2 p;
  p.x = (const u16*)d_in[0]; p.wg = (const u16*)d_in[4]; p.wu = (const u16*)d_in[5]; p.wd = (const u16*)d_in[6];
  p.out = (u16*)d_out; p.ws = (char*)d_ws;
  k2<<<256, 512, LDS_BYTES, stream>>>(p);
}
```

```cpp
#include <hip/hip_runtime.h>
#include <hip/hip_bf16.h>
#include <stdint.h>

#define GAS __attribute__((address_space(1)))
#define LAS __attribute__((address_space(3)))
#if defined(__HIP_DEVICE_COMPILE__)
#define NOPK __attribute__((target("no-packed-fp32-ops")))
#else
#define NOPK
#endif
#define DI __device__ __forceinline__ NOPK
#define LAM __attribute__((always_inline)) NOPK
typedef unsigned int u32;
typedef unsigned short u16;
typedef u32 u32x4 __attribute__((ext_vector_type(4)));
typedef u32 u32x2 __attribute__((ext_vector_type(2)));
typedef float f32x2 __attribute__((ext_vector_type(2)));
typedef float f32x4 __attribute__((ext_vector_type(4)));
typedef float f32x16 __attribute__((ext_vector_type(16)));
typedef __bf16 bf16x2 __attribute__((ext_vector_type(2)));
typedef __bf16 bf16x4 __attribute__((ext_vector_type(4)));
typedef __bf16 bf16x8 __attribute__((ext_vector_type(8)));
typedef GAS u32 gu32;
typedef _Float16 f16x8 __attribute__((ext_vector_type(8)));
typedef _Float16 f16x2 __attribute__((ext_vector_type(2)));

#define STAMPS 0

namespace {
constexpr int S = 2048, D = 640, F = 1536;
constexpr u32 WS_EP = 0;
constexpr u32 WS_OF = 1024;
constexpr u32 WS_RG = 4096;
constexpr u32 WS_RD = 6144;
constexpr u32 WS_FL = 8192;
constexpr u32 WS_KVF = 40960;
constexpr u32 WS_MODP = 81920;
constexpr u32 WS_CSP = WS_MODP + 61440;
constexpr u32 WS_STAT = WS_CSP + 16 * 16 * 1536;
constexpr u32 WS_N1 = WS_STAT + 64 * 8 * 32 * 16;
constexpr u32 WS_OAF = WS_N1 + 64 * 40 * 1024;
constexpr u32 WS_X1 = WS_OAF + 64 * 40 * 1024;
constexpr u32 WS_ACT = WS_X1 + 64 * 40 * 1024;
constexpr u32 KVB = 24576;
constexpr u32 WS_KVX = WS_ACT + 64 * 96 * 1024;
constexpr u32 WQB = 153600;
constexpr u32 WS_WQF = WS_KVX + 256 * KVB;
constexpr u32 WOB = 102400, WGB = 245760, WDB = 245760;
constexpr u32 WS_WOF = WS_WQF + 16 * WQB;
constexpr u32 WS_WGF = WS_WOF + 8 * WOB;
constexpr u32 WS_WDF = WS_WGF + 16 * WGB;
constexpr u32 WS_DBG = WS_WDF + 8 * WDB;
constexpr u32 WS_DBG1 = WS_DBG + 256 * 32 * 8;
constexpr u32 WS_END = WS_DBG1 + 1024 * 16;
constexpr u32 L_MISC = 153600;
constexpr u32 L_GS = L_MISC;
constexpr u32 L_MODS = L_MISC + 6144;
constexpr u32 L_T2 = L_MISC + 7168;
constexpr u32 L_FLAG = L_MISC + 9728;
constexpr u32 LDS_BYTES = 163840;
constexpr u32 L_K = 0, L_V = 30720;
constexpr float QSCALE = 0.22360679774997896f * 1.4426950408889634f;
constexpr int K1_BLOCKS = 0;
#define ATT_LIMIT 1.0e18f
constexpr int K2_LINES = 384;
constexpr u32 SPIN_CAP = 1u << 20;

DI float u2f(u32 v) { return __builtin_bit_cast(float, v); }
DI u32 f2u(float v) { return __builtin_bit_cast(u32, v); }
DI int tidx() { return (int)__builtin_amdgcn_workitem_id_x(); }
DI int bidx() { return (int)__builtin_amdgcn_workgroup_id_x(); }
DI float bf2f(u32 v) { return u2f(v << 16); }
DI float lo16(u32 v) { return u2f(v << 16); }
DI float hi16(u32 v) { return u2f(v & 0xffff0000u); }
DI u32 pk2(float a, float b) { f32x2 v = {a, b}; return __builtin_bit_cast(u32, __builtin_convertvector(v, bf16x2)); }
DI u32 pkh2(float a, float b) { f32x2 v = {a, b}; return __builtin_bit_cast(u32, __builtin_convertvector(v, f16x2)); }
DI float hlo(u32 v) { return (float)__builtin_bit_cast(f16x2, v).x; }
DI float hhi(u32 v) { return (float)__builtin_bit_cast(f16x2, v).y; }
DI float rbf(float a) { return lo16(pk2(a, a)); }
DI float fma1(float a, float b, float c) { return __builtin_fmaf(a, b, c); }
DI float mul1(float a, float b) { return a * b; }
DI float add1(float a, float b) { return a + b; }
DI float max3(float a, float b, float c) { return __builtin_fmaxf(__builtin_fmaxf(a, b), c); }
DI float xor32f(float v) {
  auto r = __builtin_amdgcn_permlane32_swap(f2u(v), f2u(v), false, false);
  return u2f((tidx() & 32) ? r[0] : r[1]);
}
DI float sum32(float v) { auto r = __builtin_amdgcn_permlane32_swap(f2u(v), f2u(v), false, false); return u2f(r[0]) + u2f(r[1]); }
DI float sum16(float v) { auto r = __builtin_amdgcn_permlane16_swap(f2u(v), f2u(v), false, false); return u2f(r[0]) + u2f(r[1]); }
DI float max32(float v) { auto r = __builtin_amdgcn_permlane32_swap(f2u(v), f2u(v), false, false); return __builtin_fmaxf(u2f(r[0]), u2f(r[1])); }
template <int CTRL> DI float dppf(float v) { return u2f(__builtin_amdgcn_update_dpp(0u, f2u(v), CTRL, 0xf, 0xf, true)); }

#define MFMA32(a, b, c) __builtin_amdgcn_mfma_f32_32x32x16_bf16(__builtin_bit_cast(bf16x8, a), __builtin_bit_cast(bf16x8, b), c, 0, 0, 0)
#define MFMA32H(a, b, c) __builtin_amdgcn_mfma_f32_32x32x16_f16(__builtin_bit_cast(f16x8, a), __builtin_bit_cast(f16x8, b), c, 0, 0, 0)
#define MFMA16(a, b, c) __builtin_amdgcn_mfma_f32_16x16x32_bf16(__builtin_bit_cast(bf16x8, a), __builtin_bit_cast(bf16x8, b), c, 0, 0, 0)
#define SGB(m, n, id) __builtin_amdgcn_sched_group_barrier(m, n, id)
#define SB() __builtin_amdgcn_sched_barrier(0)
template <bool B> struct BoolC { static constexpr bool v = B; };
template <int I> struct IC { static constexpr int v = I; };
template <int I, int N, class Fn> DI void sfor(Fn&& f) { if constexpr (I < N) { f(IC<I>{}); sfor<I + 1, N>(f); } }
#define PIN(x) asm volatile("" : "+v"(x))
#define WAITV(n) asm volatile("s_waitcnt vmcnt(%0)" ::"i"(n))
#define WAITVM(n) asm volatile("s_waitcnt vmcnt(%0)" ::"i"(n) : "memory")
#define BAR() asm volatile("s_waitcnt lgkmcnt(0)\n\ts_barrier" ::: "memory")
#define RLX_AGENT __ATOMIC_RELAXED, __HIP_MEMORY_SCOPE_AGENT

DI u32x4 lds128(u32 a) { return *(const LAS u32x4*)(size_t)a; }
DI f32x4 ldsf4(u32 a) { return *(const LAS f32x4*)(size_t)a; }
DI u32x2 lds64(u32 a) { return *(const LAS u32x2*)(size_t)a; }
DI float ldsf(u32 a) { return *(const LAS float*)(size_t)a; }
DI void st128(u32 a, u32x4 v) { *(LAS u32x4*)(size_t)a = v; }
DI void stf4(u32 a, f32x4 v) { *(LAS f32x4*)(size_t)a = v; }
DI void st64(u32 a, u32x2 v) { *(LAS u32x2*)(size_t)a = v; }
DI u32x2 ldstr(u32 a) { return __builtin_bit_cast(u32x2, __builtin_amdgcn_ds_read_tr16_b64_v4bf16((LAS bf16x4*)(size_t)a)); }
DI u32x4 ldstr2(u32 a, u32 b) { u32x2 lo = ldstr(a), hi = ldstr(b); return u32x4{lo.x, lo.y, hi.x, hi.y}; }

template <int IMM, bool SC1> DI void dmai(const char* sbase, u32 voff, u32 lds) {
  if constexpr (SC1) asm volatile("s_mov_b32 m0, %2\n\ts_nop 0\n\tglobal_load_lds_dwordx4 %0, %1 offset:%3 sc1" ::"v"(voff), "s"(sbase), "s"(lds), "i"(IMM));
  else asm volatile("s_mov_b32 m0, %2\n\ts_nop 0\n\tglobal_load_lds_dwordx4 %0, %1 offset:%3" ::"v"(voff), "s"(sbase), "s"(lds), "i"(IMM));
}
DI void dma(const char* sbase, u32 voff, u32 lds) { dmai<0, false>(sbase, voff, lds); }
DI void gld32(u32& r, const char* sbase, u32 voff) { asm volatile("global_load_dword %0, %1, %2" : "=v"(r) : "v"(voff), "s"(sbase)); }
DI void gld32_sc1(u32& r, const char* sbase, u32 voff) { asm volatile("global_load_dword %0, %1, %2 sc1" : "=v"(r) : "v"(voff), "s"(sbase)); }
template <int IMM> DI void gld_sc1(u32x4& r, const char* sbase, u32 voff) {
  asm volatile("global_load_dwordx4 %0, %1, %2 offset:%3 sc1" : "=v"(r) : "v"(voff), "s"(sbase), "i"(IMM));
}
template <int IMM> DI void gld(u32x4& r, const char* sbase, u32 voff) {
  asm volatile("global_load_dwordx4 %0, %1, %2 offset:%3" : "=v"(r) : "v"(voff), "s"(sbase), "i"(IMM));
}
DI void gld64(u32x2& r, const char* sbase, u32 voff) {
  asm volatile("global_load_dwordx2 %0, %1, %2" : "=v"(r) : "v"(voff), "s"(sbase));
}
DI void poll_ge(const char* ws, u32 off, u32 want) {
  gu32* p = (gu32*)(ws + off);
  for (u32 spins = 0; spins < SPIN_CAP; ++spins) {
    if ((u32)__builtin_amdgcn_readfirstlane(__hip_atomic_load(p, RLX_AGENT)) >= want) break;
    __builtin_amdgcn_s_sleep(1);
  }
  __builtin_amdgcn_fence(__ATOMIC_ACQUIRE, "wavefront");
}
DI void signal(const char* ws, u32 off) { __hip_atomic_fetch_add((gu32*)(ws + off), 1u, RLX_AGENT); }

struct P2 {
  const u16 *x, *wqkv, *wo, *wg, *wu, *wd, *cvec, *wmod;
  u16* out;
  char* ws;
};
#define MAP 0
#define OPQ(x) asm volatile("" : "+s"(x))

DI constexpr int gu_unit(int p, int kh, int i) {
  return p == 0 ? 20 * kh + i
       : p == 1 ? (i < 16 ? 40 + 16 * kh + i : 8 + 20 * kh + (i - 16))
                : (i < 8 ? 12 + 20 * kh + i : 40 + 16 * kh + (i - 8));
}
DI constexpr u32 wo_slot(int s) { return s < 7 ? (72 + 10 * s) * 1024 : (s - 7) * 10240; }
DI constexpr u32 wd_slot(int s) { return (s < 15 ? s : s - 15) * 10240; }
template <int N, int NW, bool SC1> struct Run {
  static constexpr int C = (N + NW - 1) / NW;
  const char* src; u32 lds;
  DI Run(const char* s, u32 l, int x) { int o = x * C; o = (o < N - C ? o : N - C) * 1024; src = s + o; lds = l + o; }
  template <int M> DI void go(u32 l16) const { if constexpr (M < C) dmai<(M & 3) * 1024, SC1>(src + (M >> 2) * 4096, l16, lds + (M >> 2) * 4096); }
  DI void all(u32 l16) const { sfor<0, C>([&](auto m_) LAM { go<decltype(m_)::v>(l16); }); }
};
template <int P, int I0, int I1, int NW> DI auto gu_run(const char* blob, int x) {
  const int k = x >= NW / 2;
  return Run<2 * (I1 - I0), NW / 2, true>(blob + ((P * 2 + k) * 20 + I0) * 2048, (k ? gu_unit(P, 1, I0) : gu_unit(P, 0, I0)) * 2048, x - k * (NW / 2));
}
DI u32 pkfma16(u32 x, u32 y, u32 z) { return __builtin_bit_cast(u32, __builtin_elementwise_fma(__builtin_bit_cast(f16x2, x), __builtin_bit_cast(f16x2, y), __builtin_bit_cast(f16x2, z))); }
DI void gld4_if(int cond, u32x4& r0, u32x4& r1, u32x4& r2, u32x4& r3, const char* sbase, u32 voff) {
  asm volatile("s_cmp_eq_u32 %6, 0\n\ts_cbranch_scc1 .Lskip%=\n\tglobal_load_dwordx4 %0, %4, %5 sc1\n\tglobal_load_dwordx4 %1, %4, %5 offset:1024 sc1\n\t"
               "global_load_dwordx4 %2, %4, %5 offset:2048 sc1\n\tglobal_load_dwordx4 %3, %4, %5 offset:3072 sc1\n.Lskip%=:"
               : "+v"(r0), "+v"(r1), "+v"(r2), "+v"(r3) : "v"(voff), "s"(sbase), "s"(cond) : "scc");
}
#define NRS 12
DI constexpr int dn_nA(int s) { return s < 3 ? 3 : s == 8 || s == 10 || s == 13 ? 3 : 0; }
DI constexpr int dn_nD(int s) { return s >= 5 && s < 8 ? 4 : s == 8 ? 1 : s >= 9 && s < 14 ? 2 : 0; }
DI constexpr int dn_cumA(int s) { int c = 6; for (int u = 0; u < s; ++u) c += dn_nA(u); return c; }
DI constexpr int dn_young(int s, bool old, int a_idx, int d_step) {
  int pos = 0, tgt = -1, na = 6;
  if (a_idx >= 0 && a_idx < 6) tgt = a_idx;
  pos = 6;
  for (int u = 0; u < s; ++u) {
    if (old) { pos += dn_nD(u); if (u == d_step) tgt = pos - 1; }
    for (int q = 0; q < dn_nA(u); ++q) { if (na == a_idx) tgt = pos; ++na; ++pos; }
  }
  return tgt < 0 ? 63 : pos - 1 - tgt;
}
DI constexpr int dn_wait(int s, bool old) {
  const int x = dn_young(s, old, s, -1), y = s == 13 ? dn_young(s, old, -1, 8) : s == 18 ? dn_young(s, old, -1, 13) : 63;
  return x < y ? x : y;
}
#define OWARM 0
DI void gld64_sc1(u32x2& r, const char* sbase, u32 voff) { asm volatile("global_load_dwordx2 %0, %1, %2 sc1" : "=v"(r) : "v"(voff), "s"(sbase)); }
DI bool flags_set(const u32x2& v, u32 e1, u32 e2) { return __builtin_amdgcn_ballot_w64(v.x != e1 || v.y != e2) == 0ull; }
DI void wait_flags(const char* ws, u32 voff, u32 e1, u32 e2) {
  for (u32 spins = 0; spins < SPIN_CAP; ++spins) {
    u32x2 v;
    gld64_sc1(v, ws, voff); WAITV(0); PIN(v);
    if (flags_set(v, e1, e2)) break;
    __builtin_amdgcn_s_sleep(1);
  }
  asm volatile("" ::: "memory");
}
#define RLATE 0
#define FILLP0 1
DI bool has_sent(const u32x4& v) {
  const u32 m = __builtin_elementwise_max(__builtin_elementwise_max(v.x, v.y), __builtin_elementwise_max(v.z, v.w));
  return __builtin_amdgcn_ballot_w64(m == 0xffffffffu) != 0ull;
}
template <int IMM> DI void ensure(u32x4& v, const char* sbase, u32 voff) {
  if (__builtin_expect(has_sent(v), 0)) {
    for (u32 spins = 0; spins < SPIN_CAP; ++spins) {
      gld_sc1<IMM>(v, sbase, voff); WAITV(0); PIN(v);
      if (!has_sent(v)) break;
    }
  }
}
DI void gate(const char* sbase, u32 voff) {
  for (u32 spins = 0; spins < SPIN_CAP; ++spins) {
    u32 v;
    gld32_sc1(v, sbase, voff); WAITV(0); PIN(v);
    if (__builtin_amdgcn_ballot_w64(v == 0xffffffffu) == 0ull) break;
  }
}

__global__ void __launch_bounds__(512, 2) NOPK k2(P2 p) {
  const int tid = tidx(), lane = tid & 63;
  int w = __builtin_amdgcn_readfirstlane(tid >> 6);
  const int b = bidx();
  int mt = MAP == 0 ? 4 * ((b & 7) >> 1) + (b >> 6) : (b & 7) * 2 + (b >> 7), j = MAP == 0 ? 8 * (b & 1) + ((b >> 3) & 7) : (b >> 3) & 15;
  const int c32 = lane & 31, h = lane >> 5;
  const char* ws = p.ws;
  const __amdgpu_buffer_rsrc_t rs = __builtin_amdgcn_make_buffer_rsrc(p.ws, 0, WS_END, 0x00020000);
  const u32 l16 = lane * 16;
#define PHASE() OPQ(w); OPQ(mt); OPQ(j); OPQ(ws); const int tt = w & 3, kh = w >> 2, th = j & 1, n8 = j >> 1; (void)tt; (void)kh; (void)th; (void)n8
#define FINE 0
#define CSEL 0
#define FST(k, i) do {} while (0)
#define STAMP(i) do {} while (0)
#define CST(k, i) do {} while (0)
#define PRIO 0
  if (PRIO == 1 && w >= 4) __builtin_amdgcn_s_setprio(1);
  STAMP(0); CST(3, 18);
  u32x4 a[24];
  u32x4 st[18], ro[3];
  const int rsw = c32 ^ (16 * (w >> 2));
  const u32 frow = (h * 32 + rsw) * 16;
  u32 imga[8];
  u32 e1, e2;
  {
    PHASE();
    u32 e0;
    gld32_sc1(e0, ws, WS_EP);
    const int rk16 = w < 5 ? 5 * (mt >> 1) + w : (mt * 2 + th) * 3 + (w - 5);
    const u32 rimg = 122880 + w * 3072;
    auto issR = [&]() LAM {
      const char* rsrc = w < 5 ? (const char*)((mt & 1) ? p.wu : p.wg) + (size_t)rk16 * 49152 + 192 * j : (const char*)p.wd + (size_t)rk16 * 20480 + 160 * n8;
#pragma unroll
      for (int m = 0; m < 3; ++m) {
        const u32 x = 64 * m + lane, i5 = x < 160 ? x : 159, r12 = (x * 171) >> 11, r10 = (i5 * 205) >> 11;
        dma(rsrc, w < 5 ? r12 * 2880 + x * 16 : r10 * 1120 + i5 * 16, rimg + m * 1024);
      }
    };
    if (tid == 0) st128(L_FLAG, u32x4{0u, 0u, 0u, 0u});
    const u32 lc = lane < 40 ? lane : 39;
    gld<0>(a[8], (const char*)p.x, (u32)(128 * mt + 8 * j + w) * 1280 + lc * 16); gld<640>(a[9], (const char*)p.x, (u32)(128 * mt + 8 * j + w) * 1280 + lc * 16);
    auto lnorm = [&]() LAM {
      PIN(a[8]); PIN(a[9]);
      float xv[16];
#pragma unroll
      for (int e = 0; e < 8; ++e) { const u32 v = a[8 + (e >> 2)][e & 3]; xv[2 * e] = lo16(v); xv[2 * e + 1] = hi16(v); }
      auto wsum = [&](float v) LAM -> float {
        v = lane < 40 ? v : 0.f;
        v += dppf<0xB1>(v); v += dppf<0x4E>(v); v += dppf<0x141>(v); v += dppf<0x140>(v);
        return sum32(sum16(v));
      };
      float s0 = 0.f, s1 = 0.f;
#pragma unroll
      for (int e = 0; e < 8; ++e) { s0 += xv[2 * e]; s1 += xv[2 * e + 1]; }
      const float mean = wsum(s0 + s1) * (1.f / 640.f);
      float q0 = 0.f, q1 = 0.f;
#pragma unroll
      for (int e = 0; e < 8; ++e) { const float d0 = xv[2 * e] - mean, d1 = xv[2 * e + 1] - mean; q0 = fma1(d0, d0, q0); q1 = fma1(d1, d1, q1); }
      const float q = wsum(q0 + q1);
      const float mb = rbf(mean), den = rbf(__builtin_amdgcn_sqrtf(rbf(rbf(q * (1.f / 640.f)) + 1e-5f))), rr = __builtin_amdgcn_rcpf(den);
#pragma unroll
      for (int i = 0; i < 2; ++i) {
        u32x4 o;
#pragma unroll
        for (int e = 0; e < 4; ++e) {
          o[e] = pkh2((xv[8 * i + 2 * e] - mb) * rr, (xv[8 * i + 2 * e + 1] - mb) * rr);
        }
        st128(20480 + (lc + 40 * i) * 128 + w * 16, o);
      }
    };
    if (w >= 1 && w < 4 && b < 240) {
      const char* src = b < 160 ? (const char*)p.wqkv + (size_t)(b >> 2) * 61440 + (b & 3) * 320 : (const char*)p.wo + (size_t)((b - 160) >> 1) * 20480 + (b & 1) * 640;
#pragma unroll
      for (int m = 0; m < 5; ++m) {
        const u32 x = (w - 1) * 320 + m * 64 + lane, r60 = (x * 1093) >> 16, ch = x - 60 * r60, sg = (ch * 205) >> 12;
        const u32 xo = x < 640 ? x : 639, r40 = (xo * 1639) >> 16;
        dma(src, b < 160 ? r60 * 3840 + sg * 960 + ch * 16 : r40 * 640 + xo * 16, ((w - 1) * 5 + m) * 1024);
      }
    }
    u32 cv[5];
    {
      const int bq = b < 240 ? b : 239, row = 160 * (bq & 3) + 40 * (w & 3) + (lane >> 3);
      const char* wm = (const char*)p.wmod + (bq >> 2) * 128;
      const u32 vo = (u32)row * 7680 + (lane & 7) * 16;
      asm volatile("s_cmp_eq_u32 %14, 0\n\ts_cbranch_scc1 .Lskip%=\n\t"
                   "global_load_ushort %0, %10, %11\n\tglobal_load_ushort %1, %10, %11 offset:16\n\tglobal_load_ushort %2, %10, %11 offset:32\n\t"
                   "global_load_ushort %3, %10, %11 offset:48\n\tglobal_load_ushort %4, %10, %11 offset:64\n\t"
                   "global_load_dwordx4 %5, %12, %13\n\tglobal_load_dwordx4 %6, %15, %13\n\tglobal_load_dwordx4 %7, %16, %13\n\t"
                   "global_load_dwordx4 %8, %17, %13\n\tglobal_load_dwordx4 %9, %18, %13\n.Lskip%=:"
                   : "=&v"(cv[0]), "=&v"(cv[1]), "=&v"(cv[2]), "=&v"(cv[3]), "=&v"(cv[4]), "=&v"(a[0]), "=&v"(a[1]), "=&v"(a[2]), "=&v"(a[3]), "=&v"(a[4])
                   : "v"((u32)row * 2), "s"(p.cvec), "v"(vo), "s"(wm), "s"(kh), "v"(vo + 61440), "v"(vo + 122880), "v"(vo + 184320), "v"(vo + 245760) : "scc");
    }
    if (w == 0 && b >= 240) {
      unsigned long long pc;
      asm volatile("s_getpc_b64 %0" : "=s"(pc));
      u32 cvx[K2_LINES / 128];
#pragma unroll
      for (int i = 0; i < K2_LINES / 128; ++i) gld32(cvx[i], (const char*)(pc & ~127ull) + (i * 2 + ((b >> 3) & 1)) * 8192, lane * 128);
      WAITV(0);
#pragma unroll
      for (int i = 0; i < K2_LINES / 128; ++i) PIN(cvx[i]);
    }
    WAITV(0);
#pragma unroll
    for (int i = 0; i < 5; ++i) { PIN(cv[i]); PIN(a[i]); }
    lnorm();
    if (kh) {
      float ac[8] = {0.f, 0.f, 0.f, 0.f, 0.f, 0.f, 0.f, 0.f};
#pragma unroll
      for (int i = 0; i < 5; ++i) {
        const float c = lo16(cv[i]), sv = rbf(c * __builtin_amdgcn_rcpf(1.f + __builtin_amdgcn_exp2f(-1.4426950408889634f * c)));
#pragma unroll
        for (int e = 0; e < 4; ++e) { ac[2 * e] = fma1(sv, lo16(a[i][e]), ac[2 * e]); ac[2 * e + 1] = fma1(sv, hi16(a[i][e]), ac[2 * e + 1]); }
      }
      float r4[4], r2[2];
#pragma unroll
      for (int e = 0; e < 4; ++e) { auto r = __builtin_amdgcn_permlane32_swap(f2u(ac[e]), f2u(ac[4 + e]), false, false); r4[e] = u2f(r[0]) + u2f(r[1]); }
#pragma unroll
      for (int e = 0; e < 2; ++e) { auto r = __builtin_amdgcn_permlane16_swap(f2u(r4[e]), f2u(r4[2 + e]), false, false); r2[e] = u2f(r[0]) + u2f(r[1]); r2[e] += dppf<0x128>(r2[e]); }
      st64(16384 + (w - 4) * 256 + (lane & 7) * 32 + h * 16 + ((lane >> 4) & 1) * 8, u32x2{f2u(r2[0]), f2u(r2[1])});
    }
    PIN(e0);
    e1 = __builtin_amdgcn_readfirstlane(e0) + 1u; e2 = e1 * 2654435761u + 0x9e3779b9u;
    BAR();
    FST(8, 1);
    const u32 q4 = (lane >> 2) & 3, nh = (lane >> 4) & 1, p4 = lane & 3;
    if (b < 160) {
#pragma unroll
      for (int u = 0; u < 2; ++u) {
        const int jj = w >> 1, nb = 2 * (w & 1) + u;
        const u32 n = 32 * nb + 16 * nh + 4 * p4, nc = n < 120 ? n : 116, col = nc < 80 ? 320 + (nc < 40 ? nc * 2 : 240 + nc * 2) : nc * 2 - 160;
        const u32 ra = (8 * h + q4) * 960 + jj * 80 + col;
        const u32x4 v = ldstr2(ra, ra + 3840);
        u32x4 o;
#pragma unroll
        for (int e = 0; e < 4; ++e) { const u32 x = v[e]; o[e] = pkh2(lo16(x) * 64.f, hi16(x) * 64.f); }
        const u32 dst = WS_WQF + (u32)(4 * (b & 3) + jj) * WQB + (nb < 3 ? (nb * 40 + (b >> 2)) * 1024 + l16 : (c32 < 24 ? 122880 + (b >> 2) * 768 + (h * 24 + c32) * 16 : 0xf0000000u));
        __builtin_amdgcn_raw_buffer_store_b128(o, rs, dst, 0, 16);
      }
    } else if (b < 240) {
      const int k16 = (b - 160) >> 1, k2 = k16 >= 20, kk = k16 - 20 * k2;
#pragma unroll
      for (int u = 0; u < 2; ++u) {
        if (u == 0 || w < 2) {
          const u32 nt2 = 2 * (w + 8 * u) + h, ra = (8 * nh + q4) * 640 + nt2 * 32 + p4 * 8;
          const u32 ntg = 20 * (b & 1) + nt2, n5 = (ntg * 52) >> 8;
          __builtin_amdgcn_raw_buffer_store_b128(ldstr2(ra, ra + 2560), rs, WS_WOF + n5 * WOB + ((kk >> 1) * 10 + k2 * 5 + ntg - 5 * n5) * 1024 + (kk & 1) * 512 + c32 * 16, 0, 16);
        }
      }
    }
    {
      const u32 tok = 128 * mt + 8 * j, dst = WS_N1 + (tok >> 5) * 40960 + ((tok & 31) + (lane & 7)) * 16 + (lane >> 3) * 512 + w * 4096;
      __builtin_amdgcn_raw_buffer_store_b128(lds128(20480 + w * 1024 + l16), rs, dst, 0, 16);
      if (w < 2) __builtin_amdgcn_raw_buffer_store_b128(lds128(28672 + w * 1024 + l16), rs, dst + 32768, 0, 16);
    }
    if (w == 7 && b < 240) {
      const float sv = (ldsf(16384 + lane * 4) + ldsf(16640 + lane * 4)) + (ldsf(16896 + lane * 4) + ldsf(17152 + lane * 4));
      __builtin_amdgcn_raw_buffer_store_b32(f2u(sv), rs, WS_MODP + ((b & 3) * 3840 + (b >> 2) * 64 + lane) * 4, 0, 16);
    }
    WAITVM(0);
    BAR();
    FST(8, 2);
    if (tid < 16) __builtin_amdgcn_raw_buffer_store_b64(u32x2{e1, e2}, rs, WS_FL + tid * 2048 + b * 8, 0, 16);
    if (w != 7) { __builtin_amdgcn_s_sleep(2); issR(); }
    if (FILLP0 && w >= 2 && w <= 5) {
      const int dw = w - 2;
      const u32x4 f = {~0u, ~0u, ~0u, ~0u};
      const u32 fa = WS_ACT + (u32)((mt * 4 + dw) * 96 + 6 * j) * 1024 + l16;
      const u32 fx = dw < 2 ? WS_X1 + (u32)((mt * 4 + 2 * th + dw) * 40 + 5 * n8) * 1024 + l16 : WS_STAT + (u32)((mt * 4 + 2 * th + h) * 8 + n8) * 512 + c32 * 16;
#pragma unroll
      for (int m = 0; m < 6; ++m) __builtin_amdgcn_raw_buffer_store_b128(f, rs, fa + m * 1024, 0, 16);
#pragma unroll
      for (int m = 0; m < 5; ++m) if (dw < 2 || (dw == 2 && m == 0)) __builtin_amdgcn_raw_buffer_store_b128(f, rs, fx + m * 1024, 0, 16);
    }
    if (w == 7) {
      for (u32 spins = 0; spins < SPIN_CAP; ++spins) {
        u32x4 f0, f1;
        gld_sc1<0>(f0, ws + WS_FL + mt * 2048, l16); gld_sc1<1024>(f1, ws + WS_FL + mt * 2048, l16);
        WAITV(0); PIN(f0); PIN(f1);
        if (__builtin_amdgcn_ballot_w64(((f0.x ^ e1) | (f0.z ^ e1) | (f1.x ^ e1) | (f1.z ^ e1) | (f0.y ^ e2) | (f0.w ^ e2) | (f1.y ^ e2) | (f1.w ^ e2)) != 0u) == 0ull) break;
        __builtin_amdgcn_s_sleep(2);
      }
      if (RLATE == 0) issR();
    }
    BAR();
    STAMP(18); FST(8, 3);
  }
  {
    PHASE();
    const char* wq = ws + WS_WQF + (size_t)j * WQB;
    const char* wqw = wq + (kh * 20 + tt) * 1024;
    const u32 wql = (kh * 20 + tt) * 1024;
    const char* n1 = ws + WS_N1 + (size_t)((mt * 4 + tt) * 40 + kh * 20) * 1024;
    auto issA = [&](auto k_) LAM { constexpr int k = decltype(k_)::v; gld_sc1<(k & 3) * 1024>(a[k], n1 + (k >> 2) * 4096, l16); };
    auto issW = [&](int s, int nb) LAM { dmai<0, true>(wqw + nb * 40960 + s * 4096, l16, wql + nb * 40960 + s * 4096); };
    u32x4 mp[4];
    u32 mdst; bool plus1;
    {
      const int m1 = tid >= 160, k1 = 4 * (tid - 160 * m1);
      const int u2 = tid - 320, m2 = (u2 * 205) >> 12, e2 = u2 - 20 * m2;
      const int col2 = (m2 == 0 ? 1280 : m2 == 2 ? 3200 : m2 == 4 ? 1920 : 2560) + 80 * (m2 < 3 ? n8 : (mt >> 1)) + 4 * (m2 < 5 ? e2 : 0);
      const int col = tid < 320 ? 640 * m1 + k1 : col2;
#pragma unroll
      for (int q = 0; q < 4; ++q) gld_sc1<0>(mp[q], ws + WS_MODP + q * 15360, (u32)col * 4);
      sfor<0, 4>(issA);
      issW(0, 0); issW(0, 1); issW(0, 2);
      mdst = tid < 320 ? L_GS + (k1 >> 3) * 32 + (m1 ? 0 : 16) + (k1 & 4) * 2 : L_MODS + u2 * 16;
      plus1 = tid < 320 ? m1 != 0 : (m2 & 1) != 0;
    }
    const int rk16 = w < 5 ? 5 * (mt >> 1) + w : (mt * 2 + th) * 3 + (w - 5);
    f32x16 acc[3];
    const u32 wb = kh * 20480 + frow, gb_ = L_GS + kh * 1280 + h * 32;
    u32x4 gs[3][2], wf[2][3];
    f32x4 csum = {0.f, 0.f, 0.f, 0.f};
    auto ldgs = [&](int k) LAM { gs[k % 3][0] = lds128(gb_ + k * 64); gs[k % 3][1] = lds128(gb_ + k * 64 + 16); };
    auto modulate = [&](int k) LAM {
      PIN(a[k]);
#pragma unroll
      for (int e = 0; e < 4; ++e)
        a[k][e] = pkfma16(a[k][e], gs[k % 3][0][e], gs[k % 3][1][e]);
    };
    FST(3, 1);
    WAITV(7);
    FST(3, 2);
    f32x4 mv;
    {
#pragma unroll
      for (int q = 0; q < 4; ++q) PIN(mp[q]);
      float mxv = 0.f;
#pragma unroll
      for (int e = 0; e < 4; ++e) {
        const float s = rbf((u2f(mp[0][e]) + u2f(mp[1][e])) + (u2f(mp[2][e]) + u2f(mp[3][e])));
        mv[e] = plus1 ? rbf(1.f + s) : s;
        mxv = __builtin_fmaxf(mxv, __builtin_fabsf(mv[e]));
      }
      if (tid < 320) st64(mdst, u32x2{pkh2(mv[0], mv[1]), pkh2(mv[2], mv[3])}); else if (tid < 420) stf4(mdst, mv);
      const bool big = __builtin_amdgcn_ballot_w64(tid < 320 && !(mxv < 2000.f)) != 0ull;
      if (lane == 0) *(LAS u32*)(size_t)(L_FLAG + 16 + w * 4) = big;
    }
    WAITV(0);
    BAR();
    FST(3, 3);
    auto grpA = [&](int cond, int s) LAM { gld4_if(cond, a[4 * s], a[4 * s + 1], a[4 * s + 2], a[4 * s + 3], n1 + s * 4096, l16); };
    const int kl = kh ^ 1;
    auto grpW = [&](int s) LAM { issW(s, 0); issW(s, 1); issW(s, 2); };
    grpA(kh, 1); if (kh) grpW(1);
    bool scaled;
    {
      const u32x4 f0 = lds128(L_FLAG + 16);
      const u32 f1 = *(const LAS u32*)(size_t)(L_FLAG + 32);
      ldgs(0); ldgs(1);
      scaled = __builtin_amdgcn_readfirstlane(f0.x | f0.y | f0.z | f0.w | f1) != 0;
      if (scaled) {
        BAR();
        const float sc = 9.765625e-4f;
        if (tid < 320) st64(mdst, u32x2{pkh2(mv[0] * sc, mv[1] * sc), pkh2(mv[2] * sc, mv[3] * sc)});
        BAR();
        ldgs(0); ldgs(1);
      }
    }
    modulate(0);
    SB();
    const Run<30, 8, true> rb3(wq + 122880, 122880, w);
    sfor<0, 21>([&](auto k_) LAM {
      constexpr int k = decltype(k_)::v, s = k >> 2, i = k & 3;
      if constexpr (i == 3 && k < 16) WAITV(10);
      if constexpr (i == 0 && k > 0 && k < 20) {
        FST(3, 3 + 3 * s); WAITV(k == 16 ? 4 : 7); FST(3, 4 + 3 * s); BAR(); FST(3, 5 + 3 * s);
        if (s == 2 && CSEL == 4) { CST(4, 18); STAMP(16); } if (s == 3 && CSEL == 4) { CST(4, 19); STAMP(17); }
      }
      if constexpr (k < 20) {
#pragma unroll
        for (int nb = 0; nb < 3; ++nb) wf[k & 1][nb] = lds128(wb + nb * 40960 + k * 1024);
      }
      if constexpr (k == 1) {
        f32x16 z;
#pragma unroll
        for (int e = 0; e < 16; ++e) z[e] = 0.f;
#pragma unroll
        for (int nb = 0; nb < 3; ++nb) acc[nb] = MFMA32H(wf[0][nb], a[0], z);
      } else if constexpr (k > 1) {
#pragma unroll
        for (int nb = 0; nb < 3; ++nb) acc[nb] = MFMA32H(wf[(k - 1) & 1][nb], a[k - 1], acc[nb]);
      }
      if constexpr (k + 1 < 20) modulate(k + 1);
      if constexpr (k + 2 < 20) ldgs(k + 2);
      if constexpr (k == 8) {
        const u32 q4 = (lane >> 2) & 3, nh = (lane >> 4) & 1;
        const u32 r0 = 122880 + w * 3072 + (w < 5 ? (8 * h + q4) * 192 + 32 * nh : (4 * nh + q4) * 160 + 32 * h) + (lane & 3) * 8, r1 = r0 + (w < 5 ? 768 : 1280);
#pragma unroll
        for (int u = 0; u < 3; ++u) ro[u] = ldstr2(r0 + 64 * u, r1 + 64 * u);
      }
      if constexpr (k == 9 || k == 10) {
        if (w < 5) {
          const u32 ga_ = L_MODS + 960 + w * 64 + h * 32;
          const f32x4 g0 = ldsf4(ga_), g1 = ldsf4(ga_ + 16), s0 = ldsf4(ga_ + 320), s1 = ldsf4(ga_ + 336);
#pragma unroll
          for (int u = (k == 9 ? 0 : 2); u < (k == 9 ? 2 : 3); ++u) {
            float c = 0.f, bi = 0.f;
#pragma unroll
            for (int e = 0; e < 4; ++e) {
              const u32 v = ro[u][e];
              const float x0 = lo16(v), x1 = hi16(v), ge = e < 2 ? g0[2 * e] : g1[2 * e - 4], go = e < 2 ? g0[2 * e + 1] : g1[2 * e - 3];
              const float se = e < 2 ? s0[2 * e] : s1[2 * e - 4], so_ = e < 2 ? s0[2 * e + 1] : s1[2 * e - 3];
              c = fma1(ge, x0, c); c = fma1(go, x1, c); bi = fma1(se, x0, bi); bi = fma1(so_, x1, bi);
            }
            auto r = __builtin_amdgcn_permlane32_swap(f2u(c), f2u(bi), false, false);
            *(LAS float*)(size_t)(w * 768 + u * 256 + (c32 >> 3) * 64 + h * 32 + (c32 & 7) * 4) = u2f(r[0]) + u2f(r[1]);
          }
        }
      }
      if constexpr (k == 17) {
        if (w == 7) {
#pragma unroll
          for (int u = 0; u < 5; ++u) csum += ldsf4(u * 768 + (lane < 48 ? l16 : 0u));
        }
      }
      if constexpr (k == 16) {
        const u32 x2 = 16 * kh + 4 * h, tb = 12288 + (32 * tt + c32) * 48 + 2 * x2;
#pragma unroll
        for (int u = 0; u < 8; ++u) {
          const u32 part = ((x2 + 32 * (u >> 1) + 8 * (u & 1)) * 205) >> 12;
          imga[u] = part * 18392 + tb + 64 * (u >> 1) + 16 * (u & 1); PIN(imga[u]);
        }
      }
      SB();
      if constexpr (k == 0) { grpA(kl, 1); if (!kh) grpW(1); }
      if constexpr (k == 1) grpA(kl, 2);
      if constexpr (k == 2) { if (!kh) grpW(2); grpA(kh, 2); }
      if constexpr (k == 3) { if (kh) grpW(2); }
      if constexpr (s == 1 || s == 2) {
        if constexpr (i == 0) grpA(kl, s + 2);
        if constexpr (i == 1) { if (!kh) grpW(s + 2); }
        if constexpr (i == 2) grpA(kh, s + 2);
        if constexpr (i == 3) { if (kh) grpW(s + 2); }
      }
      if constexpr (k == 12) { if (!kh) rb3.all(l16); }
      if constexpr (k == 14) { if (kh) rb3.all(l16); }
      if constexpr (k == 16) {
        u32 off0, du;
        if (w < 5) { const int k2 = rk16 >= 20, i2 = rk16 - 20 * k2; off0 = WS_WGF + j * WGB + (k2 * 20 + i2) * 2048 + (mt & 1) * 1024 + l16; du = 81920; }
        else { const int k2 = rk16 >= 48, kk = rk16 - 48 * k2; off0 = WS_WDF + n8 * WDB + ((kk >> 1) * 10 + k2 * 5 + h) * 1024 + (kk & 1) * 512 + c32 * 16; du = 2048; }
        asm volatile("" ::: "memory");
        __builtin_amdgcn_raw_buffer_store_b128(ro[0], rs, off0, 0, 16);
        __builtin_amdgcn_raw_buffer_store_b128(ro[1], rs, off0 + du, 0, 16);
        __builtin_amdgcn_raw_buffer_store_b128(ro[2], rs, (w < 5 || h == 0) ? off0 + 2 * du : 0xfffffff0u, 0, 16);
        asm volatile("" ::: "memory");
      }
      SB();
    });
    STAMP(1); CST(3, 19); FST(6, 1);
    WAITV(3);
    BAR();
    FST(6, 2);
    if (w == 7 && lane < 48) __builtin_amdgcn_raw_buffer_store_b128(__builtin_bit_cast(u32x4, csum), rs, WS_CSP + (u32)(j * 16 + mt) * 768 + l16, 0, 16);
    const u32 exw = (w >> 1) * 18432 + (w & 1) * 6144 + l16, exr = ((w ^ 4) >> 1) * 18432 + (w & 1) * 6144 + l16;
#pragma unroll
    for (int nb = 0; nb < 3; ++nb) {
      stf4(exw + nb * 2048, f32x4{acc[nb][8], acc[nb][9], acc[nb][10], acc[nb][11]});
      stf4(exw + nb * 2048 + 1024, f32x4{acc[nb][12], acc[nb][13], acc[nb][14], acc[nb][15]});
    }
    if (kh) st64((tid >> 7) * 18432 + 12288 + (tid & 127) * 48 + 40, u32x2{0x3f803f80u, 0x3f803f80u});
    BAR();
    FST(6, 3);
    const float osc = scaled ? 1024.f : 1.f, qs = QSCALE * (1.f / 4096.f) * osc, sc2 = kh ? qs : osc;
#pragma unroll
    for (int nb = 0; nb < 3; ++nb)
#pragma unroll
      for (int q = 0; q < 2; ++q) {
        const f32x4 o = ldsf4(exr + nb * 2048 + q * 1024);
        float v0 = acc[nb][4 * q] + o[0], v1 = acc[nb][4 * q + 1] + o[1], v2 = acc[nb][4 * q + 2] + o[2], v3 = acc[nb][4 * q + 3] + o[3];
        if (nb == 2) { v0 *= sc2; v1 *= sc2; v2 *= sc2; v3 *= sc2; }
        else if (scaled) { v0 *= osc; v1 *= osc; v2 *= osc; v3 *= osc; }
        st64(imga[2 * nb + q], u32x2{pk2(v0, v1), pk2(v2, v3)});
      }
    BAR();
    FST(6, 4);
    {
      const u32 dst = WS_KVX + (u32)(mt * 16 + j) * KVB + w * 3072 + l16, src = (w >> 1) * 18432 + 12288 + (w & 1) * 3072 + l16;
#pragma unroll
      for (int i = 0; i < 3; ++i) __builtin_amdgcn_raw_buffer_store_b128(lds128(src + i * 1024), rs, dst + i * 1024, 0, 16);
    }
    f32x16 acc3;
#pragma unroll
    for (int e = 0; e < 16; ++e) acc3[e] = 0.f;
    {
      const int r3 = rsw < 24 ? rsw : (rsw < 28 ? rsw - 4 : rsw - 12);
      const u32 wb3 = 122880 + kh * 15360 + (h * 24 + r3) * 16;
      u32x4 w3[4];
#pragma unroll
      for (int k = 0; k < 3; ++k) w3[k] = lds128(wb3 + k * 768);
#pragma unroll
      for (int k = 0; k < 20; ++k) {
        if (k + 3 < 20) w3[(k + 3) & 3] = lds128(wb3 + (k + 3) * 768);
        acc3 = MFMA32H(w3[k & 3], a[k], acc3);
        if (k == 9) {
          FST(6, 5);
          WAITVM(0);
          FST(6, 6);
          if (lane == 0) {
            const u32 old = __hip_atomic_fetch_add((LAS u32*)(size_t)L_FLAG, 1u, __ATOMIC_RELAXED, __HIP_MEMORY_SCOPE_WORKGROUP);
            if (old == 7u) {
              __builtin_amdgcn_raw_buffer_store_b64(u32x2{e1, e2}, rs, WS_KVF + (mt * 16 + j) * 128, 0, 16);
              __builtin_amdgcn_raw_buffer_store_b64(u32x2{e1, e2}, rs, WS_RG + j * 128 + mt * 8, 0, 16);
              __builtin_amdgcn_raw_buffer_store_b64(u32x2{e1, e2}, rs, WS_RD + n8 * 256 + (mt * 2 + th) * 8, 0, 16);
            }
          }
        }
        SB();
      }
    }
    const u32 ex3w = (w < 4 ? 73728 : 84992) + w * 2048 + l16, ex3r = (w < 4 ? 93184 : 65536) + w * 2048 + l16;
    stf4(ex3w, f32x4{acc3[8], acc3[9], acc3[10], acc3[11]});
    stf4(ex3w + 1024, f32x4{acc3[12], acc3[13], acc3[14], acc3[15]});
    FST(6, 7);
    BAR();
    FST(6, 8);
    STAMP(2);
#pragma unroll
    for (int q = 0; q < 2; ++q) {
      const f32x4 o = ldsf4(ex3r + q * 1024);
      st64(imga[6 + q], u32x2{pk2((acc3[4 * q] + o[0]) * qs, (acc3[4 * q + 1] + o[1]) * qs), pk2((acc3[4 * q + 2] + o[2]) * qs, (acc3[4 * q + 3] + o[3]) * qs)});
    }
    BAR();
    FST(6, 9);
  }
  u32x2 xr[3];
  const int c16 = lane & 15, g4 = lane >> 4;
  {
    PHASE();
    const int hh = w >> 2, qb = w < 4 ? w : 7 - w;
    u32x4 q0f, q1f;
    {
      const u32 qa = (4 + hh) * 18432 + 12288 + (32 * qb + c32) * 48;
      const u32x2 x2 = lds64(qa + 32);
      q0f = lds128(qa + 16 * h);
      q1f = u32x4{h ? 0u : x2.x, h ? 0u : x2.y, 0u, 0u};
      const int tok64 = 128 * mt + 64 * th + 16 * tt + c16;
#pragma unroll
      for (int m = 0; m < 3; ++m) gld64(xr[m], (const char*)p.x, (u32)(tok64 * 1280 + (80 * n8 + 16 * ((m + 3 * kh) % 5) + 4 * g4) * 2));
    }
    BAR();
    STAMP(3); STAMP(4); CST(1, 18); FST(4, 12);
    const char* sg0 = ws + WS_WGF + (size_t)j * WGB + (kh * 40 + tt * 10) * 1024;
    const int tmin = mt >= 2 ? 0 : 8 - 4 * mt - qb;
    const u32 kb = hh * 18432 + (32 * qb + c32) * 48, kb2 = kb + 16 * h;
    const u32 vb = (2 + hh) * 18432 + (32 * qb + 4 * h + ((lane >> 2) & 3)) * 48 + ((lane >> 4) & 1) * 32 + (lane & 3) * 8;
    const int qlim = c32 - 4 * h;
    const int dw = (w - 2) & 3;
    const bool duty = w >= 2 && w <= 5;
    const Run<70, 4, true> rwo(ws + WS_WOF + (size_t)n8 * WOB, wo_slot(0), dw);
    const u32 fa = WS_ACT + (u32)((mt * 4 + dw) * 96 + 6 * j) * 1024 + l16;
    const u32 fx = dw < 2 ? WS_X1 + (u32)((mt * 4 + 2 * th + dw) * 40 + 5 * n8) * 1024 + l16 : WS_STAT + (u32)((mt * 4 + 2 * th + h) * 8 + n8) * 512 + c32 * 16;
    const int ko = 40 * j + 20 * hh + 4 * h;
    u32 ob = WS_OAF + (u32)((mt * 4 + qb) * 40) * 1024 + c32 * 16 + (ko >> 3) * 512 + ((ko >> 2) & 1) * 8;
    PIN(ob);
    auto lds_wait = [&](u32 addr, u32 want) LAM {
      for (u32 spins = 0; spins < SPIN_CAP * 8; ++spins) {
        if ((u32)__builtin_amdgcn_readfirstlane(__hip_atomic_load((LAS u32*)(size_t)addr, __ATOMIC_RELAXED, __HIP_MEMORY_SCOPE_WORKGROUP)) >= want) break;
        __builtin_amdgcn_s_sleep(1);
      }
      asm volatile("" ::: "memory");
    };
    {
      if (!duty) {
        const int sl = w >= 6;
        const u32 l0 = (w & 1) * 36864 + sl * 6144;
        if (mt + sl >= 2) {
          wait_flags(ws, WS_KVF + ((mt - 2 + sl) * 16 + j) * 128, e1, e2);
          const char* src = ws + WS_KVX + (size_t)((mt - 2 + sl) * 16 + j) * KVB + (w & 1) * 12288;
#pragma unroll
          for (int u = 0; u < 4; ++u) {
            const u32 l1 = l0 + (u >> 1) * 18432 + (u & 1) * 3072;
            dmai<0, true>(src + u * 3072, l16, l1); dmai<1024, true>(src + u * 3072, l16, l1); dmai<2048, true>(src + u * 3072, l16, l1);
          }
        } else {
#pragma unroll
          for (int u = 0; u < 12; ++u) st128(l0 + (u / 6) * 18432 + (u % 6) * 1024 + l16, u32x4{0u, 0u, 0u, 0u});
        }
      }
    }
    auto halo = [&]() LAM {
      if (!duty) {
        asm volatile("s_waitcnt vmcnt(0) lgkmcnt(0)" ::: "memory");
        if (lane == 0) __hip_atomic_fetch_add((LAS u32*)(size_t)(L_FLAG + (w >= 6 ? 8 : 4)), 1u, __ATOMIC_RELAXED, __HIP_MEMORY_SCOPE_WORKGROUP);
      }
    };
    f32x16 oacc;
    u32x2 rcnt;
    const u32 rfo = lane < 32 ? WS_RD + n8 * 256 + lane * 8 : WS_RG + j * 128 + (lane & 15) * 8;
    auto attn = [&](auto full_) LAM {
      constexpr bool FULL = decltype(full_)::v;
      u32x4 ka, kq, vf[2][2];
      kq = u32x4{0u, 0u, 0u, 0u};
      auto ldkv = [&](int t) LAM {
        const u32x2 x2 = lds64(kb + t * 1536 + 32);
        ka = lds128(kb2 + t * 1536); kq.x = x2.x; kq.y = x2.y;
#pragma unroll
        for (int s2 = 0; s2 < 2; ++s2) { const u32 va = vb + (32 * t + 16 * s2) * 48; vf[t & 1][s2] = ldstr2(va, va + 384); }
      };
      auto splat = [&](float v) LAM -> f32x16 {
        f32x16 c;
#pragma unroll
        for (int e = 0; e < 16; ++e) c[e] = v;
        return c;
      };
      auto pre = [&](auto m_) LAM {
        constexpr int m = decltype(m_)::v;
        if constexpr (m == 2) gld64_sc1(rcnt, ws, rfo);
        if constexpr (m == 4) {
          if (duty) WAITV(FILLP0 ? 4 : 6); else WAITV(0);
          PIN(rcnt);
          if (!flags_set(rcnt, e1, e2)) wait_flags(ws, rfo, e1, e2);
        }
        if constexpr (m >= 4) {
          gld_sc1<((2 * m - 8) & 3) * 1024>(st[2 * m - 8], sg0 + ((2 * m - 8) >> 2) * 4096, l16);
          gld_sc1<((2 * m - 7) & 3) * 1024>(st[2 * m - 7], sg0 + ((2 * m - 7) >> 2) * 4096, l16);
        }
        if constexpr (m == 1 && OWARM) {
#pragma unroll
          for (int g = 0; g < 3; ++g)
            if (g < 2 || h == 0) __builtin_amdgcn_raw_buffer_store_b64(u32x2{0u, 0u}, rs, ob + g * 512, 0, 16);
          asm volatile("" ::: "memory");
        }
        if (duty) {
          rwo.go<2 * m>(l16); rwo.go<2 * m + 1>(l16);
          const u32x4 f = {~0u, ~0u, ~0u, ~0u};
          if constexpr (m < 6 && !FILLP0) __builtin_amdgcn_raw_buffer_store_b128(f, rs, fa + m * 1024, 0, 16);
          if constexpr (m >= 4 && !FILLP0) { if (dw < 2 || (dw == 2 && m == 4)) __builtin_amdgcn_raw_buffer_store_b128(f, rs, fx + (m - 4) * 1024, 0, 16); }
          asm volatile("" ::: "memory");
        }
        SB();
      };
      auto sync = [&](int t) LAM {
        if (t >= 4) { if (qb == 7 - t) lds_wait(L_FLAG + 8, 2u); } else { if (qb == 3 - t) lds_wait(L_FLAG + 4, 2u); }
      };
      f32x16 sb[2], cm;
      float pe[16];
      FST(4, 1);
      ldkv(8);
      sb[0] = MFMA32(ka, q0f, splat(0.f));
      sb[0] = MFMA32(kq, q1f, sb[0]);
      SB();
      pre(IC<0>{});
#pragma unroll
      for (int e = 0; e < 16; ++e) pe[e] = ((e & 3) + 8 * (e >> 2) > qlim) ? -1.0e30f : sb[0][e];
      float mx = __builtin_fmaxf(pe[0], pe[1]);
#pragma unroll
      for (int e = 2; e < 16; e += 2) mx = max3(mx, pe[e], pe[e + 1]);
      mx = -max32(mx);
      cm = splat(mx);
      auto cin = [&](int t) LAM -> f32x16 { if constexpr (FULL) return cm; else return splat(t >= tmin ? mx : -1.0e30f); };
#pragma unroll
      for (int s2 = 0; s2 < 2; ++s2) {
        u32x4 pf;
#pragma unroll
        for (int e = 0; e < 4; ++e) pf[e] = pk2(__builtin_amdgcn_exp2f(pe[8 * s2 + 2 * e] + mx), __builtin_amdgcn_exp2f(pe[8 * s2 + 2 * e + 1] + mx));
        if (s2 == 0) oacc = MFMA32(vf[0][0], pf, splat(0.f)); else oacc = MFMA32(vf[0][1], pf, oacc);
      }
      SB();
      FST(4, 2);
      halo();
      pre(IC<1>{});
      sync(7);
      ldkv(7);
      sb[1] = MFMA32(ka, q0f, cin(7));
      sb[1] = MFMA32(kq, q1f, sb[1]);
      SB();
      sfor<0, 8>([&](auto u_) LAM {
        constexpr int t = 7 - decltype(u_)::v, c = t & 1, n = c ^ 1;
        u32x4 pf0, pf1;
        FST(4, 3 + decltype(u_)::v);
        if constexpr (t > 0) { if constexpr (t - 1 != 7) sync(t - 1); ldkv(t - 1); }
        if constexpr (t == 0) {
#pragma unroll
          for (int e = 0; e < 16; ++e) sb[c][e] = ((e & 3) + 8 * (e >> 2) > qlim) ? sb[c][e] : -1.0e30f;
        }
#pragma unroll
        for (int e = 0; e < 8; ++e) pe[e] = __builtin_amdgcn_exp2f(sb[c][e]);
        SB();
        if constexpr (t > 0) { sb[n] = MFMA32(ka, q0f, cin(t - 1)); SB(); }
#pragma unroll
        for (int e = 8; e < 12; ++e) pe[e] = __builtin_amdgcn_exp2f(sb[c][e]);
#pragma unroll
        for (int e = 0; e < 4; ++e) pf0[e] = pk2(pe[2 * e], pe[2 * e + 1]);
        SB();
        if constexpr (t > 0) { sb[n] = MFMA32(kq, q1f, sb[n]); SB(); }
#pragma unroll
        for (int e = 12; e < 16; ++e) pe[e] = __builtin_amdgcn_exp2f(sb[c][e]);
        SB();
        oacc = MFMA32(vf[c][0], pf0, oacc);
        SB();
#pragma unroll
        for (int e = 0; e < 4; ++e) pf1[e] = pk2(pe[8 + 2 * e], pe[8 + 2 * e + 1]);
        SB();
        if constexpr (t > 0) pre(IC<9 - t>{});
        oacc = MFMA32(vf[c][1], pf1, oacc);
        SB();
      });
    };
    auto exact = [&]() LAM {
      auto sc = [&](int t, float c0, f32x16& s) LAM {
        const u32x2 x2 = lds64(kb + t * 1536 + 32);
#pragma unroll
        for (int e = 0; e < 16; ++e) s[e] = c0;
        s = MFMA32(lds128(kb2 + t * 1536), q0f, s);
        s = MFMA32((u32x4{x2.x, x2.y, 0u, 0u}), q1f, s);
#pragma unroll
        for (int e = 0; e < 16; ++e) s[e] = (u32)(32 * t + (e & 3) + 8 * (e >> 2) - qlim - 1) < 256u ? s[e] : -1.0e30f;
      };
      float mx = -1.0e30f;
      f32x16 s;
#pragma nounroll
      for (int t = tmin; t < 9; ++t) {
        sc(t, 0.f, s);
#pragma unroll
        for (int e = 0; e < 16; e += 2) mx = max3(mx, s[e], s[e + 1]);
      }
      mx = -max32(mx);
#pragma unroll
      for (int e = 0; e < 16; ++e) oacc[e] = 0.f;
#pragma nounroll
      for (int t = tmin; t < 9; ++t) {
        sc(t, mx, s);
#pragma unroll
        for (int s2 = 0; s2 < 2; ++s2) {
          u32x4 pf;
#pragma unroll
          for (int e = 0; e < 4; ++e) pf[e] = pk2(__builtin_amdgcn_exp2f(s[8 * s2 + 2 * e]), __builtin_amdgcn_exp2f(s[8 * s2 + 2 * e + 1]));
          const u32 va = vb + (32 * t + 16 * s2) * 48;
          oacc = MFMA32(ldstr2(va, va + 384), pf, oacc);
        }
      }
    };
    if (mt >= 2) attn(BoolC<true>{}); else attn(BoolC<false>{});
    if (__builtin_amdgcn_ballot_w64(h != 0 && !(oacc[8] < ATT_LIMIT)) != 0ull) exact();
    const float lx = xor32f(oacc[8]);
    const float inv = __builtin_amdgcn_rcpf(64.f * (h ? oacc[8] : lx));
#pragma unroll
    for (int g = 0; g < 3; ++g) {
      const u32x2 o = {pk2(oacc[4 * g] * inv, oacc[4 * g + 1] * inv), pk2(oacc[4 * g + 2] * inv, oacc[4 * g + 3] * inv)};
      if (g < 2 || h == 0) __builtin_amdgcn_raw_buffer_store_b64(o, rs, ob + g * 512, 0, 16);
    }
    STAMP(5); CST(1, 19); FST(4, 11);
    WAITVM(0);
    BAR();
    if (tid == 0) __builtin_amdgcn_raw_buffer_store_b64(u32x2{e1, e2}, rs, WS_OF + mt * 128 + j * 8, 0, 16);
    STAMP(6); FST(4, 13);
  }
  u32 wbase[5], avoff;
  u32x2 x1k[3];
  f32x4 c5[5];
  {
    PHASE();
    const char* sg1 = ws + WS_WGF + (size_t)j * WGB + 81920 + (kh * 40 + tt * 8) * 1024;
    auto stage1 = [&]() LAM { sfor<0, 8>([&](auto m_) LAM { constexpr int m = decltype(m_)::v; gld_sc1<(m & 3) * 1024>(st[10 + m], sg1 + (m >> 2) * 4096, l16); }); };
    if (w >= 4) { __builtin_amdgcn_s_sleep(2); Run<30, 4, true>(ws + WS_WOF + (size_t)n8 * WOB + 71680, wo_slot(7), w - 4).all(l16); }
    stage1();
#pragma unroll
    for (int m = 0; m < 5; ++m) wbase[m] = (kh * 5 + (m + 3 * kh) % 5) * 1024 + l16;
    avoff = (g4 >> 1) * 1024 + (g4 & 1) * 512 + (16 * (tt & 1) + c16) * 16;
    const int tt64 = mt * 4 + 2 * th + (tt >> 1);
    const char* ab = ws + WS_OAF + (size_t)(tt64 * 40 + 20 * kh) * 1024;
    const u32 ew = 73728 + (tt * 5) * 1024 + l16;
    u32 xb = WS_X1 + (u32)(tt64 * 40 + 5 * n8) * 1024 + (g4 >> 1) * 512 + (16 * (tt & 1) + c16) * 16 + (g4 & 1) * 8 + kh * 3072;
    u32 sta = WS_STAT + (u32)((tt64 * 8 + n8) * 32 + 16 * (tt & 1) + c16) * 16 + kh * 8;
    u32 mo = L_MODS + kh * 192 + g4 * 16;
    PIN(xb); PIN(sta); PIN(mo); PIN(avoff);
    auto iss = [&](auto s_) LAM { constexpr int s = decltype(s_)::v; gld_sc1<(s & 1) * 2048>(a[s], ab + (s >> 1) * 4096, avoff); };
    u32x4 wf[3][5];
#pragma unroll
    for (int s = 0; s < 2; ++s)
#pragma unroll
      for (int m = 0; m < 5; ++m) wf[s][m] = lds128(wbase[m] + wo_slot(s));
#pragma unroll
    for (int m = 0; m < 5; ++m) c5[m] = f32x4{0.f, 0.f, 0.f, 0.f};
    if (w == 0) wait_flags(ws, WS_OF + mt * 128 + (lane & 15) * 8, e1, e2);
    BAR();
    STAMP(7); FST(5, 1);
    sfor<0, 5>(iss);
    sfor<0, 10>([&](auto s_) LAM {
      constexpr int s = decltype(s_)::v;
      WAITV(s == 0 ? 4 : s == 1 ? 6 : 9 - s);
      FST(5, 2 + s);
      if (s == 4) BAR();
      PIN(a[s]);
      if constexpr (s + 2 < 10) {
#pragma unroll
        for (int m = 0; m < 5; ++m) wf[(s + 2) % 3][m] = lds128(wbase[m] + wo_slot(s + 2));
      }
#pragma unroll
      for (int m = 0; m < 5; ++m) c5[m] = MFMA16(wf[s % 3][m], a[s], c5[m]);
      SB();
      if constexpr (s == 0) sfor<5, 8>(iss);
      if constexpr (s == 1) sfor<8, 10>(iss);
      SB();
    });
    STAMP(8); FST(5, 12);
    stf4(ew + ((3 + 3 * kh) % 5) * 1024, c5[3]);
    stf4(ew + ((4 + 3 * kh) % 5) * 1024, c5[4]);
    if (kh) stf4(ew, c5[2]);
    BAR();
    FST(5, 13);
    float xs[12];
#pragma unroll
    for (int m = 0; m < 3; ++m) PIN(xr[m]);
#pragma unroll
    for (int m = 0; m < 3; ++m) {
      if (m < 2 || kh == 0) {
        const f32x4 o = ldsf4(ew + kh * 3072 + m * 1024), ga = ldsf4(mo + m * 64), g2 = ldsf4(mo + 320 + m * 64);
        const u32 y01 = pk2(c5[m][0] + o[0], c5[m][1] + o[1]), y23 = pk2(c5[m][2] + o[2], c5[m][3] + o[3]);
        const u32 t01 = pk2(ga[0] * lo16(y01), ga[1] * hi16(y01)), t23 = pk2(ga[2] * lo16(y23), ga[3] * hi16(y23));
        x1k[m] = u32x2{pk2(lo16(xr[m].x) + lo16(t01), hi16(xr[m].x) + hi16(t01)), pk2(lo16(xr[m].y) + lo16(t23), hi16(xr[m].y) + hi16(t23))};
        xs[4 * m] = lo16(x1k[m].x); xs[4 * m + 1] = hi16(x1k[m].x); xs[4 * m + 2] = lo16(x1k[m].y); xs[4 * m + 3] = hi16(x1k[m].y);
        __builtin_amdgcn_raw_buffer_store_b64(u32x2{pk2(xs[4 * m] * g2[0], xs[4 * m + 1] * g2[1]), pk2(xs[4 * m + 2] * g2[2], xs[4 * m + 3] * g2[3])}, rs, xb + m * 1024, 0, 16);
      } else {
        x1k[m] = u32x2{0u, 0u};
        xs[8] = xs[9] = xs[10] = xs[11] = 0.f;
      }
    }
    float s = ((xs[0] + xs[1]) + (xs[2] + xs[3])) + ((xs[4] + xs[5]) + (xs[6] + xs[7])) + ((xs[8] + xs[9]) + (xs[10] + xs[11]));
    s = sum32(sum16(s));
    const float mean = s * (kh ? 1.f / 32.f : 1.f / 48.f);
    float q0 = 0.f, q1 = 0.f;
#pragma unroll
    for (int e = 0; e < 4; ++e) { const float d0 = xs[e] - mean, d1 = xs[4 + e] - mean; q0 = fma1(d0, d0, q0); q1 = fma1(d1, d1, q1); }
    if (kh == 0) {
#pragma unroll
      for (int e = 8; e < 12; ++e) { const float d = xs[e] - mean; q0 = fma1(d, d, q0); }
    }
    const float q = sum32(sum16(q0 + q1));
    if (g4 == 0) __builtin_amdgcn_raw_buffer_store_b64(u32x2{f2u(mean), f2u(q)}, rs, sta, 0, 16);
    FST(5, 14);
    BAR();
    STAMP(9); FST(5, 16);
  }
  {
    PHASE();
    const bool old4 = w < 4;
    const char* wgb = ws + WS_WGF + (size_t)j * WGB;
    const char* wdb = ws + WS_WDF + (size_t)n8 * WDB;
    u32x4 stv[4], csv[2];
    const char* ab = ws + WS_X1 + (size_t)((mt * 4 + tt) * 40 + 20 * kh) * 1024;
    const char* sb = ws + WS_STAT + (size_t)(mt * 4 + tt) * 4096;
    auto iss = [&](auto s_) LAM { constexpr int s = decltype(s_)::v; gld_sc1<(s & 3) * 1024>(a[s], ab + (s >> 2) * 4096, l16); };
    {
      const char* cb = ws + WS_CSP + (size_t)(j * 16 + 4 * (w >> 1) + (w & 1)) * 768;
      const u32 co = lane < 48 ? l16 : 0u;
      gld_sc1<0>(csv[0], cb, co); gld_sc1<1536>(csv[1], cb, co);
    }
#pragma unroll
    for (int m = 0; m < 18; ++m) { PIN(st[m]); st128((m < 10 ? (kh * 40 + tt * 10 + m) : (80 + kh * 32 + tt * 8 + m - 10)) * 1024 + l16, st[m]); }
    float c1 = 0.f, d2 = 0.f;
    u32x4 bcor = {0u, 0u, 0u, 0u};
    u32 sw = kh * 40960 + tt * 4096 + l16, sr = (kh ^ 1) * 40960 + tt * 4096 + l16;
    u32 actb = WS_ACT + (u32)((mt * 4 + tt) * 96 + 6 * j + kh) * 1024 + l16;
    const u32 so = h * 2048 + c32 * 16;
    PIN(actb);
    const auto rC = gu_run<1, 16, 20, 4>(wgb, w & 3);
    const auto rA = gu_run<2, 0, 8, 4>(wgb, w & 3);
    const auto rB = gu_run<2, 8, 20, 4>(wgb, w & 3);
    const Run<60, 8, true> rD(wdb, 0, w);
    const Run<90, 8, true> rS(wdb + 61440, 61440, w);
    gate(sb, (c32 >> 2) * 512 + ((c32 >> 1) & 1) * 256 + (c32 & 1) * 8);
    STAMP(10); FST(2, 1); FST(5, 17);
    sfor<0, 2>(iss);
    gld_sc1<0>(stv[0], sb, so); gld_sc1<512>(stv[1], sb, so); gld_sc1<1024>(stv[2], sb, so); gld_sc1<1536>(stv[3], sb, so);
    sfor<2, 6>(iss);
    f32x16 ag[2], au[2];
    u32x4 fg[3], fu[3], outv;
    u32 ub = 0;
    auto ldw = [&](auto g_) LAM {
      constexpr int g = decltype(g_)::v, pp = g / 20, i = g % 20;
      constexpr bool first = i == 0 || gu_unit(pp, 0, i) != gu_unit(pp, 0, i - 1) + 1;
      constexpr int i0 = [] { int x = i; while (x > 0 && gu_unit(pp, 0, x) == gu_unit(pp, 0, x - 1) + 1) --x; return x; }();
      if constexpr (first) ub = frow + (kh ? gu_unit(pp, 1, i) : gu_unit(pp, 0, i)) * 2048;
      fg[g % 3] = lds128(ub + (i - i0) * 2048); fu[g % 3] = lds128(ub + (i - i0) * 2048 + 1024);
    };
    auto xwrite = [&](int pp) LAM {
      const f32x16 &G = ag[pp & 1], &U = au[pp & 1];
      stf4(sw, f32x4{G[8], G[9], G[10], G[11]}); stf4(sw + 1024, f32x4{G[12], G[13], G[14], G[15]});
      stf4(sw + 2048, f32x4{U[8], U[9], U[10], U[11]}); stf4(sw + 3072, f32x4{U[12], U[13], U[14], U[15]});
    };
    f32x4 xo[4];
    auto xread = [&]() LAM {
#pragma unroll
      for (int u = 0; u < 4; ++u) xo[u] = ldsf4(sr + u * 1024);
    };
    auto xadd = [&](int pp, int q) LAM {
#pragma unroll
      for (int i = 0; i < 4; ++i) { ag[pp & 1][4 * q + i] += xo[q][i]; au[pp & 1][4 * q + i] += xo[2 + q][i]; }
    };
    auto corr = [&](int pp) LAM {
      ag[pp & 1] = MFMA32(lds128(L_T2 + pp * 1024 + rsw * 16), bcor, ag[pp & 1]);
      au[pp & 1] = MFMA32(lds128(L_T2 + pp * 1024 + 512 + rsw * 16), bcor, au[pp & 1]);
    };
    float eu[2], ee[2];
    auto epi = [&](int pp, int o, int ph) LAM {
#pragma unroll
      for (int i = 0; i < 2; ++i) {
        if (ph == 0) { ee[i] = __builtin_amdgcn_exp2f(ag[pp & 1][2 * o + i] * c1); eu[i] = ag[pp & 1][2 * o + i] * au[pp & 1][2 * o + i]; }
        if (ph == 1) ee[i] = __builtin_amdgcn_rcpf(fma1(ee[i], d2, d2));
      }
      if (ph == 2) outv[o] = pk2(eu[0] * ee[0], eu[1] * ee[1]);
    };
    auto ostore = [&](int pp) LAM { __builtin_amdgcn_raw_buffer_store_b128(outv, rs, actb + pp * 2048, 0, 16); asm volatile("" ::: "memory"); };
    BAR();
    ldw(IC<0>{}); ldw(IC<1>{});
    auto nextA = [&](auto g_) LAM {
      constexpr int g = decltype(g_)::v;
      WAITV(g == 0 ? 9 : g == 1 ? 12 : g == 2 ? 11 : g == 3 ? 14 : 19 - g);
      PIN(a[g]);
      ensure<(g & 3) * 1024>(a[g], ab + (g >> 2) * 4096, l16);
    };
    nextA(IC<0>{});
    sfor<0, 60>([&](auto g_) LAM {
      constexpr int g = decltype(g_)::v, pp = g / 20, i = g % 20;
      if (g == 32) WAITV(0);
      if (g == 43) WAITV(NRS >= 3 ? 4 : 1 + NRS);
      if ((g & 3) == 0) FST(2, 2 + g / 4);
      if (g == 12 || g == 22 || g == 32 || g == 43 || g == 50) BAR();
      if (g == 22 && CSEL != 4) { STAMP(16); CST(0, 18); }
      if (g == 43 && CSEL != 4) { STAMP(17); CST(0, 19); }
      if constexpr (g + 2 < 60) ldw(IC<g + 2>{});
      if (g == 22 || g == 43) xread();
      if (i == 0) {
        f32x16 z;
#pragma unroll
        for (int e = 0; e < 16; ++e) z[e] = 0.f;
        ag[pp & 1] = MFMA32(fg[g % 3], a[i], z); au[pp & 1] = MFMA32(fu[g % 3], a[i], z);
      } else {
        ag[pp & 1] = MFMA32(fg[g % 3], a[i], ag[pp & 1]); au[pp & 1] = MFMA32(fu[g % 3], a[i], au[pp & 1]);
      }
      if (g == 4) {
#pragma unroll
        for (int u = 0; u < 4; ++u) PIN(stv[u]);
        ensure<0>(stv[0], sb, so); ensure<512>(stv[1], sb, so); ensure<1024>(stv[2], sb, so); ensure<1536>(stv[3], sb, so);
        PIN(csv[0]); PIN(csv[1]);
        f32x4 sm;
#pragma unroll
        for (int e = 0; e < 4; ++e) sm[e] = u2f(csv[0][e]) + u2f(csv[1][e]);
        if (lane < 48) stf4(L_GS + (w >> 1) * 1536 + (w & 1) * 768 + l16, sm);
        float s = 0.f;
#pragma unroll
        for (int u = 0; u < 4; ++u) s += 48.f * u2f(stv[u][0]) + 32.f * u2f(stv[u][2]);
        const float mean = sum32(s) * (1.f / 640.f);
        float q = 0.f;
#pragma unroll
        for (int u = 0; u < 4; ++u) {
          const float d0 = u2f(stv[u][0]) - mean, d1 = u2f(stv[u][2]) - mean;
          q += u2f(stv[u][1]) + u2f(stv[u][3]) + 48.f * d0 * d0 + 32.f * d1 * d1;
        }
        q = sum32(q);
        const float den = rbf(__builtin_amdgcn_sqrtf(rbf(rbf(q * (1.f / 640.f)) + 1e-5f)));
        c1 = -1.4426950408889634f * __builtin_amdgcn_rcpf(den); d2 = den * den;
        const u32 nm = pk2(-mean, -mean), dd = pk2(den, den);
        bcor = u32x4{h ? 0u : nm, h ? 0u : ((nm & 0xffffu) | (dd & 0xffff0000u)), h ? 0u : dd, 0u};
      }
      if (g == 12 && tid < 192) {
        const u32 t4 = L_GS + (tid >> 3) * 64 + (tid & 7) * 4;
        const float cs = (ldsf(t4) + ldsf(t4 + 1536)) + (ldsf(t4 + 3072) + ldsf(t4 + 4608));
        const float bi = (ldsf(t4 + 32) + ldsf(t4 + 1568)) + (ldsf(t4 + 3104) + ldsf(t4 + 4640));
        const u32 h0 = pk2(cs, bi);
        const float cr = cs - lo16(h0), br = bi - hi16(h0);
        const u32 h1 = pk2(cr, br), h2 = pk2(cr - lo16(h1), br - hi16(h1));
        const int gu = tid >= 96, nn = tid - 96 * gu;
        st128(L_T2 + (nn >> 5) * 1024 + gu * 512 + (nn & 31) * 16,
              u32x4{__builtin_amdgcn_perm(h1, h0, 0x05040100u), __builtin_amdgcn_perm(h0, h2, 0x07060100u), __builtin_amdgcn_perm(h2, h1, 0x07060302u), 0u});
      }
      if (g == 21 || g == 41) xwrite(pp - 1);
      if (g == 22 || g == 42) corr(pp - 1);
      if (g == 24 || g == 25) xadd(0, g - 24);
      if (g >= 28 && g <= 37 && (g - 28) % 3 == 0) epi(0, (g - 28) / 3, 2);
      if (g >= 26 && g < 37 && (g - 26) % 3 < 2) epi(0, (g - 26) / 3, (g - 26) % 3);
      if (g == 44 || g == 45) xadd(1, g - 44);
      if (g >= 48 && g <= 57 && (g - 48) % 3 == 0) epi(1, (g - 48) / 3, 2);
      if (g >= 46 && g < 57 && (g - 46) % 3 < 2) epi(1, (g - 46) / 3, (g - 46) % 3);
      SGB(0x100, 6, 0); SGB(0x008, 1, 0); SGB(0x002, 4, 0); SGB(0x008, 1, 0);
      SB();
      if constexpr (g < 3) sfor<6 + 4 * g, 10 + 4 * g>(iss);
      if constexpr (g == 3) sfor<18, 20>(iss);
      if constexpr (g + 1 < 20) nextA(IC<g + 1>{});
      if (g == 40) ostore(0);
      if constexpr (g >= 40 && g < 40 + NRS) gld_sc1<((g - 40) & 3) * 1024>(a[g - 40], rS.src + ((g - 40) >> 2) * 4096, l16);
      if constexpr (g >= 50 && g < 58) rD.template go<g - 50>(l16);
      if (g == 57) ostore(1);
      if constexpr ((g >= 16 && g < 18) || (g >= 22 && g < 26) || (g >= 32 && g < 38)) {
        if (old4) {
          if constexpr (g < 18) { rC.template go<2 * (g - 16)>(l16); rC.template go<2 * (g - 16) + 1>(l16); }
          else if constexpr (g < 26) { rA.template go<2 * (g - 22)>(l16); rA.template go<2 * (g - 22) + 1>(l16); }
          else { rB.template go<2 * (g - 32)>(l16); rB.template go<2 * (g - 32) + 1>(l16); }
        }
      }
      SB();
    });
    { const u32 lo_ = 65536 + tt * 4096, hi_ = (tt < 2 ? 106496 : 131072) + tt * 4096; sw = (kh ? hi_ : lo_) + l16; sr = (kh ? lo_ : hi_) + l16; }
    xwrite(2);
    corr(2);
    BAR();
    STAMP(11); FST(2, 17);
    xread(); xadd(2, 0); xadd(2, 1);
#pragma unroll
    for (int o = 0; o < 4; ++o)
#pragma unroll
      for (int ph = 0; ph < 3; ++ph) epi(2, o, ph);
    ostore(2);
    WAITV(8);
    BAR();
#pragma unroll
    for (int m = 0; m < NRS; ++m) { PIN(a[m]); st128(rS.lds + m * 1024 + l16, a[m]); }
    sfor<NRS, 12>([&](auto m_) LAM { rS.template go<decltype(m_)::v>(l16); });
    STAMP(12);
  }
  {
    PHASE();
    const bool old4 = w < 4;
    const char* wdb = ws + WS_WDF + (size_t)n8 * WDB;
    const int tt64 = mt * 4 + 2 * th + (tt >> 1);
    const char* ab = ws + WS_ACT + (size_t)(tt64 * 96 + 48 * kh) * 1024;
    auto iss = [&](auto s_) LAM { constexpr int s = decltype(s_)::v; gld_sc1<(s & 1) * 2048>(a[s], ab + (s >> 1) * 4096, avoff); };
#pragma unroll
    for (int m = 0; m < 5; ++m) c5[m] = f32x4{0.f, 0.f, 0.f, 0.f};
    const Run<50, 4, true> rE(wdb + 153600, 0, w & 3);
    const Run<40, 4, true> rF(wdb + 204800, 51200, w & 3);
    const u32 ew = 92160 + (tt * 5) * 1024 + l16;
    u32 oo = (u32)(128 * mt + 64 * th + 16 * tt + c16) * 1280 + (80 * n8 + 48 * kh + 4 * g4) * 2;
    u32 mo = L_MODS + 640 + kh * 192 + g4 * 16;
    PIN(oo); PIN(mo);
    gate(ab, ((c16 >> 1) * 6 + 4 + (c16 & 1)) * 1024 + (tt & 1) * 256);
    STAMP(13); CST(2, 18); FST(1, 1);
    sfor<0, 6>(iss);
    u32 wb2[5], wb3[5];
#pragma unroll
    for (int m = 0; m < 5; ++m) { wb2[m] = wbase[m] + 61440; wb3[m] = wbase[m] + 122880; PIN(wb2[m]); PIN(wb3[m]); }
    auto nextA = [&](auto s_) LAM {
      constexpr int s = decltype(s_)::v;
      if constexpr (dn_wait(s, true) == dn_wait(s, false)) WAITV(dn_wait(s, true)); else { if (old4) WAITV(dn_wait(s, true)); else WAITV(dn_wait(s, false)); }
      PIN(a[s]);
      ensure<(s & 1) * 2048>(a[s], ab + (s >> 1) * 4096, avoff);
    };
    BAR();
    u32x4 wf[3][5];
#pragma unroll
    for (int u = 0; u < 2; ++u)
#pragma unroll
      for (int m = 0; m < 5; ++m) wf[u][m] = lds128(wbase[m] + wd_slot(u));
    nextA(IC<0>{});
    sfor<0, 24>([&](auto s_) LAM {
      constexpr int s = decltype(s_)::v;
      if ((s & 1) == 0) FST(1, 2 + s / 2);
      if (s == 5 || s == 9 || s == 13 || s == 18) BAR();
      if constexpr (s + 2 < 24) {
        constexpr u32 so = wd_slot(s + 2);
#pragma unroll
        for (int m = 0; m < 5; ++m) wf[(s + 2) % 3][m] = lds128((so < 61440 ? wbase[m] : so < 122880 ? wb2[m] : wb3[m]) + so % 61440);
      }
#pragma unroll
      for (int m = 0; m < 5; ++m) c5[m] = MFMA16(wf[s % 3][m], a[s], c5[m]);
      SB();
      if constexpr (s >= 5 && s < 14) {
        if (old4) {
          if constexpr (s < 9) { sfor<0, 4>([&](auto u_) LAM { rE.template go<4 * (s - 5) + decltype(u_)::v>(l16); }); }
          else { rF.template go<2 * (s - 9)>(l16); rF.template go<2 * (s - 9) + 1>(l16); }
        }
      }
      sfor<dn_cumA(s), dn_cumA(s + 1)>(iss);
      if constexpr (s + 1 < 24) nextA(IC<s + 1>{});
      SB();
    });
    STAMP(14); CST(2, 19); FST(1, 14);
    stf4(ew + ((3 + 3 * kh) % 5) * 1024, c5[3]);
    stf4(ew + ((4 + 3 * kh) % 5) * 1024, c5[4]);
    if (kh) stf4(ew, c5[2]);
    BAR();
#pragma unroll
    for (int m = 0; m < 3; ++m)
      if (m < 2 || kh == 0) {
        const f32x4 o = ldsf4(ew + kh * 3072 + m * 1024), gb = ldsf4(mo + m * 64);
        const u32 y01 = pk2(c5[m][0] + o[0], c5[m][1] + o[1]), y23 = pk2(c5[m][2] + o[2], c5[m][3] + o[3]);
        const u32 t01 = pk2(gb[0] * lo16(y01), gb[1] * hi16(y01)), t23 = pk2(gb[2] * lo16(y23), gb[3] * hi16(y23));
        const u32x2 r = {pk2(lo16(x1k[m].x) + lo16(t01), hi16(x1k[m].x) + hi16(t01)), pk2(lo16(x1k[m].y) + lo16(t23), hi16(x1k[m].y) + hi16(t23))};
        *(GAS u32x2*)((GAS char*)p.out + oo + m * 32) = r;
      }
  }
  if (b == 0 && tid == 0) __builtin_amdgcn_raw_buffer_store_b32(e1, rs, WS_EP, 0, 16);
}
}

extern "C" void kernel_launch(void* const* d_in, const int* in_sizes, int n_in, void* d_out, int out_size, void* d_ws, size_t ws_size,
                              hipStream_t stream) {
  (void)in_sizes; (void)n_in; (void)out_size; (void)ws_size;
  (void)hipFuncSetAttribute((const void*)k2, hipFuncAttributeMaxDynamicSharedMemorySize, LDS_BYTES);
  P2 p;
  p.x = (const u16*)d_in[0]; p.wqkv = (const u16*)d_in[1]; p.wo = (const u16*)d_in[2]; p.wg = (const u16*)d_in[4]; p.wu = (const u16*)d_in[5];
  p.wd = (const u16*)d_in[6]; p.cvec = (const u16*)d_in[7]; p.wmod = (const u16*)d_in[8];
  p.out = (u16*)d_out; p.ws = (char*)d_ws;
  k2<<<256, 512, LDS_BYTES, stream>>>(p);
}
```

```cpp
#include <hip/hip_runtime.h>
#include <hip/hip_bf16.h>
#include <stdint.h>

#define GAS __attribute__((address_space(1)))
#define LAS __attribute__((address_space(3)))
#if defined(__HIP_DEVICE_COMPILE__)
#define NOPK __attribute__((target("no-packed-fp32-ops")))
#else
#define NOPK
#endif
#define DI __device__ __forceinline__ NOPK
#define LAM __attribute__((always_inline)) NOPK
typedef unsigned int u32;
typedef unsigned short u16;
typedef u32 u32x4 __attribute__((ext_vector_type(4)));
typedef u32 u32x2 __attribute__((ext_vector_type(2)));
typedef float f32x2 __attribute__((ext_vector_type(2)));
typedef float f32x4 __attribute__((ext_vector_type(4)));
typedef float f32x16 __attribute__((ext_vector_type(16)));
typedef __bf16 bf16x2 __attribute__((ext_vector_type(2)));
typedef __bf16 bf16x4 __attribute__((ext_vector_type(4)));
typedef __bf16 bf16x8 __attribute__((ext_vector_type(8)));
typedef GAS u32 gu32;
typedef _Float16 f16x8 __attribute__((ext_vector_type(8)));
typedef _Float16 f16x2 __attribute__((ext_vector_type(2)));

#define STAMPS 0

namespace {
constexpr int S = 2048, D = 640, F = 1536;
constexpr u32 WS_KVF = 0;
constexpr u32 WS_CNT = 32768;
constexpr u32 WS_MODP = 65536;
constexpr u32 WS_CSP = WS_MODP + 61440;
constexpr u32 WS_STAT = WS_CSP + 16 * 16 * 1536;
constexpr u32 WS_N1 = WS_STAT + 64 * 8 * 32 * 16;
constexpr u32 WS_OAF = WS_N1 + 64 * 40 * 1024;
constexpr u32 WS_X1 = WS_OAF + 64 * 40 * 1024;
constexpr u32 WS_ACT = WS_X1 + 64 * 40 * 1024;
constexpr u32 KVB = 24576;
constexpr u32 WS_KVX = WS_ACT + 64 * 96 * 1024;
constexpr u32 WQB = 153600;
constexpr u32 WS_WQF = WS_KVX + 256 * KVB;
constexpr u32 WOB = 102400, WGB = 245760, WDB = 245760;
constexpr u32 WS_WOF = WS_WQF + 16 * WQB;
constexpr u32 WS_WGF = WS_WOF + 8 * WOB;
constexpr u32 WS_WDF = WS_WGF + 16 * WGB;
constexpr u32 WS_DBG = WS_WDF + 8 * WDB;
constexpr u32 WS_DBG1 = WS_DBG + 256 * 32 * 8;
constexpr u32 WS_END = WS_DBG1 + 1024 * 16;
constexpr u32 L_MISC = 153600;
constexpr u32 L_GS = L_MISC;
constexpr u32 L_MODS = L_MISC + 6144;
constexpr u32 L_T2 = L_MISC + 7168;
constexpr u32 L_FLAG = L_MISC + 9728;
constexpr u32 LDS_BYTES = 163840;
constexpr u32 L_K = 0, L_V = 30720;
constexpr float QSCALE = 0.22360679774997896f * 1.4426950408889634f;
constexpr int K1_BLOCKS = 488;
#define ATT_LIMIT 1.0e18f
constexpr int K2_LINES = 300;
constexpr u32 SPIN_CAP = 1u << 20;

DI float u2f(u32 v) { return __builtin_bit_cast(float, v); }
DI u32 f2u(float v) { return __builtin_bit_cast(u32, v); }
DI int tidx() { return (int)__builtin_amdgcn_workitem_id_x(); }
DI int bidx() { return (int)__builtin_amdgcn_workgroup_id_x(); }
DI float bf2f(u32 v) { return u2f(v << 16); }
DI float lo16(u32 v) { return u2f(v << 16); }
DI float hi16(u32 v) { return u2f(v & 0xffff0000u); }
DI u32 pk2(float a, float b) { f32x2 v = {a, b}; return __builtin_bit_cast(u32, __builtin_convertvector(v, bf16x2)); }
DI u32 pkh2(float a, float b) { f32x2 v = {a, b}; return __builtin_bit_cast(u32, __builtin_convertvector(v, f16x2)); }
DI float hlo(u32 v) { return (float)__builtin_bit_cast(f16x2, v).x; }
DI float hhi(u32 v) { return (float)__builtin_bit_cast(f16x2, v).y; }
DI float rbf(float a) { return lo16(pk2(a, a)); }
DI float fma1(float a, float b, float c) { return __builtin_fmaf(a, b, c); }
DI float mul1(float a, float b) { return a * b; }
DI float add1(float a, float b) { return a + b; }
DI float max3(float a, float b, float c) { return __builtin_fmaxf(__builtin_fmaxf(a, b), c); }
DI float xor32f(float v) {
  auto r = __builtin_amdgcn_permlane32_swap(f2u(v), f2u(v), false, false);
  return u2f((tidx() & 32) ? r[0] : r[1]);
}
DI float sum32(float v) { auto r = __builtin_amdgcn_permlane32_swap(f2u(v), f2u(v), false, false); return u2f(r[0]) + u2f(r[1]); }
DI float sum16(float v) { auto r = __builtin_amdgcn_permlane16_swap(f2u(v), f2u(v), false, false); return u2f(r[0]) + u2f(r[1]); }
DI float max32(float v) { auto r = __builtin_amdgcn_permlane32_swap(f2u(v), f2u(v), false, false); return __builtin_fmaxf(u2f(r[0]), u2f(r[1])); }
template <int CTRL> DI float dppf(float v) { return u2f(__builtin_amdgcn_update_dpp(0u, f2u(v), CTRL, 0xf, 0xf, true)); }

#define MFMA32(a, b, c) __builtin_amdgcn_mfma_f32_32x32x16_bf16(__builtin_bit_cast(bf16x8, a), __builtin_bit_cast(bf16x8, b), c, 0, 0, 0)
#define MFMA32H(a, b, c) __builtin_amdgcn_mfma_f32_32x32x16_f16(__builtin_bit_cast(f16x8, a), __builtin_bit_cast(f16x8, b), c, 0, 0, 0)
#define MFMA16(a, b, c) __builtin_amdgcn_mfma_f32_16x16x32_bf16(__builtin_bit_cast(bf16x8, a), __builtin_bit_cast(bf16x8, b), c, 0, 0, 0)
#define SGB(m, n, id) __builtin_amdgcn_sched_group_barrier(m, n, id)
#define SB() __builtin_amdgcn_sched_barrier(0)
template <bool B> struct BoolC { static constexpr bool v = B; };
template <int I> struct IC { static constexpr int v = I; };
template <int I, int N, class Fn> DI void sfor(Fn&& f) { if constexpr (I < N) { f(IC<I>{}); sfor<I + 1, N>(f); } }
#define PIN(x) asm volatile("" : "+v"(x))
#define WAITV(n) asm volatile("s_waitcnt vmcnt(%0)" ::"i"(n))
#define WAITVM(n) asm volatile("s_waitcnt vmcnt(%0)" ::"i"(n) : "memory")
#define BAR() asm volatile("s_waitcnt lgkmcnt(0)\n\ts_barrier" ::: "memory")
#define RLX_AGENT __ATOMIC_RELAXED, __HIP_MEMORY_SCOPE_AGENT

DI u32x4 lds128(u32 a) { return *(const LAS u32x4*)(size_t)a; }
DI f32x4 ldsf4(u32 a) { return *(const LAS f32x4*)(size_t)a; }
DI u32x2 lds64(u32 a) { return *(const LAS u32x2*)(size_t)a; }
DI float ldsf(u32 a) { return *(const LAS float*)(size_t)a; }
DI void st128(u32 a, u32x4 v) { *(LAS u32x4*)(size_t)a = v; }
DI void stf4(u32 a, f32x4 v) { *(LAS f32x4*)(size_t)a = v; }
DI void st64(u32 a, u32x2 v) { *(LAS u32x2*)(size_t)a = v; }
DI u32x2 ldstr(u32 a) { return __builtin_bit_cast(u32x2, __builtin_amdgcn_ds_read_tr16_b64_v4bf16((LAS bf16x4*)(size_t)a)); }
DI u32x4 ldstr2(u32 a, u32 b) { u32x2 lo = ldstr(a), hi = ldstr(b); return u32x4{lo.x, lo.y, hi.x, hi.y}; }

template <int IMM, bool SC1> DI void dmai(const char* sbase, u32 voff, u32 lds) {
  if constexpr (SC1) asm volatile("s_mov_b32 m0, %2\n\ts_nop 0\n\tglobal_load_lds_dwordx4 %0, %1 offset:%3 sc1" ::"v"(voff), "s"(sbase), "s"(lds), "i"(IMM));
  else asm volatile("s_mov_b32 m0, %2\n\ts_nop 0\n\tglobal_load_lds_dwordx4 %0, %1 offset:%3" ::"v"(voff), "s"(sbase), "s"(lds), "i"(IMM));
}
DI void dma(const char* sbase, u32 voff, u32 lds) { dmai<0, false>(sbase, voff, lds); }
DI void dma_nt(const char* sbase, u32 voff, u32 lds) { asm volatile("s_mov_b32 m0, %2\n\ts_nop 0\n\tglobal_load_lds_dwordx4 %0, %1 nt" ::"v"(voff), "s"(sbase), "s"(lds)); }
DI void gld32(u32& r, const char* sbase, u32 voff) { asm volatile("global_load_dword %0, %1, %2" : "=v"(r) : "v"(voff), "s"(sbase)); }
DI void gld32_sc1(u32& r, const char* sbase, u32 voff) { asm volatile("global_load_dword %0, %1, %2 sc1" : "=v"(r) : "v"(voff), "s"(sbase)); }
template <int IMM> DI void gld_sc1(u32x4& r, const char* sbase, u32 voff) {
  asm volatile("global_load_dwordx4 %0, %1, %2 offset:%3 sc1" : "=v"(r) : "v"(voff), "s"(sbase), "i"(IMM));
}
template <int IMM> DI void gld(u32x4& r, const char* sbase, u32 voff) {
  asm volatile("global_load_dwordx4 %0, %1, %2 offset:%3" : "=v"(r) : "v"(voff), "s"(sbase), "i"(IMM));
}
DI void gld64(u32x2& r, const char* sbase, u32 voff) {
  asm volatile("global_load_dwordx2 %0, %1, %2" : "=v"(r) : "v"(voff), "s"(sbase));
}
DI void poll_ge(const char* ws, u32 off, u32 want) {
  gu32* p = (gu32*)(ws + off);
  for (u32 spins = 0; spins < SPIN_CAP; ++spins) {
    if ((u32)__builtin_amdgcn_readfirstlane(__hip_atomic_load(p, RLX_AGENT)) >= want) break;
    __builtin_amdgcn_s_sleep(1);
  }
  __builtin_amdgcn_fence(__ATOMIC_ACQUIRE, "wavefront");
}
DI void signal(const char* ws, u32 off) { __hip_atomic_fetch_add((gu32*)(ws + off), 1u, RLX_AGENT); }

struct P1 {
  const u16 *x, *wqkv, *wo, *cvec, *wmod;
  char* ws;
};
#define K1AUX 0
__global__ void __launch_bounds__(256) NOPK k1(P1 p) {
  __shared__ float red[32 * 64 + 4 * 64 + 320];
  const int b = bidx(), t = tidx();
  char* ws = p.ws;
  const __amdgpu_buffer_rsrc_t rs = __builtin_amdgcn_make_buffer_rsrc(p.ws, 0, WS_END, 0x00020000);
  if (b >= 408 && b < 424) {
    unsigned long long pc;
    asm volatile("s_getpc_b64 s[40:41]\n\ts_add_u32 s40, s40, _ZN12_GLOBAL__N_12k2ENS_2P2E@rel32@lo+4\n\ts_addc_u32 s41, s41, _ZN12_GLOBAL__N_12k2ENS_2P2E@rel32@hi+12\n\ts_mov_b64 %0, s[40:41]"
                 : "=s"(pc) : : "scc", "s40", "s41");
    const int o = ((b - 408) >> 3) * 256 + t;
    if (o < K2_LINES) { u32 v_ = *(const GAS u32*)(pc + (size_t)o * 128); asm volatile("" ::"v"(v_)); }
  }
  if (b < 120) {
    const int ch = t & 7, r = t >> 3, col0 = 64 * (b >> 1), row0 = 320 * (b & 1);
    const u32x4 cv = *(const u32x4*)(p.cvec + row0 + 8 * (t < 40 ? t : 39));
    u32x4 wv[10];
#pragma unroll
    for (int i = 0; i < 10; ++i) wv[i] = __builtin_nontemporal_load((const u32x4*)(p.wmod + (size_t)(row0 + r + 32 * i) * 3840 + col0 + 8 * ch));
    if (t < 40) {
#pragma unroll
      for (int e = 0; e < 8; ++e) {
        const float c = (e & 1) ? hi16(cv[e >> 1]) : lo16(cv[e >> 1]);
        red[2304 + 8 * t + e] = rbf(c * __builtin_amdgcn_rcpf(1.f + __builtin_amdgcn_exp2f(-1.4426950408889634f * c)));
      }
    }
    BAR();
    float acc[8] = {0, 0, 0, 0, 0, 0, 0, 0};
#pragma unroll
    for (int i = 0; i < 10; ++i) {
      const float s = red[2304 + r + 32 * i];
#pragma unroll
      for (int e = 0; e < 4; ++e) { acc[2 * e] = fma1(s, lo16(wv[i][e]), acc[2 * e]); acc[2 * e + 1] = fma1(s, hi16(wv[i][e]), acc[2 * e + 1]); }
    }
    *(f32x4*)&red[r * 64 + 8 * ch] = f32x4{acc[0], acc[1], acc[2], acc[3]};
    *(f32x4*)&red[r * 64 + 8 * ch + 4] = f32x4{acc[4], acc[5], acc[6], acc[7]};
    BAR();
    { const int c = t & 63, part = t >> 6; float s = 0;
#pragma unroll
      for (int i = 0; i < 8; ++i) s += red[(part * 8 + i) * 64 + c];
      red[2048 + part * 64 + c] = s; }
    BAR();
    if (t < 64) {
      const float s = (red[2048 + t] + red[2112 + t]) + (red[2176 + t] + red[2240 + t]);
      __builtin_amdgcn_raw_buffer_store_b32(f2u(s), rs, WS_MODP + ((b & 1) * 3840 + col0 + t) * 4, 0, K1AUX);
    }
    if (b == 0) {
      *(u32*)(ws + WS_KVF + t * 128) = 0u;
      if (t < 72) *(u32*)(ws + WS_CNT + t * 256) = 0u;
    }
  } else if (b < 248) {
    const int hb = b - 120, c = t & 15, tok = 16 * (hb & 1) + (t >> 4);
    const u16* row = p.x + (size_t)(32 * (hb >> 1) + tok) * D;
    u32x4 v[5];
#pragma unroll
    for (int i = 0; i < 5; ++i) v[i] = *(const u32x4*)(row + 8 * (c + 16 * i));
    float s = 0;
#pragma unroll
    for (int i = 0; i < 5; ++i)
#pragma unroll
      for (int e = 0; e < 4; ++e) s += lo16(v[i][e]) + hi16(v[i][e]);
    s += dppf<0xB1>(s); s += dppf<0x4E>(s); s += dppf<0x141>(s); s += dppf<0x140>(s);
    const float mean = s * (1.f / 640.f);
    float q = 0;
#pragma unroll
    for (int i = 0; i < 5; ++i)
#pragma unroll
      for (int e = 0; e < 4; ++e) { const float a = lo16(v[i][e]) - mean, bb = hi16(v[i][e]) - mean; q = fma1(a, a, q); q = fma1(bb, bb, q); }
    q += dppf<0xB1>(q); q += dppf<0x4E>(q); q += dppf<0x141>(q); q += dppf<0x140>(q);
    const float mb = rbf(mean), den = rbf(__builtin_amdgcn_sqrtf(rbf(rbf(q * (1.f / 640.f)) + 1e-5f))), rr = __builtin_amdgcn_rcpf(den);
    const u32 dst = WS_N1 + (u32)(hb >> 1) * 40960 + tok * 16;
#pragma unroll
    for (int i = 0; i < 5; ++i) {
      u32x4 o;
#pragma unroll
      for (int e = 0; e < 4; ++e) {
        const u32 d = pk2(lo16(v[i][e]) - mb, hi16(v[i][e]) - mb);
        const u32 n = pk2(lo16(d) * rr, hi16(d) * rr);
        o[e] = pkh2(lo16(n), hi16(n));
      }
      __builtin_amdgcn_raw_buffer_store_b128(o, rs, dst + (c + 16 * i) * 512, 0, K1AUX);
    }
  } else {
    const u16* src; int stride; u32 off0, off1; bool act = true;
    if (b < 408) {
      const int idx = b - 248, ks = idx >> 2, jq = idx & 3, tc = t < 240 ? t : 239;
      act = t < 240;
      const int seg = tc / 80, cp = tc - 80 * seg;
      src = p.wqkv + (size_t)(16 * ks) * 1920 + 640 * seg + 160 * jq + 2 * cp; stride = 1920;
      const int jj = (2 * cp) / 40, within = 2 * cp - 40 * jj, j = 4 * jq + jj;
      const int n = (seg == 1 ? 0 : seg == 2 ? 40 : 80) + within, nb = n >> 5, r = n & 31;
      off0 = WS_WQF + j * WQB + (nb < 3 ? ((nb * 40 + ks) * 64 + r) * 16 : 122880 + ks * 768 + r * 16);
      off1 = off0 + (nb < 3 ? 512 : 384);
    } else {
      const int idx = b - 408, k16 = idx >> 1, tc = t < 160 ? t : 159, n = (idx & 1) * 320 + 2 * tc;
      act = t < 160;
      src = p.wo + (size_t)(16 * k16) * 640 + n; stride = 640;
      const int kh = k16 >= 20, kk = k16 - 20 * kh, st = kk >> 1, H = kk & 1;
      const int ntg = n >> 4, n8 = ntg / 5, nt = ntg - 5 * n8;
      off0 = WS_WOF + n8 * WOB + (st * 10 + kh * 5 + nt) * 1024 + (2 * H * 16 + (n & 15)) * 16;
      off1 = off0 + 256;
    }
    u32 v[16];
#pragma unroll
    for (int i = 0; i < 16; ++i) v[i] = __builtin_nontemporal_load((const u32*)(src + (size_t)i * stride));
    if (b < 408) {
#pragma unroll
      for (int i = 0; i < 16; ++i) v[i] = pkh2(lo16(v[i]) * 64.f, hi16(v[i]) * 64.f);
    }
    if (act) {
#pragma unroll
      for (int h = 0; h < 2; ++h) {
        u32x4 a, bq;
#pragma unroll
        for (int e = 0; e < 4; ++e) {
          a[e] = __builtin_amdgcn_perm(v[8 * h + 2 * e + 1], v[8 * h + 2 * e], 0x05040100u);
          bq[e] = __builtin_amdgcn_perm(v[8 * h + 2 * e + 1], v[8 * h + 2 * e], 0x07060302u);
        }
        __builtin_amdgcn_raw_buffer_store_b128(a, rs, h ? off1 : off0, 0, K1AUX);
        __builtin_amdgcn_raw_buffer_store_b128(bq, rs, (h ? off1 : off0) + 16, 0, K1AUX);
      }
    }
  }
}

struct P2 {
  const u16 *x, *wg, *wu, *wd;
  u16* out;
  char* ws;
};
#define MAP 0
constexpr u32 C_O = WS_CNT, C_RG = WS_CNT + 48 * 256, C_RD = WS_CNT + 64 * 256;
#define OPQ(x) asm volatile("" : "+s"(x))

DI constexpr int gu_unit(int p, int kh, int i) {
  return p == 0 ? 20 * kh + i
       : p == 1 ? (i < 16 ? 40 + 16 * kh + i : 8 + 20 * kh + (i - 16))
                : (i < 8 ? 12 + 20 * kh + i : 40 + 16 * kh + (i - 8));
}
DI constexpr u32 wo_slot(int s) { return s < 7 ? (72 + 10 * s) * 1024 : (s - 7) * 10240; }
DI constexpr u32 wd_slot(int s) { return (s < 15 ? s : s - 15) * 10240; }
template <int N, int NW, bool SC1> struct Run {
  static constexpr int C = (N + NW - 1) / NW;
  const char* src; u32 lds;
  DI Run(const char* s, u32 l, int x) { int o = x * C; o = (o < N - C ? o : N - C) * 1024; src = s + o; lds = l + o; }
  template <int M> DI void go(u32 l16) const { if constexpr (M < C) dmai<(M & 3) * 1024, SC1>(src + (M >> 2) * 4096, l16, lds + (M >> 2) * 4096); }
  DI void all(u32 l16) const { sfor<0, C>([&](auto m_) LAM { go<decltype(m_)::v>(l16); }); }
};
template <int P, int I0, int I1, int NW> DI auto gu_run(const char* blob, int x) {
  const int k = x >= NW / 2;
  return Run<2 * (I1 - I0), NW / 2, true>(blob + ((P * 2 + k) * 20 + I0) * 2048, (k ? gu_unit(P, 1, I0) : gu_unit(P, 0, I0)) * 2048, x - k * (NW / 2));
}
DI u32 pkfma16(u32 x, u32 y, u32 z) { return __builtin_bit_cast(u32, __builtin_elementwise_fma(__builtin_bit_cast(f16x2, x), __builtin_bit_cast(f16x2, y), __builtin_bit_cast(f16x2, z))); }
DI void gld4_if(int cond, u32x4& r0, u32x4& r1, u32x4& r2, u32x4& r3, const char* sbase, u32 voff) {
  asm volatile("s_cmp_eq_u32 %6, 0\n\ts_cbranch_scc1 .Lskip%=\n\tglobal_load_dwordx4 %0, %4, %5\n\tglobal_load_dwordx4 %1, %4, %5 offset:1024\n\t"
               "global_load_dwordx4 %2, %4, %5 offset:2048\n\tglobal_load_dwordx4 %3, %4, %5 offset:3072\n.Lskip%=:"
               : "+v"(r0), "+v"(r1), "+v"(r2), "+v"(r3) : "v"(voff), "s"(sbase), "s"(cond) : "scc");
}
#define NRS 12
DI constexpr int dn_nA(int s) { return s < 3 ? 3 : s == 8 || s == 10 || s == 13 ? 3 : 0; }
DI constexpr int dn_nD(int s) { return s >= 5 && s < 8 ? 4 : s == 8 ? 1 : s >= 9 && s < 14 ? 2 : 0; }
DI constexpr int dn_cumA(int s) { int c = 6; for (int u = 0; u < s; ++u) c += dn_nA(u); return c; }
DI constexpr int dn_young(int s, bool old, int a_idx, int d_step) {
  int pos = 0, tgt = -1, na = 6;
  if (a_idx >= 0 && a_idx < 6) tgt = a_idx;
  pos = 6;
  for (int u = 0; u < s; ++u) {
    if (old) { pos += dn_nD(u); if (u == d_step) tgt = pos - 1; }
    for (int q = 0; q < dn_nA(u); ++q) { if (na == a_idx) tgt = pos; ++na; ++pos; }
  }
  return tgt < 0 ? 63 : pos - 1 - tgt;
}
DI constexpr int dn_wait(int s, bool old) {
  const int x = dn_young(s, old, s, -1), y = s == 13 ? dn_young(s, old, -1, 8) : s == 18 ? dn_young(s, old, -1, 13) : 63;
  return x < y ? x : y;
}
#define OWARM 0
DI bool has_sent(const u32x4& v) {
  const u32 m = __builtin_elementwise_max(__builtin_elementwise_max(v.x, v.y), __builtin_elementwise_max(v.z, v.w));
  return __builtin_amdgcn_ballot_w64(m == 0xffffffffu) != 0ull;
}
template <int IMM> DI void ensure(u32x4& v, const char* sbase, u32 voff) {
  if (__builtin_expect(has_sent(v), 0)) {
    for (u32 spins = 0; spins < SPIN_CAP; ++spins) {
      gld_sc1<IMM>(v, sbase, voff); WAITV(0); PIN(v);
      if (!has_sent(v)) break;
    }
  }
}
DI void gate(const char* sbase, u32 voff) {
  for (u32 spins = 0; spins < SPIN_CAP; ++spins) {
    u32 v;
    gld32_sc1(v, sbase, voff); WAITV(0); PIN(v);
    if (__builtin_amdgcn_ballot_w64(v == 0xffffffffu) == 0ull) break;
  }
}

__global__ void __launch_bounds__(512, 2) NOPK k2(P2 p) {
  const int tid = tidx(), lane = tid & 63;
  int w = __builtin_amdgcn_readfirstlane(tid >> 6);
  const int b = bidx();
  int mt = MAP == 0 ? 4 * ((b & 7) >> 1) + (b >> 6) : (b & 7) * 2 + (b >> 7), j = MAP == 0 ? 8 * (b & 1) + ((b >> 3) & 7) : (b >> 3) & 15;
  const int c32 = lane & 31, h = lane >> 5;
  const char* ws = p.ws;
  const __amdgpu_buffer_rsrc_t rs = __builtin_amdgcn_make_buffer_rsrc(p.ws, 0, WS_END, 0x00020000);
  const u32 l16 = lane * 16;
#define PHASE() OPQ(w); OPQ(mt); OPQ(j); OPQ(ws); const int tt = w & 3, kh = w >> 2, th = j & 1, n8 = j >> 1; (void)tt; (void)kh; (void)th; (void)n8
#define FINE 0
#define CSEL 0
#define FST(k, i) do {} while (0)
#define STAMP(i) do {} while (0)
#define CST(k, i) do {} while (0)
#define PRIO 0
  if (PRIO == 1 && w >= 4) __builtin_amdgcn_s_setprio(1);
  STAMP(0); CST(3, 18);
  u32x4 a[24];
  u32x4 st[18];
  const int rsw = c32 ^ (16 * (w >> 2));
  const u32 frow = (h * 32 + rsw) * 16;
  u32 imga[8];
  {
    PHASE();
    const char* wq = ws + WS_WQF + (size_t)j * WQB;
    const char* wqw = wq + (kh * 20 + tt) * 1024;
    const u32 wql = (kh * 20 + tt) * 1024;
    const char* n1 = ws + WS_N1 + (size_t)((mt * 4 + tt) * 40 + kh * 20) * 1024;
    auto issA = [&](auto k_) LAM { constexpr int k = decltype(k_)::v; gld<(k & 3) * 1024>(a[k], n1 + (k >> 2) * 4096, l16); };
    auto issW = [&](int s, int nb) LAM { dma(wqw + nb * 40960 + s * 4096, l16, wql + nb * 40960 + s * 4096); };
    u32x4 mp[2];
    u32 mdst; bool plus1;
    {
      const int m1 = tid >= 160, k1 = 4 * (tid - 160 * m1);
      const int u2 = tid - 320, m2 = (u2 * 205) >> 12, e2 = u2 - 20 * m2;
      const int col2 = (m2 == 0 ? 1280 : m2 == 2 ? 3200 : m2 == 4 ? 1920 : 2560) + 80 * (m2 < 3 ? n8 : (mt >> 1)) + 4 * (m2 < 5 ? e2 : 0);
      const int col = tid < 320 ? 640 * m1 + k1 : col2;
#pragma unroll
      for (int q = 0; q < 2; ++q) gld<0>(mp[q], ws + WS_MODP + q * 15360, (u32)col * 4);
      sfor<0, 4>(issA);
      issW(0, 0); issW(0, 1); issW(0, 2);
      mdst = tid < 320 ? L_GS + (k1 >> 3) * 32 + (m1 ? 0 : 16) + (k1 & 4) * 2 : L_MODS + u2 * 16;
      plus1 = tid < 320 ? m1 != 0 : (m2 & 1) != 0;
    }
    if (tid == 0) st128(L_FLAG, u32x4{0u, 0u, 0u, 0u});
    const int rk16 = w < 5 ? 5 * (mt >> 1) + w : (mt * 2 + th) * 3 + (w - 5);
    const char* rsrc = w < 5 ? (const char*)((mt & 1) ? p.wu : p.wg) + (size_t)rk16 * 49152 + 192 * j : (const char*)p.wd + (size_t)rk16 * 20480 + 160 * n8;
    const u32 rimg = 122880 + w * 3072;
    auto issR = [&]() LAM {
#pragma unroll
      for (int m = 0; m < 3; ++m) {
        const u32 x = 64 * m + lane, i5 = x < 160 ? x : 159, r12 = (x * 171) >> 11, r10 = (i5 * 205) >> 11;
        dma_nt(rsrc, w < 5 ? r12 * 2880 + x * 16 : r10 * 1120 + i5 * 16, rimg + m * 1024);
      }
    };
    f32x16 acc[3];
    const u32 wb = kh * 20480 + frow, gb_ = L_GS + kh * 1280 + h * 32;
    u32x4 gs[3][2], wf[2][3], ro[3];
    f32x4 csum = {0.f, 0.f, 0.f, 0.f};
    auto ldgs = [&](int k) LAM { gs[k % 3][0] = lds128(gb_ + k * 64); gs[k % 3][1] = lds128(gb_ + k * 64 + 16); };
    auto modulate = [&](int k) LAM {
      PIN(a[k]);
#pragma unroll
      for (int e = 0; e < 4; ++e)
        a[k][e] = pkfma16(a[k][e], gs[k % 3][0][e], gs[k % 3][1][e]);
    };
    FST(3, 1);
    WAITV(7);
    FST(3, 2);
    f32x4 mv;
    {
#pragma unroll
      for (int q = 0; q < 2; ++q) PIN(mp[q]);
      float mxv = 0.f;
#pragma unroll
      for (int e = 0; e < 4; ++e) {
        const float s = rbf(u2f(mp[0][e]) + u2f(mp[1][e]));
        mv[e] = plus1 ? rbf(1.f + s) : s;
        mxv = __builtin_fmaxf(mxv, __builtin_fabsf(mv[e]));
      }
      if (tid < 320) st64(mdst, u32x2{pkh2(mv[0], mv[1]), pkh2(mv[2], mv[3])}); else if (tid < 420) stf4(mdst, mv);
      const bool big = __builtin_amdgcn_ballot_w64(tid < 320 && !(mxv < 2000.f)) != 0ull;
      if (lane == 0) *(LAS u32*)(size_t)(L_FLAG + 16 + w * 4) = big;
    }
    WAITV(0);
    BAR();
    FST(3, 3);
    auto grpA = [&](int cond, int s) LAM { gld4_if(cond, a[4 * s], a[4 * s + 1], a[4 * s + 2], a[4 * s + 3], n1 + s * 4096, l16); };
    const int kl = kh ^ 1;
    auto grpW = [&](int s) LAM { issW(s, 0); issW(s, 1); issW(s, 2); };
    grpA(kh, 1); if (kh) { grpW(1); issR(); }
    bool scaled;
    {
      const u32x4 f0 = lds128(L_FLAG + 16);
      const u32 f1 = *(const LAS u32*)(size_t)(L_FLAG + 32);
      ldgs(0); ldgs(1);
      scaled = __builtin_amdgcn_readfirstlane(f0.x | f0.y | f0.z | f0.w | f1) != 0;
      if (scaled) {
        BAR();
        const float sc = 9.765625e-4f;
        if (tid < 320) st64(mdst, u32x2{pkh2(mv[0] * sc, mv[1] * sc), pkh2(mv[2] * sc, mv[3] * sc)});
        BAR();
        ldgs(0); ldgs(1);
      }
    }
    modulate(0);
    SB();
    const Run<30, 8, false> rb3(wq + 122880, 122880, w);
    sfor<0, 21>([&](auto k_) LAM {
      constexpr int k = decltype(k_)::v, s = k >> 2, i = k & 3;
      if constexpr (i == 3 && k < 16) WAITV(k == 3 ? 13 : 10);
      if constexpr (i == 0 && k > 0 && k < 20) {
        FST(3, 3 + 3 * s); WAITV(k == 4 ? 10 : k == 16 ? 4 : 7); FST(3, 4 + 3 * s); BAR(); FST(3, 5 + 3 * s);
        if (s == 2 && CSEL == 4) { CST(4, 18); STAMP(16); } if (s == 3 && CSEL == 4) { CST(4, 19); STAMP(17); }
      }
      if constexpr (k < 20) {
#pragma unroll
        for (int nb = 0; nb < 3; ++nb) wf[k & 1][nb] = lds128(wb + nb * 40960 + k * 1024);
      }
      if constexpr (k == 1) {
        f32x16 z;
#pragma unroll
        for (int e = 0; e < 16; ++e) z[e] = 0.f;
#pragma unroll
        for (int nb = 0; nb < 3; ++nb) acc[nb] = MFMA32H(wf[0][nb], a[0], z);
      } else if constexpr (k > 1) {
#pragma unroll
        for (int nb = 0; nb < 3; ++nb) acc[nb] = MFMA32H(wf[(k - 1) & 1][nb], a[k - 1], acc[nb]);
      }
      if constexpr (k + 1 < 20) modulate(k + 1);
      if constexpr (k + 2 < 20) ldgs(k + 2);
      if constexpr (k == 8) {
        const u32 q4 = (lane >> 2) & 3, nh = (lane >> 4) & 1;
        const u32 r0 = rimg + (w < 5 ? (8 * h + q4) * 192 + 32 * nh : (4 * nh + q4) * 160 + 32 * h) + (lane & 3) * 8, r1 = r0 + (w < 5 ? 768 : 1280);
#pragma unroll
        for (int u = 0; u < 3; ++u) ro[u] = ldstr2(r0 + 64 * u, r1 + 64 * u);
      }
      if constexpr (k == 9 || k == 10) {
        if (w < 5) {
          const u32 ga_ = L_MODS + 960 + w * 64 + h * 32;
          const f32x4 g0 = ldsf4(ga_), g1 = ldsf4(ga_ + 16), s0 = ldsf4(ga_ + 320), s1 = ldsf4(ga_ + 336);
#pragma unroll
          for (int u = (k == 9 ? 0 : 2); u < (k == 9 ? 2 : 3); ++u) {
            float c = 0.f, bi = 0.f;
#pragma unroll
            for (int e = 0; e < 4; ++e) {
              const u32 v = ro[u][e];
              const float x0 = lo16(v), x1 = hi16(v), ge = e < 2 ? g0[2 * e] : g1[2 * e - 4], go = e < 2 ? g0[2 * e + 1] : g1[2 * e - 3];
              const float se = e < 2 ? s0[2 * e] : s1[2 * e - 4], so_ = e < 2 ? s0[2 * e + 1] : s1[2 * e - 3];
              c = fma1(ge, x0, c); c = fma1(go, x1, c); bi = fma1(se, x0, bi); bi = fma1(so_, x1, bi);
            }
            auto r = __builtin_amdgcn_permlane32_swap(f2u(c), f2u(bi), false, false);
            *(LAS float*)(size_t)(w * 768 + u * 256 + (c32 >> 3) * 64 + h * 32 + (c32 & 7) * 4) = u2f(r[0]) + u2f(r[1]);
          }
        }
      }
      if constexpr (k == 17) {
        if (w == 7) {
#pragma unroll
          for (int u = 0; u < 5; ++u) csum += ldsf4(u * 768 + (lane < 48 ? l16 : 0u));
        }
      }
      if constexpr (k == 16) {
        const u32 x2 = 16 * kh + 4 * h, tb = 12288 + (32 * tt + c32) * 48 + 2 * x2;
#pragma unroll
        for (int u = 0; u < 8; ++u) {
          const u32 part = ((x2 + 32 * (u >> 1) + 8 * (u & 1)) * 205) >> 12;
          imga[u] = part * 18392 + tb + 64 * (u >> 1) + 16 * (u & 1); PIN(imga[u]);
        }
      }
      SB();
      if constexpr (k == 0) { grpA(kl, 1); if (!kh) { grpW(1); issR(); } }
      if constexpr (k == 1) grpA(kl, 2);
      if constexpr (k == 2) { if (!kh) grpW(2); grpA(kh, 2); }
      if constexpr (k == 3) { if (kh) grpW(2); }
      if constexpr (s == 1 || s == 2) {
        if constexpr (i == 0) grpA(kl, s + 2);
        if constexpr (i == 1) { if (!kh) grpW(s + 2); }
        if constexpr (i == 2) grpA(kh, s + 2);
        if constexpr (i == 3) { if (kh) grpW(s + 2); }
      }
      if constexpr (k == 12) { if (!kh) rb3.all(l16); }
      if constexpr (k == 14) { if (kh) rb3.all(l16); }
      if constexpr (k == 16) {
        u32 off0, du;
        if (w < 5) { const int k2 = rk16 >= 20, i2 = rk16 - 20 * k2; off0 = WS_WGF + j * WGB + (k2 * 20 + i2) * 2048 + (mt & 1) * 1024 + l16; du = 81920; }
        else { const int k2 = rk16 >= 48, kk = rk16 - 48 * k2; off0 = WS_WDF + n8 * WDB + ((kk >> 1) * 10 + k2 * 5 + h) * 1024 + (kk & 1) * 512 + c32 * 16; du = 2048; }
        asm volatile("" ::: "memory");
        __builtin_amdgcn_raw_buffer_store_b128(ro[0], rs, off0, 0, 16);
        __builtin_amdgcn_raw_buffer_store_b128(ro[1], rs, off0 + du, 0, 16);
        __builtin_amdgcn_raw_buffer_store_b128(ro[2], rs, (w < 5 || h == 0) ? off0 + 2 * du : 0xfffffff0u, 0, 16);
        asm volatile("" ::: "memory");
      }
      SB();
    });
    STAMP(1); CST(3, 19); FST(6, 1);
    WAITV(3);
    BAR();
    FST(6, 2);
    if (w == 7 && lane < 48) __builtin_amdgcn_raw_buffer_store_b128(__builtin_bit_cast(u32x4, csum), rs, WS_CSP + (u32)(j * 16 + mt) * 768 + l16, 0, 16);
    const u32 exw = (w >> 1) * 18432 + (w & 1) * 6144 + l16, exr = ((w ^ 4) >> 1) * 18432 + (w & 1) * 6144 + l16;
#pragma unroll
    for (int nb = 0; nb < 3; ++nb) {
      stf4(exw + nb * 2048, f32x4{acc[nb][8], acc[nb][9], acc[nb][10], acc[nb][11]});
      stf4(exw + nb * 2048 + 1024, f32x4{acc[nb][12], acc[nb][13], acc[nb][14], acc[nb][15]});
    }
    if (kh) st64((tid >> 7) * 18432 + 12288 + (tid & 127) * 48 + 40, u32x2{0x3f803f80u, 0x3f803f80u});
    BAR();
    FST(6, 3);
    const float osc = scaled ? 1024.f : 1.f, qs = QSCALE * (1.f / 4096.f) * osc, sc2 = kh ? qs : osc;
#pragma unroll
    for (int nb = 0; nb < 3; ++nb)
#pragma unroll
      for (int q = 0; q < 2; ++q) {
        const f32x4 o = ldsf4(exr + nb * 2048 + q * 1024);
        float v0 = acc[nb][4 * q] + o[0], v1 = acc[nb][4 * q + 1] + o[1], v2 = acc[nb][4 * q + 2] + o[2], v3 = acc[nb][4 * q + 3] + o[3];
        if (nb == 2) { v0 *= sc2; v1 *= sc2; v2 *= sc2; v3 *= sc2; }
        else if (scaled) { v0 *= osc; v1 *= osc; v2 *= osc; v3 *= osc; }
        st64(imga[2 * nb + q], u32x2{pk2(v0, v1), pk2(v2, v3)});
      }
    BAR();
    FST(6, 4);
    {
      const u32 dst = WS_KVX + (u32)(mt * 16 + j) * KVB + w * 3072 + l16, src = (w >> 1) * 18432 + 12288 + (w & 1) * 3072 + l16;
#pragma unroll
      for (int i = 0; i < 3; ++i) __builtin_amdgcn_raw_buffer_store_b128(lds128(src + i * 1024), rs, dst + i * 1024, 0, 16);
    }
    f32x16 acc3;
#pragma unroll
    for (int e = 0; e < 16; ++e) acc3[e] = 0.f;
    {
      const int r3 = rsw < 24 ? rsw : (rsw < 28 ? rsw - 4 : rsw - 12);
      const u32 wb3 = 122880 + kh * 15360 + (h * 24 + r3) * 16;
      u32x4 w3[4];
#pragma unroll
      for (int k = 0; k < 3; ++k) w3[k] = lds128(wb3 + k * 768);
#pragma unroll
      for (int k = 0; k < 20; ++k) {
        if (k + 3 < 20) w3[(k + 3) & 3] = lds128(wb3 + (k + 3) * 768);
        acc3 = MFMA32H(w3[k & 3], a[k], acc3);
        if (k == 9) {
          FST(6, 5);
          WAITVM(0);
          FST(6, 6);
          if (lane == 0) {
            const u32 old = __hip_atomic_fetch_add((LAS u32*)(size_t)L_FLAG, 1u, __ATOMIC_RELAXED, __HIP_MEMORY_SCOPE_WORKGROUP);
            if (old == 7u) {
              __hip_atomic_store((gu32*)(ws + WS_KVF + (mt * 16 + j) * 128), 1u, RLX_AGENT);
              signal(ws, C_RG + j * 256); signal(ws, C_RD + n8 * 256);
            }
          }
        }
        SB();
      }
    }
    const u32 ex3w = (w < 4 ? 73728 : 84992) + w * 2048 + l16, ex3r = (w < 4 ? 93184 : 65536) + w * 2048 + l16;
    stf4(ex3w, f32x4{acc3[8], acc3[9], acc3[10], acc3[11]});
    stf4(ex3w + 1024, f32x4{acc3[12], acc3[13], acc3[14], acc3[15]});
    FST(6, 7);
    BAR();
    FST(6, 8);
    STAMP(2);
#pragma unroll
    for (int q = 0; q < 2; ++q) {
      const f32x4 o = ldsf4(ex3r + q * 1024);
      st64(imga[6 + q], u32x2{pk2((acc3[4 * q] + o[0]) * qs, (acc3[4 * q + 1] + o[1]) * qs), pk2((acc3[4 * q + 2] + o[2]) * qs, (acc3[4 * q + 3] + o[3]) * qs)});
    }
    BAR();
    FST(6, 9);
  }
  u32x2 xr[3];
  const int c16 = lane & 15, g4 = lane >> 4;
  {
    PHASE();
    const int hh = w >> 2, qb = w < 4 ? w : 7 - w;
    u32x4 q0f, q1f;
    {
      const u32 qa = (4 + hh) * 18432 + 12288 + (32 * qb + c32) * 48;
      const u32x2 x2 = lds64(qa + 32);
      q0f = lds128(qa + 16 * h);
      q1f = u32x4{h ? 0u : x2.x, h ? 0u : x2.y, 0u, 0u};
      const int tok64 = 128 * mt + 64 * th + 16 * tt + c16;
#pragma unroll
      for (int m = 0; m < 3; ++m) gld64(xr[m], (const char*)p.x, (u32)(tok64 * 1280 + (80 * n8 + 16 * ((m + 3 * kh) % 5) + 4 * g4) * 2));
    }
    BAR();
    STAMP(3); STAMP(4); CST(1, 18); FST(4, 12);
    const char* sg0 = ws + WS_WGF + (size_t)j * WGB + (kh * 40 + tt * 10) * 1024;
    const int tmin = mt >= 2 ? 0 : 8 - 4 * mt - qb;
    const u32 kb = hh * 18432 + (32 * qb + c32) * 48, kb2 = kb + 16 * h;
    const u32 vb = (2 + hh) * 18432 + (32 * qb + 4 * h + ((lane >> 2) & 3)) * 48 + ((lane >> 4) & 1) * 32 + (lane & 3) * 8;
    const int qlim = c32 - 4 * h;
    const int dw = (w - 2) & 3;
    const bool duty = w >= 2 && w <= 5;
    const Run<70, 4, false> rwo(ws + WS_WOF + (size_t)n8 * WOB, wo_slot(0), dw);
    const u32 fa = WS_ACT + (u32)((mt * 4 + dw) * 96 + 6 * j) * 1024 + l16;
    const u32 fx = dw < 2 ? WS_X1 + (u32)((mt * 4 + 2 * th + dw) * 40 + 5 * n8) * 1024 + l16 : WS_STAT + (u32)((mt * 4 + 2 * th + h) * 8 + n8) * 512 + c32 * 16;
    const int ko = 40 * j + 20 * hh + 4 * h;
    u32 ob = WS_OAF + (u32)((mt * 4 + qb) * 40) * 1024 + c32 * 16 + (ko >> 3) * 512 + ((ko >> 2) & 1) * 8;
    PIN(ob);
    auto lds_wait = [&](u32 addr, u32 want) LAM {
      for (u32 spins = 0; spins < SPIN_CAP * 8; ++spins) {
        if ((u32)__builtin_amdgcn_readfirstlane(__hip_atomic_load((LAS u32*)(size_t)addr, __ATOMIC_RELAXED, __HIP_MEMORY_SCOPE_WORKGROUP)) >= want) break;
        __builtin_amdgcn_s_sleep(1);
      }
      asm volatile("" ::: "memory");
    };
    {
      if (!duty) {
        const int sl = w >= 6;
        const u32 l0 = (w & 1) * 36864 + sl * 6144;
        if (mt + sl >= 2) {
          poll_ge(ws, WS_KVF + ((mt - 2 + sl) * 16 + j) * 128, 1u);
          const char* src = ws + WS_KVX + (size_t)((mt - 2 + sl) * 16 + j) * KVB + (w & 1) * 12288;
#pragma unroll
          for (int u = 0; u < 4; ++u) {
            const u32 l1 = l0 + (u >> 1) * 18432 + (u & 1) * 3072;
            dmai<0, true>(src + u * 3072, l16, l1); dmai<1024, true>(src + u * 3072, l16, l1); dmai<2048, true>(src + u * 3072, l16, l1);
          }
        } else {
#pragma unroll
          for (int u = 0; u < 12; ++u) st128(l0 + (u / 6) * 18432 + (u % 6) * 1024 + l16, u32x4{0u, 0u, 0u, 0u});
        }
      }
    }
    auto halo = [&]() LAM {
      if (!duty) {
        asm volatile("s_waitcnt vmcnt(0) lgkmcnt(0)" ::: "memory");
        if (lane == 0) __hip_atomic_fetch_add((LAS u32*)(size_t)(L_FLAG + (w >= 6 ? 8 : 4)), 1u, __ATOMIC_RELAXED, __HIP_MEMORY_SCOPE_WORKGROUP);
      }
    };
    f32x16 oacc;
    u32 rcnt;
    auto attn = [&](auto full_) LAM {
      constexpr bool FULL = decltype(full_)::v;
      u32x4 ka, kq, vf[2][2];
      kq = u32x4{0u, 0u, 0u, 0u};
      auto ldkv = [&](int t) LAM {
        const u32x2 x2 = lds64(kb + t * 1536 + 32);
        ka = lds128(kb2 + t * 1536); kq.x = x2.x; kq.y = x2.y;
#pragma unroll
        for (int s2 = 0; s2 < 2; ++s2) { const u32 va = vb + (32 * t + 16 * s2) * 48; vf[t & 1][s2] = ldstr2(va, va + 384); }
      };
      auto splat = [&](float v) LAM -> f32x16 {
        f32x16 c;
#pragma unroll
        for (int e = 0; e < 16; ++e) c[e] = v;
        return c;
      };
      auto pre = [&](auto m_) LAM {
        constexpr int m = decltype(m_)::v;
        if constexpr (m == 2) gld32_sc1(rcnt, ws, lane < 32 ? C_RG + j * 256 : C_RD + n8 * 256);
        if constexpr (m == 4) {
          if (duty) WAITV(6); else WAITV(0);
          PIN(rcnt);
          if (__builtin_amdgcn_ballot_w64(rcnt < (lane < 32 ? 16u : 32u)) != 0ull) { poll_ge(ws, C_RG + j * 256, 16u); poll_ge(ws, C_RD + n8 * 256, 32u); }
        }
        if constexpr (m >= 4) {
          gld_sc1<((2 * m - 8) & 3) * 1024>(st[2 * m - 8], sg0 + ((2 * m - 8) >> 2) * 4096, l16);
          gld_sc1<((2 * m - 7) & 3) * 1024>(st[2 * m - 7], sg0 + ((2 * m - 7) >> 2) * 4096, l16);
        }
        if constexpr (m == 1 && OWARM) {
#pragma unroll
          for (int g = 0; g < 3; ++g)
            if (g < 2 || h == 0) __builtin_amdgcn_raw_buffer_store_b64(u32x2{0u, 0u}, rs, ob + g * 512, 0, 16);
          asm volatile("" ::: "memory");
        }
        if (duty) {
          rwo.go<2 * m>(l16); rwo.go<2 * m + 1>(l16);
          const u32x4 f = {~0u, ~0u, ~0u, ~0u};
          if constexpr (m < 6) __builtin_amdgcn_raw_buffer_store_b128(f, rs, fa + m * 1024, 0, 16);
          if constexpr (m >= 4) { if (dw < 2 || (dw == 2 && m == 4)) __builtin_amdgcn_raw_buffer_store_b128(f, rs, fx + (m - 4) * 1024, 0, 16); }
          asm volatile("" ::: "memory");
        }
        SB();
      };
      auto sync = [&](int t) LAM {
        if (t >= 4) { if (qb == 7 - t) lds_wait(L_FLAG + 8, 2u); } else { if (qb == 3 - t) lds_wait(L_FLAG + 4, 2u); }
      };
      f32x16 sb[2], cm;
      float pe[16];
      FST(4, 1);
      ldkv(8);
      sb[0] = MFMA32(ka, q0f, splat(0.f));
      sb[0] = MFMA32(kq, q1f, sb[0]);
      SB();
      pre(IC<0>{});
#pragma unroll
      for (int e = 0; e < 16; ++e) pe[e] = ((e & 3) + 8 * (e >> 2) > qlim) ? -1.0e30f : sb[0][e];
      float mx = __builtin_fmaxf(pe[0], pe[1]);
#pragma unroll
      for (int e = 2; e < 16; e += 2) mx = max3(mx, pe[e], pe[e + 1]);
      mx = -max32(mx);
      cm = splat(mx);
      auto cin = [&](int t) LAM -> f32x16 { if constexpr (FULL) return cm; else return splat(t >= tmin ? mx : -1.0e30f); };
#pragma unroll
      for (int s2 = 0; s2 < 2; ++s2) {
        u32x4 pf;
#pragma unroll
        for (int e = 0; e < 4; ++e) pf[e] = pk2(__builtin_amdgcn_exp2f(pe[8 * s2 + 2 * e] + mx), __builtin_amdgcn_exp2f(pe[8 * s2 + 2 * e + 1] + mx));
        if (s2 == 0) oacc = MFMA32(vf[0][0], pf, splat(0.f)); else oacc = MFMA32(vf[0][1], pf, oacc);
      }
      SB();
      FST(4, 2);
      halo();
      pre(IC<1>{});
      sync(7);
      ldkv(7);
      sb[1] = MFMA32(ka, q0f, cin(7));
      sb[1] = MFMA32(kq, q1f, sb[1]);
      SB();
      sfor<0, 8>([&](auto u_) LAM {
        constexpr int t = 7 - decltype(u_)::v, c = t & 1, n = c ^ 1;
        u32x4 pf0, pf1;
        FST(4, 3 + decltype(u_)::v);
        if constexpr (t > 0) { if constexpr (t - 1 != 7) sync(t - 1); ldkv(t - 1); }
        if constexpr (t == 0) {
#pragma unroll
          for (int e = 0; e < 16; ++e) sb[c][e] = ((e & 3) + 8 * (e >> 2) > qlim) ? sb[c][e] : -1.0e30f;
        }
#pragma unroll
        for (int e = 0; e < 8; ++e) pe[e] = __builtin_amdgcn_exp2f(sb[c][e]);
        SB();
        if constexpr (t > 0) { sb[n] = MFMA32(ka, q0f, cin(t - 1)); SB(); }
#pragma unroll
        for (int e = 8; e < 12; ++e) pe[e] = __builtin_amdgcn_exp2f(sb[c][e]);
#pragma unroll
        for (int e = 0; e < 4; ++e) pf0[e] = pk2(pe[2 * e], pe[2 * e + 1]);
        SB();
        if constexpr (t > 0) { sb[n] = MFMA32(kq, q1f, sb[n]); SB(); }
#pragma unroll
        for (int e = 12; e < 16; ++e) pe[e] = __builtin_amdgcn_exp2f(sb[c][e]);
        SB();
        oacc = MFMA32(vf[c][0], pf0, oacc);
        SB();
#pragma unroll
        for (int e = 0; e < 4; ++e) pf1[e] = pk2(pe[8 + 2 * e], pe[8 + 2 * e + 1]);
        SB();
        if constexpr (t > 0) pre(IC<9 - t>{});
        oacc = MFMA32(vf[c][1], pf1, oacc);
        SB();
      });
    };
    auto exact = [&]() LAM {
      auto sc = [&](int t, float c0, f32x16& s) LAM {
        const u32x2 x2 = lds64(kb + t * 1536 + 32);
#pragma unroll
        for (int e = 0; e < 16; ++e) s[e] = c0;
        s = MFMA32(lds128(kb2 + t * 1536), q0f, s);
        s = MFMA32((u32x4{x2.x, x2.y, 0u, 0u}), q1f, s);
#pragma unroll
        for (int e = 0; e < 16; ++e) s[e] = (u32)(32 * t + (e & 3) + 8 * (e >> 2) - qlim - 1) < 256u ? s[e] : -1.0e30f;
      };
      float mx = -1.0e30f;
      f32x16 s;
#pragma nounroll
      for (int t = tmin; t < 9; ++t) {
        sc(t, 0.f, s);
#pragma unroll
        for (int e = 0; e < 16; e += 2) mx = max3(mx, s[e], s[e + 1]);
      }
      mx = -max32(mx);
#pragma unroll
      for (int e = 0; e < 16; ++e) oacc[e] = 0.f;
#pragma nounroll
      for (int t = tmin; t < 9; ++t) {
        sc(t, mx, s);
#pragma unroll
        for (int s2 = 0; s2 < 2; ++s2) {
          u32x4 pf;
#pragma unroll
          for (int e = 0; e < 4; ++e) pf[e] = pk2(__builtin_amdgcn_exp2f(s[8 * s2 + 2 * e]), __builtin_amdgcn_exp2f(s[8 * s2 + 2 * e + 1]));
          const u32 va = vb + (32 * t + 16 * s2) * 48;
          oacc = MFMA32(ldstr2(va, va + 384), pf, oacc);
        }
      }
    };
    if (mt >= 2) attn(BoolC<true>{}); else attn(BoolC<false>{});
    if (__builtin_amdgcn_ballot_w64(h != 0 && !(oacc[8] < ATT_LIMIT)) != 0ull) exact();
    const float lx = xor32f(oacc[8]);
    const float inv = __builtin_amdgcn_rcpf(64.f * (h ? oacc[8] : lx));
#pragma unroll
    for (int g = 0; g < 3; ++g) {
      const u32x2 o = {pk2(oacc[4 * g] * inv, oacc[4 * g + 1] * inv), pk2(oacc[4 * g + 2] * inv, oacc[4 * g + 3] * inv)};
      if (g < 2 || h == 0) __builtin_amdgcn_raw_buffer_store_b64(o, rs, ob + g * 512, 0, 16);
    }
    STAMP(5); CST(1, 19); FST(4, 11);
    WAITVM(0);
    BAR();
    if (tid == 0) signal(ws, C_O + mt * 256);
    STAMP(6); FST(4, 13);
  }
  u32 wbase[5], avoff;
  u32x2 x1k[3];
  f32x4 c5[5];
  {
    PHASE();
    const char* sg1 = ws + WS_WGF + (size_t)j * WGB + 81920 + (kh * 40 + tt * 8) * 1024;
    auto stage1 = [&]() LAM { sfor<0, 8>([&](auto m_) LAM { constexpr int m = decltype(m_)::v; gld_sc1<(m & 3) * 1024>(st[10 + m], sg1 + (m >> 2) * 4096, l16); }); };
    if (w >= 4) { __builtin_amdgcn_s_sleep(2); Run<30, 4, false>(ws + WS_WOF + (size_t)n8 * WOB + 71680, wo_slot(7), w - 4).all(l16); }
    stage1();
#pragma unroll
    for (int m = 0; m < 5; ++m) wbase[m] = (kh * 5 + (m + 3 * kh) % 5) * 1024 + l16;
    avoff = (g4 >> 1) * 1024 + (g4 & 1) * 512 + (16 * (tt & 1) + c16) * 16;
    const int tt64 = mt * 4 + 2 * th + (tt >> 1);
    const char* ab = ws + WS_OAF + (size_t)(tt64 * 40 + 20 * kh) * 1024;
    const u32 ew = 73728 + (tt * 5) * 1024 + l16;
    u32 xb = WS_X1 + (u32)(tt64 * 40 + 5 * n8) * 1024 + (g4 >> 1) * 512 + (16 * (tt & 1) + c16) * 16 + (g4 & 1) * 8 + kh * 3072;
    u32 sta = WS_STAT + (u32)((tt64 * 8 + n8) * 32 + 16 * (tt & 1) + c16) * 16 + kh * 8;
    u32 mo = L_MODS + kh * 192 + g4 * 16;
    PIN(xb); PIN(sta); PIN(mo); PIN(avoff);
    auto iss = [&](auto s_) LAM { constexpr int s = decltype(s_)::v; gld_sc1<(s & 1) * 2048>(a[s], ab + (s >> 1) * 4096, avoff); };
    u32x4 wf[3][5];
#pragma unroll
    for (int s = 0; s < 2; ++s)
#pragma unroll
      for (int m = 0; m < 5; ++m) wf[s][m] = lds128(wbase[m] + wo_slot(s));
#pragma unroll
    for (int m = 0; m < 5; ++m) c5[m] = f32x4{0.f, 0.f, 0.f, 0.f};
    if (w == 0) poll_ge(ws, C_O + mt * 256, 16u);
    BAR();
    STAMP(7); FST(5, 1);
    sfor<0, 5>(iss);
    sfor<0, 10>([&](auto s_) LAM {
      constexpr int s = decltype(s_)::v;
      WAITV(s == 0 ? 4 : s == 1 ? 6 : 9 - s);
      FST(5, 2 + s);
      if (s == 4) BAR();
      PIN(a[s]);
      if constexpr (s + 2 < 10) {
#pragma unroll
        for (int m = 0; m < 5; ++m) wf[(s + 2) % 3][m] = lds128(wbase[m] + wo_slot(s + 2));
      }
#pragma unroll
      for (int m = 0; m < 5; ++m) c5[m] = MFMA16(wf[s % 3][m], a[s], c5[m]);
      SB();
      if constexpr (s == 0) sfor<5, 8>(iss);
      if constexpr (s == 1) sfor<8, 10>(iss);
      SB();
    });
    STAMP(8); FST(5, 12);
    stf4(ew + ((3 + 3 * kh) % 5) * 1024, c5[3]);
    stf4(ew + ((4 + 3 * kh) % 5) * 1024, c5[4]);
    if (kh) stf4(ew, c5[2]);
    BAR();
    FST(5, 13);
    float xs[12];
#pragma unroll
    for (int m = 0; m < 3; ++m) PIN(xr[m]);
#pragma unroll
    for (int m = 0; m < 3; ++m) {
      if (m < 2 || kh == 0) {
        const f32x4 o = ldsf4(ew + kh * 3072 + m * 1024), ga = ldsf4(mo + m * 64), g2 = ldsf4(mo + 320 + m * 64);
        const u32 y01 = pk2(c5[m][0] + o[0], c5[m][1] + o[1]), y23 = pk2(c5[m][2] + o[2], c5[m][3] + o[3]);
        const u32 t01 = pk2(ga[0] * lo16(y01), ga[1] * hi16(y01)), t23 = pk2(ga[2] * lo16(y23), ga[3] * hi16(y23));
        x1k[m] = u32x2{pk2(lo16(xr[m].x) + lo16(t01), hi16(xr[m].x) + hi16(t01)), pk2(lo16(xr[m].y) + lo16(t23), hi16(xr[m].y) + hi16(t23))};
        xs[4 * m] = lo16(x1k[m].x); xs[4 * m + 1] = hi16(x1k[m].x); xs[4 * m + 2] = lo16(x1k[m].y); xs[4 * m + 3] = hi16(x1k[m].y);
        __builtin_amdgcn_raw_buffer_store_b64(u32x2{pk2(xs[4 * m] * g2[0], xs[4 * m + 1] * g2[1]), pk2(xs[4 * m + 2] * g2[2], xs[4 * m + 3] * g2[3])}, rs, xb + m * 1024, 0, 16);
      } else {
        x1k[m] = u32x2{0u, 0u};
        xs[8] = xs[9] = xs[10] = xs[11] = 0.f;
      }
    }
    float s = ((xs[0] + xs[1]) + (xs[2] + xs[3])) + ((xs[4] + xs[5]) + (xs[6] + xs[7])) + ((xs[8] + xs[9]) + (xs[10] + xs[11]));
    s = sum32(sum16(s));
    const float mean = s * (kh ? 1.f / 32.f : 1.f / 48.f);
    float q0 = 0.f, q1 = 0.f;
#pragma unroll
    for (int e = 0; e < 4; ++e) { const float d0 = xs[e] - mean, d1 = xs[4 + e] - mean; q0 = fma1(d0, d0, q0); q1 = fma1(d1, d1, q1); }
    if (kh == 0) {
#pragma unroll
      for (int e = 8; e < 12; ++e) { const float d = xs[e] - mean; q0 = fma1(d, d, q0); }
    }
    const float q = sum32(sum16(q0 + q1));
    if (g4 == 0) __builtin_amdgcn_raw_buffer_store_b64(u32x2{f2u(mean), f2u(q)}, rs, sta, 0, 16);
    FST(5, 14);
    BAR();
    STAMP(9); FST(5, 16);
  }
  {
    PHASE();
    const bool old4 = w < 4;
    const char* wgb = ws + WS_WGF + (size_t)j * WGB;
    const char* wdb = ws + WS_WDF + (size_t)n8 * WDB;
    u32x4 stv[4], csv[2];
    const char* ab = ws + WS_X1 + (size_t)((mt * 4 + tt) * 40 + 20 * kh) * 1024;
    const char* sb = ws + WS_STAT + (size_t)(mt * 4 + tt) * 4096;
    auto iss = [&](auto s_) LAM { constexpr int s = decltype(s_)::v; gld_sc1<(s & 3) * 1024>(a[s], ab + (s >> 2) * 4096, l16); };
    {
      const char* cb = ws + WS_CSP + (size_t)(j * 16 + 4 * (w >> 1) + (w & 1)) * 768;
      const u32 co = lane < 48 ? l16 : 0u;
      gld_sc1<0>(csv[0], cb, co); gld_sc1<1536>(csv[1], cb, co);
    }
#pragma unroll
    for (int m = 0; m < 18; ++m) { PIN(st[m]); st128((m < 10 ? (kh * 40 + tt * 10 + m) : (80 + kh * 32 + tt * 8 + m - 10)) * 1024 + l16, st[m]); }
    float c1 = 0.f, d2 = 0.f;
    u32x4 bcor = {0u, 0u, 0u, 0u};
    u32 sw = kh * 40960 + tt * 4096 + l16, sr = (kh ^ 1) * 40960 + tt * 4096 + l16;
    u32 actb = WS_ACT + (u32)((mt * 4 + tt) * 96 + 6 * j + kh) * 1024 + l16;
    const u32 so = h * 2048 + c32 * 16;
    PIN(actb);
    const auto rC = gu_run<1, 16, 20, 4>(wgb, w & 3);
    const auto rA = gu_run<2, 0, 8, 4>(wgb, w & 3);
    const auto rB = gu_run<2, 8, 20, 4>(wgb, w & 3);
    const Run<60, 8, true> rD(wdb, 0, w);
    const Run<90, 8, true> rS(wdb + 61440, 61440, w);
    gate(sb, (c32 >> 2) * 512 + ((c32 >> 1) & 1) * 256 + (c32 & 1) * 8);
    STAMP(10); FST(2, 1); FST(5, 17);
    sfor<0, 2>(iss);
    gld_sc1<0>(stv[0], sb, so); gld_sc1<512>(stv[1], sb, so); gld_sc1<1024>(stv[2], sb, so); gld_sc1<1536>(stv[3], sb, so);
    sfor<2, 6>(iss);
    f32x16 ag[2], au[2];
    u32x4 fg[3], fu[3], outv;
    u32 ub = 0;
    auto ldw = [&](auto g_) LAM {
      constexpr int g = decltype(g_)::v, pp = g / 20, i = g % 20;
      constexpr bool first = i == 0 || gu_unit(pp, 0, i) != gu_unit(pp, 0, i - 1) + 1;
      constexpr int i0 = [] { int x = i; while (x > 0 && gu_unit(pp, 0, x) == gu_unit(pp, 0, x - 1) + 1) --x; return x; }();
      if constexpr (first) ub = frow + (kh ? gu_unit(pp, 1, i) : gu_unit(pp, 0, i)) * 2048;
      fg[g % 3] = lds128(ub + (i - i0) * 2048); fu[g % 3] = lds128(ub + (i - i0) * 2048 + 1024);
    };
    auto xwrite = [&](int pp) LAM {
      const f32x16 &G = ag[pp & 1], &U = au[pp & 1];
      stf4(sw, f32x4{G[8], G[9], G[10], G[11]}); stf4(sw + 1024, f32x4{G[12], G[13], G[14], G[15]});
      stf4(sw + 2048, f32x4{U[8], U[9], U[10], U[11]}); stf4(sw + 3072, f32x4{U[12], U[13], U[14], U[15]});
    };
    f32x4 xo[4];
    auto xread = [&]() LAM {
#pragma unroll
      for (int u = 0; u < 4; ++u) xo[u] = ldsf4(sr + u * 1024);
    };
    auto xadd = [&](int pp, int q) LAM {
#pragma unroll
      for (int i = 0; i < 4; ++i) { ag[pp & 1][4 * q + i] += xo[q][i]; au[pp & 1][4 * q + i] += xo[2 + q][i]; }
    };
    auto corr = [&](int pp) LAM {
      ag[pp & 1] = MFMA32(lds128(L_T2 + pp * 1024 + rsw * 16), bcor, ag[pp & 1]);
      au[pp & 1] = MFMA32(lds128(L_T2 + pp * 1024 + 512 + rsw * 16), bcor, au[pp & 1]);
    };
    float eu[2], ee[2];
    auto epi = [&](int pp, int o, int ph) LAM {
#pragma unroll
      for (int i = 0; i < 2; ++i) {
        if (ph == 0) { ee[i] = __builtin_amdgcn_exp2f(ag[pp & 1][2 * o + i] * c1); eu[i] = ag[pp & 1][2 * o + i] * au[pp & 1][2 * o + i]; }
        if (ph == 1) ee[i] = __builtin_amdgcn_rcpf(fma1(ee[i], d2, d2));
      }
      if (ph == 2) outv[o] = pk2(eu[0] * ee[0], eu[1] * ee[1]);
    };
    auto ostore = [&](int pp) LAM { __builtin_amdgcn_raw_buffer_store_b128(outv, rs, actb + pp * 2048, 0, 16); asm volatile("" ::: "memory"); };
    BAR();
    ldw(IC<0>{}); ldw(IC<1>{});
    auto nextA = [&](auto g_) LAM {
      constexpr int g = decltype(g_)::v;
      WAITV(g == 0 ? 9 : g == 1 ? 12 : g == 2 ? 11 : g == 3 ? 14 : 19 - g);
      PIN(a[g]);
      ensure<(g & 3) * 1024>(a[g], ab + (g >> 2) * 4096, l16);
    };
    nextA(IC<0>{});
    sfor<0, 60>([&](auto g_) LAM {
      constexpr int g = decltype(g_)::v, pp = g / 20, i = g % 20;
      if (g == 32) WAITV(0);
      if (g == 43) WAITV(NRS >= 3 ? 4 : 1 + NRS);
      if ((g & 3) == 0) FST(2, 2 + g / 4);
      if (g == 12 || g == 22 || g == 32 || g == 43 || g == 50) BAR();
      if (g == 22 && CSEL != 4) { STAMP(16); CST(0, 18); }
      if (g == 43 && CSEL != 4) { STAMP(17); CST(0, 19); }
      if constexpr (g + 2 < 60) ldw(IC<g + 2>{});
      if (g == 22 || g == 43) xread();
      if (i == 0) {
        f32x16 z;
#pragma unroll
        for (int e = 0; e < 16; ++e) z[e] = 0.f;
        ag[pp & 1] = MFMA32(fg[g % 3], a[i], z); au[pp & 1] = MFMA32(fu[g % 3], a[i], z);
      } else {
        ag[pp & 1] = MFMA32(fg[g % 3], a[i], ag[pp & 1]); au[pp & 1] = MFMA32(fu[g % 3], a[i], au[pp & 1]);
      }
      if (g == 4) {
#pragma unroll
        for (int u = 0; u < 4; ++u) PIN(stv[u]);
        ensure<0>(stv[0], sb, so); ensure<512>(stv[1], sb, so); ensure<1024>(stv[2], sb, so); ensure<1536>(stv[3], sb, so);
        PIN(csv[0]); PIN(csv[1]);
        f32x4 sm;
#pragma unroll
        for (int e = 0; e < 4; ++e) sm[e] = u2f(csv[0][e]) + u2f(csv[1][e]);
        if (lane < 48) stf4(L_GS + (w >> 1) * 1536 + (w & 1) * 768 + l16, sm);
        float s = 0.f;
#pragma unroll
        for (int u = 0; u < 4; ++u) s += 48.f * u2f(stv[u][0]) + 32.f * u2f(stv[u][2]);
        const float mean = sum32(s) * (1.f / 640.f);
        float q = 0.f;
#pragma unroll
        for (int u = 0; u < 4; ++u) {
          const float d0 = u2f(stv[u][0]) - mean, d1 = u2f(stv[u][2]) - mean;
          q += u2f(stv[u][1]) + u2f(stv[u][3]) + 48.f * d0 * d0 + 32.f * d1 * d1;
        }
        q = sum32(q);
        const float den = rbf(__builtin_amdgcn_sqrtf(rbf(rbf(q * (1.f / 640.f)) + 1e-5f)));
        c1 = -1.4426950408889634f * __builtin_amdgcn_rcpf(den); d2 = den * den;
        const u32 nm = pk2(-mean, -mean), dd = pk2(den, den);
        bcor = u32x4{h ? 0u : nm, h ? 0u : ((nm & 0xffffu) | (dd & 0xffff0000u)), h ? 0u : dd, 0u};
      }
      if (g == 12 && tid < 192) {
        const u32 t4 = L_GS + (tid >> 3) * 64 + (tid & 7) * 4;
        const float cs = (ldsf(t4) + ldsf(t4 + 1536)) + (ldsf(t4 + 3072) + ldsf(t4 + 4608));
        const float bi = (ldsf(t4 + 32) + ldsf(t4 + 1568)) + (ldsf(t4 + 3104) + ldsf(t4 + 4640));
        const u32 h0 = pk2(cs, bi);
        const float cr = cs - lo16(h0), br = bi - hi16(h0);
        const u32 h1 = pk2(cr, br), h2 = pk2(cr - lo16(h1), br - hi16(h1));
        const int gu = tid >= 96, nn = tid - 96 * gu;
        st128(L_T2 + (nn >> 5) * 1024 + gu * 512 + (nn & 31) * 16,
              u32x4{__builtin_amdgcn_perm(h1, h0, 0x05040100u), __builtin_amdgcn_perm(h0, h2, 0x07060100u), __builtin_amdgcn_perm(h2, h1, 0x07060302u), 0u});
      }
      if (g == 21 || g == 41) xwrite(pp - 1);
      if (g == 22 || g == 42) corr(pp - 1);
      if (g == 24 || g == 25) xadd(0, g - 24);
      if (g >= 28 && g <= 37 && (g - 28) % 3 == 0) epi(0, (g - 28) / 3, 2);
      if (g >= 26 && g < 37 && (g - 26) % 3 < 2) epi(0, (g - 26) / 3, (g - 26) % 3);
      if (g == 44 || g == 45) xadd(1, g - 44);
      if (g >= 48 && g <= 57 && (g - 48) % 3 == 0) epi(1, (g - 48) / 3, 2);
      if (g >= 46 && g < 57 && (g - 46) % 3 < 2) epi(1, (g - 46) / 3, (g - 46) % 3);
      SGB(0x100, 6, 0); SGB(0x008, 1, 0); SGB(0x002, 4, 0); SGB(0x008, 1, 0);
      SB();
      if constexpr (g < 3) sfor<6 + 4 * g, 10 + 4 * g>(iss);
      if constexpr (g == 3) sfor<18, 20>(iss);
      if constexpr (g + 1 < 20) nextA(IC<g + 1>{});
      if (g == 40) ostore(0);
      if constexpr (g >= 40 && g < 40 + NRS) gld_sc1<((g - 40) & 3) * 1024>(a[g - 40], rS.src + ((g - 40) >> 2) * 4096, l16);
      if constexpr (g >= 50 && g < 58) rD.template go<g - 50>(l16);
      if (g == 57) ostore(1);
      if constexpr ((g >= 16 && g < 18) || (g >= 22 && g < 26) || (g >= 32 && g < 38)) {
        if (old4) {
          if constexpr (g < 18) { rC.template go<2 * (g - 16)>(l16); rC.template go<2 * (g - 16) + 1>(l16); }
          else if constexpr (g < 26) { rA.template go<2 * (g - 22)>(l16); rA.template go<2 * (g - 22) + 1>(l16); }
          else { rB.template go<2 * (g - 32)>(l16); rB.template go<2 * (g - 32) + 1>(l16); }
        }
      }
      SB();
    });
    { const u32 lo_ = 65536 + tt * 4096, hi_ = (tt < 2 ? 106496 : 131072) + tt * 4096; sw = (kh ? hi_ : lo_) + l16; sr = (kh ? lo_ : hi_) + l16; }
    xwrite(2);
    corr(2);
    BAR();
    STAMP(11); FST(2, 17);
    xread(); xadd(2, 0); xadd(2, 1);
#pragma unroll
    for (int o = 0; o < 4; ++o)
#pragma unroll
      for (int ph = 0; ph < 3; ++ph) epi(2, o, ph);
    ostore(2);
    WAITV(8);
    BAR();
#pragma unroll
    for (int m = 0; m < NRS; ++m) { PIN(a[m]); st128(rS.lds + m * 1024 + l16, a[m]); }
    sfor<NRS, 12>([&](auto m_) LAM { rS.template go<decltype(m_)::v>(l16); });
    STAMP(12);
  }
  {
    PHASE();
    const bool old4 = w < 4;
    const char* wdb = ws + WS_WDF + (size_t)n8 * WDB;
    const int tt64 = mt * 4 + 2 * th + (tt >> 1);
    const char* ab = ws + WS_ACT + (size_t)(tt64 * 96 + 48 * kh) * 1024;
    auto iss = [&](auto s_) LAM { constexpr int s = decltype(s_)::v; gld_sc1<(s & 1) * 2048>(a[s], ab + (s >> 1) * 4096, avoff); };
#pragma unroll
    for (int m = 0; m < 5; ++m) c5[m] = f32x4{0.f, 0.f, 0.f, 0.f};
    const Run<50, 4, true> rE(wdb + 153600, 0, w & 3);
    const Run<40, 4, true> rF(wdb + 204800, 51200, w & 3);
    const u32 ew = 92160 + (tt * 5) * 1024 + l16;
    u32 oo = (u32)(128 * mt + 64 * th + 16 * tt + c16) * 1280 + (80 * n8 + 48 * kh + 4 * g4) * 2;
    u32 mo = L_MODS + 640 + kh * 192 + g4 * 16;
    PIN(oo); PIN(mo);
    gate(ab, ((c16 >> 1) * 6 + 4 + (c16 & 1)) * 1024 + (tt & 1) * 256);
    STAMP(13); CST(2, 18); FST(1, 1);
    sfor<0, 6>(iss);
    u32 wb2[5], wb3[5];
#pragma unroll
    for (int m = 0; m < 5; ++m) { wb2[m] = wbase[m] + 61440; wb3[m] = wbase[m] + 122880; PIN(wb2[m]); PIN(wb3[m]); }
    auto nextA = [&](auto s_) LAM {
      constexpr int s = decltype(s_)::v;
      if constexpr (dn_wait(s, true) == dn_wait(s, false)) WAITV(dn_wait(s, true)); else { if (old4) WAITV(dn_wait(s, true)); else WAITV(dn_wait(s, false)); }
      PIN(a[s]);
      ensure<(s & 1) * 2048>(a[s], ab + (s >> 1) * 4096, avoff);
    };
    BAR();
    u32x4 wf[3][5];
#pragma unroll
    for (int u = 0; u < 2; ++u)
#pragma unroll
      for (int m = 0; m < 5; ++m) wf[u][m] = lds128(wbase[m] + wd_slot(u));
    nextA(IC<0>{});
    sfor<0, 24>([&](auto s_) LAM {
      constexpr int s = decltype(s_)::v;
      if ((s & 1) == 0) FST(1, 2 + s / 2);
      if (s == 5 || s == 9 || s == 13 || s == 18) BAR();
      if constexpr (s + 2 < 24) {
        constexpr u32 so = wd_slot(s + 2);
#pragma unroll
        for (int m = 0; m < 5; ++m) wf[(s + 2) % 3][m] = lds128((so < 61440 ? wbase[m] : so < 122880 ? wb2[m] : wb3[m]) + so % 61440);
      }
#pragma unroll
      for (int m = 0; m < 5; ++m) c5[m] = MFMA16(wf[s % 3][m], a[s], c5[m]);
      SB();
      if constexpr (s >= 5 && s < 14) {
        if (old4) {
          if constexpr (s < 9) { sfor<0, 4>([&](auto u_) LAM { rE.template go<4 * (s - 5) + decltype(u_)::v>(l16); }); }
          else { rF.template go<2 * (s - 9)>(l16); rF.template go<2 * (s - 9) + 1>(l16); }
        }
      }
      sfor<dn_cumA(s), dn_cumA(s + 1)>(iss);
      if constexpr (s + 1 < 24) nextA(IC<s + 1>{});
      SB();
    });
    STAMP(14); CST(2, 19); FST(1, 14);
    stf4(ew + ((3 + 3 * kh) % 5) * 1024, c5[3]);
    stf4(ew + ((4 + 3 * kh) % 5) * 1024, c5[4]);
    if (kh) stf4(ew, c5[2]);
    BAR();
#pragma unroll
    for (int m = 0; m < 3; ++m)
      if (m < 2 || kh == 0) {
        const f32x4 o = ldsf4(ew + kh * 3072 + m * 1024), gb = ldsf4(mo + m * 64);
        const u32 y01 = pk2(c5[m][0] + o[0], c5[m][1] + o[1]), y23 = pk2(c5[m][2] + o[2], c5[m][3] + o[3]);
        const u32 t01 = pk2(gb[0] * lo16(y01), gb[1] * hi16(y01)), t23 = pk2(gb[2] * lo16(y23), gb[3] * hi16(y23));
        const u32x2 r = {pk2(lo16(x1k[m].x) + lo16(t01), hi16(x1k[m].x) + hi16(t01)), pk2(lo16(x1k[m].y) + lo16(t23), hi16(x1k[m].y) + hi16(t23))};
        *(GAS u32x2*)((GAS char*)p.out + oo + m * 32) = r;
      }
  }
}
}

extern "C" void kernel_launch(void* const* d_in, const int* in_sizes, int n_in, void* d_out, int out_size, void* d_ws, size_t ws_size,
                              hipStream_t stream) {
  (void)in_sizes; (void)n_in; (void)out_size; (void)ws_size;
  (void)hipFuncSetAttribute((const void*)k2, hipFuncAttributeMaxDynamicSharedMemorySize, LDS_BYTES);
  P1 q;
  q.x = (const u16*)d_in[0]; q.wqkv = (const u16*)d_in[1]; q.wo = (const u16*)d_in[2];
  q.cvec = (const u16*)d_in[7]; q.wmod = (const u16*)d_in[8]; q.ws = (char*)d_ws;
  k1<<<K1_BLOCKS, 256, 0, stream>>>(q);
  P2 p;
  p.x = (const u16*)d_in[0]; p.wg = (const u16*)d_in[4]; p.wu = (const u16*)d_in[5]; p.wd = (const u16*)d_in[6];
  p.out = (u16*)d_out; p.ws = (char*)d_ws;
  k2<<<256, 512, LDS_BYTES, stream>>>(p);
}
```

```cpp
#include <hip/hip_runtime.h>
#include <hip/hip_bf16.h>
#include <stdint.h>

#define GAS __attribute__((address_space(1)))
#define LAS __attribute__((address_space(3)))
#if defined(__HIP_DEVICE_COMPILE__)
#define NOPK __attribute__((target("no-packed-fp32-ops")))
#else
#define NOPK
#endif
#define DI __device__ __forceinline__ NOPK
#define LAM __attribute__((always_inline)) NOPK
typedef unsigned int u32;
typedef unsigned short u16;
typedef u32 u32x4 __attribute__((ext_vector_type(4)));
typedef u32 u32x2 __attribute__((ext_vector_type(2)));
typedef float f32x2 __attribute__((ext_vector_type(2)));
typedef float f32x4 __attribute__((ext_vector_type(4)));
typedef float f32x16 __attribute__((ext_vector_type(16)));
typedef __bf16 bf16x2 __attribute__((ext_vector_type(2)));
typedef __bf16 bf16x4 __attribute__((ext_vector_type(4)));
typedef __bf16 bf16x8 __attribute__((ext_vector_type(8)));
typedef GAS u32 gu32;
typedef _Float16 f16x8 __attribute__((ext_vector_type(8)));
typedef _Float16 f16x2 __attribute__((ext_vector_type(2)));

namespace {
constexpr int D = 640;
constexpr u32 WS_KVF = 0;
constexpr u32 WS_CNT = 32768;
constexpr u32 WS_MODP = 65536;
constexpr u32 WS_CSP = WS_MODP + 61440;
constexpr u32 WS_STAT = WS_CSP + 16 * 16 * 1536;
constexpr u32 WS_N1 = WS_STAT + 64 * 8 * 32 * 16;
constexpr u32 WS_OAF = WS_N1 + 64 * 40 * 1024;
constexpr u32 WS_X1 = WS_OAF + 64 * 40 * 1024;
constexpr u32 WS_ACT = WS_X1 + 64 * 40 * 1024;
constexpr u32 KVB = 24576;
constexpr u32 WS_KVX = WS_ACT + 64 * 96 * 1024;
constexpr u32 WQB = 153600;
constexpr u32 WS_WQF = WS_KVX + 256 * KVB;
constexpr u32 WOB = 102400, WGB = 245760, WDB = 245760;
constexpr u32 WS_WOF = WS_WQF + 16 * WQB;
constexpr u32 WS_WGF = WS_WOF + 8 * WOB;
constexpr u32 WS_WDF = WS_WGF + 16 * WGB;
constexpr u32 WS_END = WS_WDF + 8 * WDB;
constexpr u32 L_MISC = 153600;
constexpr u32 L_GS = L_MISC;
constexpr u32 L_MODS = L_MISC + 6144;
constexpr u32 L_T2 = L_MISC + 7168;
constexpr u32 L_FLAG = L_MISC + 9728;
constexpr u32 LDS_BYTES = 163840;
constexpr float QSCALE = 0.22360679774997896f * 1.4426950408889634f;
constexpr int K1_BLOCKS = 488;
#define ATT_LIMIT 1.0e18f
constexpr int K2_LINES = 364;
constexpr u32 SPIN_CAP = 1u << 20;

DI float u2f(u32 v) { return __builtin_bit_cast(float, v); }
DI u32 f2u(float v) { return __builtin_bit_cast(u32, v); }
DI int tidx() { return (int)__builtin_amdgcn_workitem_id_x(); }
DI int bidx() { return (int)__builtin_amdgcn_workgroup_id_x(); }
DI float lo16(u32 v) { return u2f(v << 16); }
DI float hi16(u32 v) { return u2f(v & 0xffff0000u); }
DI u32 pk2(float a, float b) { f32x2 v = {a, b}; return __builtin_bit_cast(u32, __builtin_convertvector(v, bf16x2)); }
DI u32 pkh2(float a, float b) { f32x2 v = {a, b}; return __builtin_bit_cast(u32, __builtin_convertvector(v, f16x2)); }
DI float rbf(float a) { return lo16(pk2(a, a)); }
DI float fma1(float a, float b, float c) { return __builtin_fmaf(a, b, c); }
DI float max3(float a, float b, float c) { return __builtin_fmaxf(__builtin_fmaxf(a, b), c); }
DI float xor32f(float v) {
  auto r = __builtin_amdgcn_permlane32_swap(f2u(v), f2u(v), false, false);
  return u2f((tidx() & 32) ? r[0] : r[1]);
}
DI float sum32(float v) { auto r = __builtin_amdgcn_permlane32_swap(f2u(v), f2u(v), false, false); return u2f(r[0]) + u2f(r[1]); }
DI float sum16(float v) { auto r = __builtin_amdgcn_permlane16_swap(f2u(v), f2u(v), false, false); return u2f(r[0]) + u2f(r[1]); }
DI float max32(float v) { auto r = __builtin_amdgcn_permlane32_swap(f2u(v), f2u(v), false, false); return __builtin_fmaxf(u2f(r[0]), u2f(r[1])); }
template <int CTRL> DI float dppf(float v) { return u2f(__builtin_amdgcn_update_dpp(0u, f2u(v), CTRL, 0xf, 0xf, true)); }

#define MFMA32(a, b, c) __builtin_amdgcn_mfma_f32_32x32x16_bf16(__builtin_bit_cast(bf16x8, a), __builtin_bit_cast(bf16x8, b), c, 0, 0, 0)
#define MFMA32H(a, b, c) __builtin_amdgcn_mfma_f32_32x32x16_f16(__builtin_bit_cast(f16x8, a), __builtin_bit_cast(f16x8, b), c, 0, 0, 0)
#define MFMA16(a, b, c) __builtin_amdgcn_mfma_f32_16x16x32_bf16(__builtin_bit_cast(bf16x8, a), __builtin_bit_cast(bf16x8, b), c, 0, 0, 0)
#define SGB(m, n, id) __builtin_amdgcn_sched_group_barrier(m, n, id)
#define SB() __builtin_amdgcn_sched_barrier(0)
template <bool B> struct BoolC { static constexpr bool v = B; };
template <int I> struct IC { static constexpr int v = I; };
template <int I, int N, class Fn> DI void sfor(Fn&& f) { if constexpr (I < N) { f(IC<I>{}); sfor<I + 1, N>(f); } }
#define PIN(x) asm volatile("" : "+v"(x))
#define WAITV(n) asm volatile("s_waitcnt vmcnt(%0)" ::"i"(n))
#define WAITVM(n) asm volatile("s_waitcnt vmcnt(%0)" ::"i"(n) : "memory")
#define BAR() asm volatile("s_waitcnt lgkmcnt(0)\n\ts_barrier" ::: "memory")
#define RLX_AGENT __ATOMIC_RELAXED, __HIP_MEMORY_SCOPE_AGENT

DI u32x4 lds128(u32 a) { return *(const LAS u32x4*)(size_t)a; }
DI f32x4 ldsf4(u32 a) { return *(const LAS f32x4*)(size_t)a; }
DI u32x2 lds64(u32 a) { return *(const LAS u32x2*)(size_t)a; }
DI float ldsf(u32 a) { return *(const LAS float*)(size_t)a; }
DI void st128(u32 a, u32x4 v) { *(LAS u32x4*)(size_t)a = v; }
DI void stf4(u32 a, f32x4 v) { *(LAS f32x4*)(size_t)a = v; }
DI void st64(u32 a, u32x2 v) { *(LAS u32x2*)(size_t)a = v; }
DI u32x2 ldstr(u32 a) { return __builtin_bit_cast(u32x2, __builtin_amdgcn_ds_read_tr16_b64_v4bf16((LAS bf16x4*)(size_t)a)); }
DI u32x4 ldstr2(u32 a, u32 b) { u32x2 lo = ldstr(a), hi = ldstr(b); return u32x4{lo.x, lo.y, hi.x, hi.y}; }

template <int IMM, bool SC1> DI void dmai(const char* sbase, u32 voff, u32 lds) {
  if constexpr (SC1) asm volatile("s_mov_b32 m0, %2\n\ts_nop 0\n\tglobal_load_lds_dwordx4 %0, %1 offset:%3 sc1" ::"v"(voff), "s"(sbase), "s"(lds), "i"(IMM));
  else asm volatile("s_mov_b32 m0, %2\n\ts_nop 0\n\tglobal_load_lds_dwordx4 %0, %1 offset:%3" ::"v"(voff), "s"(sbase), "s"(lds), "i"(IMM));
}
DI void dma(const char* sbase, u32 voff, u32 lds) { dmai<0, false>(sbase, voff, lds); }
DI void dma_nt(const char* sbase, u32 voff, u32 lds) { asm volatile("s_mov_b32 m0, %2\n\ts_nop 0\n\tglobal_load_lds_dwordx4 %0, %1 nt" ::"v"(voff), "s"(sbase), "s"(lds)); }
DI void gld32_sc1(u32& r, const char* sbase, u32 voff) { asm volatile("global_load_dword %0, %1, %2 sc1" : "=v"(r) : "v"(voff), "s"(sbase)); }
template <int IMM> DI void gld_sc1(u32x4& r, const char* sbase, u32 voff) {
  asm volatile("global_load_dwordx4 %0, %1, %2 offset:%3 sc1" : "=v"(r) : "v"(voff), "s"(sbase), "i"(IMM));
}
template <int IMM> DI void gld(u32x4& r, const char* sbase, u32 voff) {
  asm volatile("global_load_dwordx4 %0, %1, %2 offset:%3" : "=v"(r) : "v"(voff), "s"(sbase), "i"(IMM));
}
DI void gld64(u32x2& r, const char* sbase, u32 voff) {
  asm volatile("global_load_dwordx2 %0, %1, %2" : "=v"(r) : "v"(voff), "s"(sbase));
}
DI void poll_ge(const char* ws, u32 off, u32 want) {
  gu32* p = (gu32*)(ws + off);
  for (u32 spins = 0; spins < SPIN_CAP; ++spins) {
    if ((u32)__builtin_amdgcn_readfirstlane(__hip_atomic_load(p, RLX_AGENT)) >= want) break;
    __builtin_amdgcn_s_sleep(4);
  }
  __builtin_amdgcn_fence(__ATOMIC_ACQUIRE, "wavefront");
}
DI void signal(const char* ws, u32 off) { __hip_atomic_fetch_add((gu32*)(ws + off), 1u, RLX_AGENT); }

struct P1 {
  const u16 *x, *wqkv, *wo, *cvec, *wmod;
  char* ws;
};
#define K1AUX 0
__global__ void __launch_bounds__(256) NOPK k1(P1 p) {
  __shared__ float red[32 * 64 + 4 * 64 + 320];
  const int b = bidx(), t = tidx();
  char* ws = p.ws;
  const __amdgpu_buffer_rsrc_t rs = __builtin_amdgcn_make_buffer_rsrc(p.ws, 0, WS_END, 0x00020000);
  if (b >= 408 && b < 424) {
    unsigned long long pc;
    asm volatile("s_getpc_b64 s[40:41]\n\ts_add_u32 s40, s40, _ZN12_GLOBAL__N_12k2ENS_2P2E@rel32@lo+4\n\ts_addc_u32 s41, s41, _ZN12_GLOBAL__N_12k2ENS_2P2E@rel32@hi+12\n\ts_mov_b64 %0, s[40:41]"
                 : "=s"(pc) : : "scc", "s40", "s41");
    const int o = ((b - 408) >> 3) * 256 + t;
    if (o < K2_LINES) { u32 v_ = *(const GAS u32*)(pc + (size_t)o * 128); asm volatile("" ::"v"(v_)); }
  }
  if (b < 120) {
    const int ch = t & 7, r = t >> 3, col0 = 64 * (b >> 1), row0 = 320 * (b & 1);
    const u32x4 cv = *(const u32x4*)(p.cvec + row0 + 8 * (t < 40 ? t : 39));
    u32x4 wv[10];
#pragma unroll
    for (int i = 0; i < 10; ++i) wv[i] = __builtin_nontemporal_load((const u32x4*)(p.wmod + (size_t)(row0 + r + 32 * i) * 3840 + col0 + 8 * ch));
    if (t < 40) {
#pragma unroll
      for (int e = 0; e < 8; ++e) {
        const float c = (e & 1) ? hi16(cv[e >> 1]) : lo16(cv[e >> 1]);
        red[2304 + 8 * t + e] = rbf(c * __builtin_amdgcn_rcpf(1.f + __builtin_amdgcn_exp2f(-1.4426950408889634f * c)));
      }
    }
    BAR();
    float acc[8] = {0, 0, 0, 0, 0, 0, 0, 0};
#pragma unroll
    for (int i = 0; i < 10; ++i) {
      const float s = red[2304 + r + 32 * i];
#pragma unroll
      for (int e = 0; e < 4; ++e) { acc[2 * e] = fma1(s, lo16(wv[i][e]), acc[2 * e]); acc[2 * e + 1] = fma1(s, hi16(wv[i][e]), acc[2 * e + 1]); }
    }
    *(f32x4*)&red[r * 64 + 8 * ch] = f32x4{acc[0], acc[1], acc[2], acc[3]};
    *(f32x4*)&red[r * 64 + 8 * ch + 4] = f32x4{acc[4], acc[5], acc[6], acc[7]};
    BAR();
    { const int c = t & 63, part = t >> 6; float s = 0;
#pragma unroll
      for (int i = 0; i < 8; ++i) s += red[(part * 8 + i) * 64 + c];
      red[2048 + part * 64 + c] = s; }
    BAR();
    if (t < 64) {
      const float s = (red[2048 + t] + red[2112 + t]) + (red[2176 + t] + red[2240 + t]);
      __builtin_amdgcn_raw_buffer_store_b32(f2u(s), rs, WS_MODP + ((b & 1) * 3840 + col0 + t) * 4, 0, K1AUX);
    }
    if (b == 0) {
      *(u32*)(ws + WS_KVF + t * 128) = 0u;
      if (t < 72) *(u32*)(ws + WS_CNT + t * 256) = 0u;
    }
  } else if (b < 248) {
    const int hb = b - 120, c = t & 15, tok = 16 * (hb & 1) + (t >> 4);
    const u16* row = p.x + (size_t)(32 * (hb >> 1) + tok) * D;
    u32x4 v[5];
#pragma unroll
    for (int i = 0; i < 5; ++i) v[i] = *(const u32x4*)(row + 8 * (c + 16 * i));
    float s = 0;
#pragma unroll
    for (int i = 0; i < 5; ++i)
#pragma unroll
      for (int e = 0; e < 4; ++e) s += lo16(v[i][e]) + hi16(v[i][e]);
    s += dppf<0xB1>(s); s += dppf<0x4E>(s); s += dppf<0x141>(s); s += dppf<0x140>(s);
    const float mean = s * (1.f / 640.f);
    float q = 0;
#pragma unroll
    for (int i = 0; i < 5; ++i)
#pragma unroll
      for (int e = 0; e < 4; ++e) { const float a = lo16(v[i][e]) - mean, bb = hi16(v[i][e]) - mean; q = fma1(a, a, q); q = fma1(bb, bb, q); }
    q += dppf<0xB1>(q); q += dppf<0x4E>(q); q += dppf<0x141>(q); q += dppf<0x140>(q);
    const float mb = rbf(mean), den = rbf(__builtin_amdgcn_sqrtf(rbf(rbf(q * (1.f / 640.f)) + 1e-5f))), rr = __builtin_amdgcn_rcpf(den);
    const u32 dst = WS_N1 + (u32)(hb >> 1) * 40960 + tok * 16;
#pragma unroll
    for (int i = 0; i < 5; ++i) {
      u32x4 o;
#pragma unroll
      for (int e = 0; e < 4; ++e) {
        const u32 d = pk2(lo16(v[i][e]) - mb, hi16(v[i][e]) - mb);
        const u32 n = pk2(lo16(d) * rr, hi16(d) * rr);
        o[e] = pkh2(lo16(n), hi16(n));
      }
      __builtin_amdgcn_raw_buffer_store_b128(o, rs, dst + (c + 16 * i) * 512, 0, K1AUX);
    }
  } else {
    const u16* src; int stride; u32 off0, off1; bool act = true;
    if (b < 408) {
      const int idx = b - 248, ks = idx >> 2, jq = idx & 3, tc = t < 240 ? t : 239;
      act = t < 240;
      const int seg = tc / 80, cp = tc - 80 * seg;
      src = p.wqkv + (size_t)(16 * ks) * 1920 + 640 * seg + 160 * jq + 2 * cp; stride = 1920;
      const int jj = (2 * cp) / 40, within = 2 * cp - 40 * jj, j = 4 * jq + jj;
      const int n = (seg == 1 ? 0 : seg == 2 ? 40 : 80) + within, nb = n >> 5, r = n & 31;
      off0 = WS_WQF + j * WQB + (nb < 3 ? ((nb * 40 + ks) * 64 + r) * 16 : 122880 + ks * 768 + r * 16);
      off1 = off0 + (nb < 3 ? 512 : 384);
    } else {
      const int idx = b - 408, k16 = idx >> 1, tc = t < 160 ? t : 159, n = (idx & 1) * 320 + 2 * tc;
      act = t < 160;
      src = p.wo + (size_t)(16 * k16) * 640 + n; stride = 640;
      const int kh = k16 >= 20, kk = k16 - 20 * kh, st = kk >> 1, H = kk & 1;
      const int ntg = n >> 4, n8 = ntg / 5, nt = ntg - 5 * n8;
      off0 = WS_WOF + n8 * WOB + (st * 10 + kh * 5 + nt) * 1024 + (2 * H * 16 + (n & 15)) * 16;
      off1 = off0 + 256;
    }
    u32 v[16];
#pragma unroll
    for (int i = 0; i < 16; ++i) v[i] = __builtin_nontemporal_load((const u32*)(src + (size_t)i * stride));
    if (b < 408) {
#pragma unroll
      for (int i = 0; i < 16; ++i) v[i] = pkh2(lo16(v[i]) * 64.f, hi16(v[i]) * 64.f);
    }
    if (act) {
#pragma unroll
      for (int h = 0; h < 2; ++h) {
        u32x4 a, bq;
#pragma unroll
        for (int e = 0; e < 4; ++e) {
          a[e] = __builtin_amdgcn_perm(v[8 * h + 2 * e + 1], v[8 * h + 2 * e], 0x05040100u);
          bq[e] = __builtin_amdgcn_perm(v[8 * h + 2 * e + 1], v[8 * h + 2 * e], 0x07060302u);
        }
        __builtin_amdgcn_raw_buffer_store_b128(a, rs, h ? off1 : off0, 0, K1AUX);
        __builtin_amdgcn_raw_buffer_store_b128(bq, rs, (h ? off1 : off0) + 16, 0, K1AUX);
      }
    }
  }
}

struct P2 {
  const u16 *x, *wg, *wu, *wd;
  u16* out;
  char* ws;
};
#define MAP 0
constexpr u32 C_O = WS_CNT, C_RG = WS_CNT + 48 * 256, C_RD = WS_CNT + 64 * 256;
#define OPQ(x) asm volatile("" : "+s"(x))

DI constexpr int gu_unit(int p, int kh, int i) {
  return p == 0 ? 20 * kh + i
       : p == 1 ? (i < 16 ? 40 + 16 * kh + i : 8 + 20 * kh + (i - 16))
                : (i < 8 ? 12 + 20 * kh + i : 40 + 16 * kh + (i - 8));
}
DI constexpr u32 wo_slot(int s) { return s < 7 ? (72 + 10 * s) * 1024 : (s - 7) * 10240; }
DI constexpr u32 wd_slot(int s) { return (s < 15 ? s : s - 15) * 10240; }
template <int N, int NW, bool SC1> struct Run {
  static constexpr int C = (N + NW - 1) / NW;
  const char* src; u32 lds;
  DI Run(const char* s, u32 l, int x) { int o = x * C; o = (o < N - C ? o : N - C) * 1024; src = s + o; lds = l + o; }
  template <int M> DI void go(u32 l16) const { if constexpr (M < C) dmai<(M & 3) * 1024, SC1>(src + (M >> 2) * 4096, l16, lds + (M >> 2) * 4096); }
  DI void all(u32 l16) const { sfor<0, C>([&](auto m_) LAM { go<decltype(m_)::v>(l16); }); }
};
template <int P, int I0, int I1, int NW> DI auto gu_run(const char* blob, int x) {
  const int k = x >= NW / 2;
  return Run<2 * (I1 - I0), NW / 2, true>(blob + ((P * 2 + k) * 20 + I0) * 2048, (k ? gu_unit(P, 1, I0) : gu_unit(P, 0, I0)) * 2048, x - k * (NW / 2));
}
DI u32 pkfma16(u32 x, u32 y, u32 z) { return __builtin_bit_cast(u32, __builtin_elementwise_fma(__builtin_bit_cast(f16x2, x), __builtin_bit_cast(f16x2, y), __builtin_bit_cast(f16x2, z))); }
DI void gld4_if(int cond, u32x4& r0, u32x4& r1, u32x4& r2, u32x4& r3, const char* sbase, u32 voff) {
  asm volatile("s_cmp_eq_u32 %6, 0\n\ts_cbranch_scc1 .Lskip%=\n\tglobal_load_dwordx4 %0, %4, %5\n\tglobal_load_dwordx4 %1, %4, %5 offset:1024\n\t"
               "global_load_dwordx4 %2, %4, %5 offset:2048\n\tglobal_load_dwordx4 %3, %4, %5 offset:3072\n.Lskip%=:"
               : "+v"(r0), "+v"(r1), "+v"(r2), "+v"(r3) : "v"(voff), "s"(sbase), "s"(cond) : "scc");
}
#define NRS 12
DI constexpr int dn_nA(int s) { return s < 3 ? 3 : s == 8 || s == 10 || s == 13 ? 3 : 0; }
DI constexpr int dn_nD(int s) { return s >= 5 && s < 8 ? 4 : s == 8 ? 1 : s >= 9 && s < 14 ? 2 : 0; }
DI constexpr int dn_cumA(int s) { int c = 6; for (int u = 0; u < s; ++u) c += dn_nA(u); return c; }
DI constexpr int dn_young(int s, bool old, int a_idx, int d_step) {
  int pos = 0, tgt = -1, na = 6;
  if (a_idx >= 0 && a_idx < 6) tgt = a_idx;
  pos = 6;
  for (int u = 0; u < s; ++u) {
    if (old) { pos += dn_nD(u); if (u == d_step) tgt = pos - 1; }
    for (int q = 0; q < dn_nA(u); ++q) { if (na == a_idx) tgt = pos; ++na; ++pos; }
  }
  return tgt < 0 ? 63 : pos - 1 - tgt;
}
DI constexpr int dn_wait(int s, bool old) {
  const int x = dn_young(s, old, s, -1), y = s == 13 ? dn_young(s, old, -1, 8) : s == 18 ? dn_young(s, old, -1, 13) : 63;
  return x < y ? x : y;
}
#define OWARM 0
#define RPOS 0
DI bool has_sent(const u32x4& v) {
  const u32 m = __builtin_elementwise_max(__builtin_elementwise_max(v.x, v.y), __builtin_elementwise_max(v.z, v.w));
  return __builtin_amdgcn_ballot_w64(m == 0xffffffffu) != 0ull;
}
template <int IMM> DI void ensure(u32x4& v, const char* sbase, u32 voff) {
  if (__builtin_expect(has_sent(v), 0)) {
    for (u32 spins = 0; spins < SPIN_CAP; ++spins) {
      gld_sc1<IMM>(v, sbase, voff); WAITV(0); PIN(v);
      if (!has_sent(v)) break;
    }
  }
}
DI void gate(const char* sbase, u32 voff) {
  for (u32 spins = 0; spins < SPIN_CAP; ++spins) {
    u32 v;
    gld32_sc1(v, sbase, voff); WAITV(0); PIN(v);
    if (__builtin_amdgcn_ballot_w64(v == 0xffffffffu) == 0ull) break;
    __builtin_amdgcn_s_sleep(3);
  }
}

__global__ void __launch_bounds__(512, 2) NOPK k2(P2 p) {
  const int tid = tidx(), lane = tid & 63;
  int w = __builtin_amdgcn_readfirstlane(tid >> 6);
  const int b = bidx();
  int mt = MAP == 0 ? 4 * ((b & 7) >> 1) + (b >> 6) : (b & 7) * 2 + (b >> 7), j = MAP == 0 ? 8 * (b & 1) + ((b >> 3) & 7) : (b >> 3) & 15;
  const int c32 = lane & 31, h = lane >> 5;
  const char* ws = p.ws;
  const __amdgpu_buffer_rsrc_t rs = __builtin_amdgcn_make_buffer_rsrc(p.ws, 0, WS_END, 0x00020000);
  const u32 l16 = lane * 16;
#define PHASE() OPQ(w); OPQ(mt); OPQ(j); OPQ(ws); const int tt = w & 3, kh = w >> 2, th = j & 1, n8 = j >> 1; (void)tt; (void)kh; (void)th; (void)n8
  u32x4 a[24];
  u32x4 st[18];
  const int rsw = c32 ^ (16 * (w >> 2));
  const u32 frow = (h * 32 + rsw) * 16;
  u32 imga[8];
  {
    PHASE();
    const char* wq = ws + WS_WQF + (size_t)j * WQB;
    const char* wqw = wq + (kh * 20 + tt) * 1024;
    const u32 wql = (kh * 20 + tt) * 1024;
    const char* n1 = ws + WS_N1 + (size_t)((mt * 4 + tt) * 40 + kh * 20) * 1024;
    auto issA = [&](auto k_) LAM { constexpr int k = decltype(k_)::v; gld<(k & 3) * 1024>(a[k], n1 + (k >> 2) * 4096, l16); };
    auto issW = [&](int s, int nb) LAM { dma(wqw + nb * 40960 + s * 4096, l16, wql + nb * 40960 + s * 4096); };
    const int rk16 = w < 5 ? 5 * (mt >> 1) + w : (mt * 2 + th) * 3 + (w - 5);
    const char* rsrc = w < 5 ? (const char*)((mt & 1) ? p.wu : p.wg) + (size_t)rk16 * 49152 + 192 * j : (const char*)p.wd + (size_t)rk16 * 20480 + 160 * n8;
    const u32 rimg = 122880 + w * 3072;
    auto issR = [&]() LAM {
#pragma unroll
      for (int m = 0; m < 3; ++m) {
        const u32 x = 64 * m + lane, i5 = x < 160 ? x : 159, r12 = (x * 171) >> 11, r10 = (i5 * 205) >> 11;
        dma_nt(rsrc, w < 5 ? r12 * 2880 + x * 16 : r10 * 1120 + i5 * 16, rimg + m * 1024);
      }
    };
    if (RPOS == 1) issR();
    u32x4 mp[2];
    u32 mdst; bool plus1;
    {
      const int m1 = tid >= 160, k1 = 4 * (tid - 160 * m1);
      const int u2 = tid - 320, m2 = (u2 * 205) >> 12, e2 = u2 - 20 * m2;
      const int col2 = (m2 == 0 ? 1280 : m2 == 2 ? 3200 : m2 == 4 ? 1920 : 2560) + 80 * (m2 < 3 ? n8 : (mt >> 1)) + 4 * (m2 < 5 ? e2 : 0);
      const int col = tid < 320 ? 640 * m1 + k1 : col2;
#pragma unroll
      for (int q = 0; q < 2; ++q) gld<0>(mp[q], ws + WS_MODP + q * 15360, (u32)col * 4);
      sfor<0, 4>(issA);
      issW(0, 0); issW(0, 1); issW(0, 2);
      if (RPOS == 2) issR();
      mdst = tid < 320 ? L_GS + (k1 >> 3) * 32 + (m1 ? 0 : 16) + (k1 & 4) * 2 : L_MODS + u2 * 16;
      plus1 = tid < 320 ? m1 != 0 : (m2 & 1) != 0;
    }
    if (tid == 0) st128(L_FLAG, u32x4{0u, 0u, 0u, 0u});
    f32x16 acc[3];
    const u32 wb = kh * 20480 + frow, gb_ = L_GS + kh * 1280 + h * 32;
    u32x4 gs[3][2], wf[2][3], ro[3];
    f32x4 csum = {0.f, 0.f, 0.f, 0.f};
    auto ldgs = [&](int k) LAM { gs[k % 3][0] = lds128(gb_ + k * 64); gs[k % 3][1] = lds128(gb_ + k * 64 + 16); };
    auto modulate = [&](int k) LAM {
      PIN(a[k]);
#pragma unroll
      for (int e = 0; e < 4; ++e)
        a[k][e] = pkfma16(a[k][e], gs[k % 3][0][e], gs[k % 3][1][e]);
    };
    WAITV(RPOS == 2 ? 10 : 7);
    f32x4 mv;
    {
#pragma unroll
      for (int q = 0; q < 2; ++q) PIN(mp[q]);
      float mxv = 0.f;
#pragma unroll
      for (int e = 0; e < 4; ++e) {
        const float s = rbf(u2f(mp[0][e]) + u2f(mp[1][e]));
        mv[e] = plus1 ? rbf(1.f + s) : s;
        mxv = __builtin_fmaxf(mxv, __builtin_fabsf(mv[e]));
      }
      if (tid < 320) st64(mdst, u32x2{pkh2(mv[0], mv[1]), pkh2(mv[2], mv[3])}); else if (tid < 420) stf4(mdst, mv);
      const bool big = __builtin_amdgcn_ballot_w64(tid < 320 && !(mxv < 2000.f)) != 0ull;
      if (lane == 0) *(LAS u32*)(size_t)(L_FLAG + 16 + w * 4) = big;
    }
    WAITV(RPOS == 2 ? 3 : 0);
    BAR();
    auto grpA = [&](int cond, int s) LAM { gld4_if(cond, a[4 * s], a[4 * s + 1], a[4 * s + 2], a[4 * s + 3], n1 + s * 4096, l16); };
    const int kl = kh ^ 1;
    auto grpW = [&](int s) LAM { issW(s, 0); issW(s, 1); issW(s, 2); };
    grpA(kh, 1); if (kh) { grpW(1); if (RPOS == 0) issR(); }
    bool scaled;
    {
      const u32x4 f0 = lds128(L_FLAG + 16);
      const u32 f1 = *(const LAS u32*)(size_t)(L_FLAG + 32);
      ldgs(0); ldgs(1);
      scaled = __builtin_amdgcn_readfirstlane(f0.x | f0.y | f0.z | f0.w | f1) != 0;
      if (scaled) {
        BAR();
        const float sc = 9.765625e-4f;
        if (tid < 320) st64(mdst, u32x2{pkh2(mv[0] * sc, mv[1] * sc), pkh2(mv[2] * sc, mv[3] * sc)});
        BAR();
        ldgs(0); ldgs(1);
      }
    }
    modulate(0);
    SB();
    const Run<30, 8, false> rb3(wq + 122880, 122880, w);
    sfor<0, 21>([&](auto k_) LAM {
      constexpr int k = decltype(k_)::v, s = k >> 2, i = k & 3;
      if constexpr (i == 3 && k < 16) WAITV(k == 3 && RPOS == 0 ? 13 : 10);
      if constexpr (i == 0 && k > 0 && k < 20) {
        WAITV(k == 4 && RPOS == 0 ? 10 : k == 16 ? 4 : 7); BAR();
      }
      if constexpr (k < 20) {
#pragma unroll
        for (int nb = 0; nb < 3; ++nb) wf[k & 1][nb] = lds128(wb + nb * 40960 + k * 1024);
      }
      if constexpr (k == 1) {
        f32x16 z;
#pragma unroll
        for (int e = 0; e < 16; ++e) z[e] = 0.f;
#pragma unroll
        for (int nb = 0; nb < 3; ++nb) acc[nb] = MFMA32H(wf[0][nb], a[0], z);
      } else if constexpr (k > 1) {
#pragma unroll
        for (int nb = 0; nb < 3; ++nb) acc[nb] = MFMA32H(wf[(k - 1) & 1][nb], a[k - 1], acc[nb]);
      }
      if constexpr (k + 1 < 20) modulate(k + 1);
      if constexpr (k + 2 < 20) ldgs(k + 2);
      if constexpr (k == 8) {
        const u32 q4 = (lane >> 2) & 3, nh = (lane >> 4) & 1;
        const u32 r0 = rimg + (w < 5 ? (8 * h + q4) * 192 + 32 * nh : (4 * nh + q4) * 160 + 32 * h) + (lane & 3) * 8, r1 = r0 + (w < 5 ? 768 : 1280);
#pragma unroll
        for (int u = 0; u < 3; ++u) ro[u] = ldstr2(r0 + 64 * u, r1 + 64 * u);
      }
      if constexpr (k == 9 || k == 10) {
        if (w < 5) {
          const u32 ga_ = L_MODS + 960 + w * 64 + h * 32;
          const f32x4 g0 = ldsf4(ga_), g1 = ldsf4(ga_ + 16), s0 = ldsf4(ga_ + 320), s1 = ldsf4(ga_ + 336);
#pragma unroll
          for (int u = (k == 9 ? 0 : 2); u < (k == 9 ? 2 : 3); ++u) {
            float c = 0.f, bi = 0.f;
#pragma unroll
            for (int e = 0; e < 4; ++e) {
              const u32 v = ro[u][e];
              const float x0 = lo16(v), x1 = hi16(v), ge = e < 2 ? g0[2 * e] : g1[2 * e - 4], go = e < 2 ? g0[2 * e + 1] : g1[2 * e - 3];
              const float se = e < 2 ? s0[2 * e] : s1[2 * e - 4], so_ = e < 2 ? s0[2 * e + 1] : s1[2 * e - 3];
              c = fma1(ge, x0, c); c = fma1(go, x1, c); bi = fma1(se, x0, bi); bi = fma1(so_, x1, bi);
            }
            auto r = __builtin_amdgcn_permlane32_swap(f2u(c), f2u(bi), false, false);
            *(LAS float*)(size_t)(w * 768 + u * 256 + (c32 >> 3) * 64 + h * 32 + (c32 & 7) * 4) = u2f(r[0]) + u2f(r[1]);
          }
        }
      }
      if constexpr (k == 17) {
        if (w == 7) {
#pragma unroll
          for (int u = 0; u < 5; ++u) csum += ldsf4(u * 768 + (lane < 48 ? l16 : 0u));
        }
      }
      if constexpr (k == 16) {
        const u32 x2 = 16 * kh + 4 * h, tb = 12288 + (32 * tt + c32) * 48 + 2 * x2;
#pragma unroll
        for (int u = 0; u < 8; ++u) {
          const u32 part = ((x2 + 32 * (u >> 1) + 8 * (u & 1)) * 205) >> 12;
          imga[u] = part * 18392 + tb + 64 * (u >> 1) + 16 * (u & 1); PIN(imga[u]);
        }
      }
      SB();
      if constexpr (k == 0) { grpA(kl, 1); if (!kh) { grpW(1); if (RPOS == 0) issR(); } }
      if constexpr (k == 1) grpA(kl, 2);
      if constexpr (k == 2) { if (!kh) grpW(2); grpA(kh, 2); }
      if constexpr (k == 3) { if (kh) grpW(2); }
      if constexpr (s == 1 || s == 2) {
        if constexpr (i == 0) grpA(kl, s + 2);
        if constexpr (i == 1) { if (!kh) grpW(s + 2); }
        if constexpr (i == 2) grpA(kh, s + 2);
        if constexpr (i == 3) { if (kh) grpW(s + 2); }
      }
      if constexpr (k == 12) { if (!kh) rb3.all(l16); }
      if constexpr (k == 14) { if (kh) rb3.all(l16); }
      if constexpr (k == 16) {
        u32 off0, du;
        if (w < 5) { const int k2 = rk16 >= 20, i2 = rk16 - 20 * k2; off0 = WS_WGF + j * WGB + (k2 * 20 + i2) * 2048 + (mt & 1) * 1024 + l16; du = 81920; }
        else { const int k2 = rk16 >= 48, kk = rk16 - 48 * k2; off0 = WS_WDF + n8 * WDB + ((kk >> 1) * 10 + k2 * 5 + h) * 1024 + (kk & 1) * 512 + c32 * 16; du = 2048; }
        asm volatile("" ::: "memory");
        __builtin_amdgcn_raw_buffer_store_b128(ro[0], rs, off0, 0, 16);
        __builtin_amdgcn_raw_buffer_store_b128(ro[1], rs, off0 + du, 0, 16);
        __builtin_amdgcn_raw_buffer_store_b128(ro[2], rs, (w < 5 || h == 0) ? off0 + 2 * du : 0xfffffff0u, 0, 16);
        asm volatile("" ::: "memory");
      }
      SB();
    });
    WAITV(3);
    BAR();
    if (w == 7 && lane < 48) __builtin_amdgcn_raw_buffer_store_b128(__builtin_bit_cast(u32x4, csum), rs, WS_CSP + (u32)(j * 16 + mt) * 768 + l16, 0, 16);
    const u32 exw = (w >> 1) * 18432 + (w & 1) * 6144 + l16, exr = ((w ^ 4) >> 1) * 18432 + (w & 1) * 6144 + l16;
#pragma unroll
    for (int nb = 0; nb < 3; ++nb) {
      stf4(exw + nb * 2048, f32x4{acc[nb][8], acc[nb][9], acc[nb][10], acc[nb][11]});
      stf4(exw + nb * 2048 + 1024, f32x4{acc[nb][12], acc[nb][13], acc[nb][14], acc[nb][15]});
    }
    if (kh) st64((tid >> 7) * 18432 + 12288 + (tid & 127) * 48 + 40, u32x2{0x3f803f80u, 0x3f803f80u});
    BAR();
    const float osc = scaled ? 1024.f : 1.f, qs = QSCALE * (1.f / 4096.f) * osc, sc2 = kh ? qs : osc;
#pragma unroll
    for (int nb = 0; nb < 3; ++nb)
#pragma unroll
      for (int q = 0; q < 2; ++q) {
        const f32x4 o = ldsf4(exr + nb * 2048 + q * 1024);
        float v0 = acc[nb][4 * q] + o[0], v1 = acc[nb][4 * q + 1] + o[1], v2 = acc[nb][4 * q + 2] + o[2], v3 = acc[nb][4 * q + 3] + o[3];
        if (nb == 2) { v0 *= sc2; v1 *= sc2; v2 *= sc2; v3 *= sc2; }
        else if (scaled) { v0 *= osc; v1 *= osc; v2 *= osc; v3 *= osc; }
        st64(imga[2 * nb + q], u32x2{pk2(v0, v1), pk2(v2, v3)});
      }
    BAR();
    {
      const u32 dst = WS_KVX + (u32)(mt * 16 + j) * KVB + w * 3072 + l16, src = (w >> 1) * 18432 + 12288 + (w & 1) * 3072 + l16;
#pragma unroll
      for (int i = 0; i < 3; ++i) __builtin_amdgcn_raw_buffer_store_b128(lds128(src + i * 1024), rs, dst + i * 1024, 0, 16);
    }
    f32x16 acc3;
#pragma unroll
    for (int e = 0; e < 16; ++e) acc3[e] = 0.f;
    {
      const int r3 = rsw < 24 ? rsw : (rsw < 28 ? rsw - 4 : rsw - 12);
      const u32 wb3 = 122880 + kh * 15360 + (h * 24 + r3) * 16;
      u32x4 w3[4];
#pragma unroll
      for (int k = 0; k < 3; ++k) w3[k] = lds128(wb3 + k * 768);
#pragma unroll
      for (int k = 0; k < 20; ++k) {
        if (k + 3 < 20) w3[(k + 3) & 3] = lds128(wb3 + (k + 3) * 768);
        acc3 = MFMA32H(w3[k & 3], a[k], acc3);
        if (k == 9) {
          WAITVM(0);
          if (lane == 0) {
            const u32 old = __hip_atomic_fetch_add((LAS u32*)(size_t)L_FLAG, 1u, __ATOMIC_RELAXED, __HIP_MEMORY_SCOPE_WORKGROUP);
            if (old == 7u) {
              __hip_atomic_store((gu32*)(ws + WS_KVF + (mt * 16 + j) * 128), 1u, RLX_AGENT);
              signal(ws, C_RG + j * 256); signal(ws, C_RD + n8 * 256);
            }
          }
        }
        SB();
      }
    }
    const u32 ex3w = (w < 4 ? 73728 : 84992) + w * 2048 + l16, ex3r = (w < 4 ? 93184 : 65536) + w * 2048 + l16;
    stf4(ex3w, f32x4{acc3[8], acc3[9], acc3[10], acc3[11]});
    stf4(ex3w + 1024, f32x4{acc3[12], acc3[13], acc3[14], acc3[15]});
    BAR();

#pragma unroll
    for (int q = 0; q < 2; ++q) {
      const f32x4 o = ldsf4(ex3r + q * 1024);
      st64(imga[6 + q], u32x2{pk2((acc3[4 * q] + o[0]) * qs, (acc3[4 * q + 1] + o[1]) * qs), pk2((acc3[4 * q + 2] + o[2]) * qs, (acc3[4 * q + 3] + o[3]) * qs)});
    }
    BAR();
  }
  u32x2 xr[3];
  const int c16 = lane & 15, g4 = lane >> 4;
  {
    PHASE();
    const int hh = w >> 2, qb = w < 4 ? w : 7 - w;
    u32x4 q0f, q1f;
    {
      const u32 qa = (4 + hh) * 18432 + 12288 + (32 * qb + c32) * 48;
      const u32x2 x2 = lds64(qa + 32);
      q0f = lds128(qa + 16 * h);
      q1f = u32x4{h ? 0u : x2.x, h ? 0u : x2.y, 0u, 0u};
      const int tok64 = 128 * mt + 64 * th + 16 * tt + c16;
#pragma unroll
      for (int m = 0; m < 3; ++m) gld64(xr[m], (const char*)p.x, (u32)(tok64 * 1280 + (80 * n8 + 16 * ((m + 3 * kh) % 5) + 4 * g4) * 2));
    }
    BAR();
    const char* sg0 = ws + WS_WGF + (size_t)j * WGB + (kh * 40 + tt * 10) * 1024;
    const int tmin = mt >= 2 ? 0 : 8 - 4 * mt - qb;
    const u32 kb = hh * 18432 + (32 * qb + c32) * 48, kb2 = kb + 16 * h;
    const u32 vb = (2 + hh) * 18432 + (32 * qb + 4 * h + ((lane >> 2) & 3)) * 48 + ((lane >> 4) & 1) * 32 + (lane & 3) * 8;
    const int qlim = c32 - 4 * h;
    const int dw = (w - 2) & 3;
    const bool duty = w >= 2 && w <= 5;
    const Run<70, 4, false> rwo(ws + WS_WOF + (size_t)n8 * WOB, wo_slot(0), dw);
    const u32 fa = WS_ACT + (u32)((mt * 4 + dw) * 96 + 6 * j) * 1024 + l16;
    const u32 fx = dw < 2 ? WS_X1 + (u32)((mt * 4 + 2 * th + dw) * 40 + 5 * n8) * 1024 + l16 : WS_STAT + (u32)((mt * 4 + 2 * th + h) * 8 + n8) * 512 + c32 * 16;
    const int ko = 40 * j + 20 * hh + 4 * h;
    u32 ob = WS_OAF + (u32)((mt * 4 + qb) * 40) * 1024 + c32 * 16 + (ko >> 3) * 512 + ((ko >> 2) & 1) * 8;
    PIN(ob);
    auto lds_wait = [&](u32 addr, u32 want) LAM {
      for (u32 spins = 0; spins < SPIN_CAP * 8; ++spins) {
        if ((u32)__builtin_amdgcn_readfirstlane(__hip_atomic_load((LAS u32*)(size_t)addr, __ATOMIC_RELAXED, __HIP_MEMORY_SCOPE_WORKGROUP)) >= want) break;
        __builtin_amdgcn_s_sleep(1);
      }
      asm volatile("" ::: "memory");
    };
    {
      if (!duty) {
        const int sl = w >= 6;
        const u32 l0 = (w & 1) * 36864 + sl * 6144;
        if (mt + sl >= 2) {
          poll_ge(ws, WS_KVF + ((mt - 2 + sl) * 16 + j) * 128, 1u);
          const char* src = ws + WS_KVX + (size_t)((mt - 2 + sl) * 16 + j) * KVB + (w & 1) * 12288;
#pragma unroll
          for (int u = 0; u < 4; ++u) {
            const u32 l1 = l0 + (u >> 1) * 18432 + (u & 1) * 3072;
            dmai<0, true>(src + u * 3072, l16, l1); dmai<1024, true>(src + u * 3072, l16, l1); dmai<2048, true>(src + u * 3072, l16, l1);
          }
        } else {
#pragma unroll
          for (int u = 0; u < 12; ++u) st128(l0 + (u / 6) * 18432 + (u % 6) * 1024 + l16, u32x4{0u, 0u, 0u, 0u});
        }
      }
    }
    auto halo = [&]() LAM {
      if (!duty) {
        asm volatile("s_waitcnt vmcnt(0) lgkmcnt(0)" ::: "memory");
        if (lane == 0) __hip_atomic_fetch_add((LAS u32*)(size_t)(L_FLAG + (w >= 6 ? 8 : 4)), 1u, __ATOMIC_RELAXED, __HIP_MEMORY_SCOPE_WORKGROUP);
      }
    };
    f32x16 oacc;
    u32 rcnt;
    auto attn = [&](auto full_) LAM {
      constexpr bool FULL = decltype(full_)::v;
      u32x4 ka, kq, vf[2][2];
      kq = u32x4{0u, 0u, 0u, 0u};
      auto ldkv = [&](int t) LAM {
        const u32x2 x2 = lds64(kb + t * 1536 + 32);
        ka = lds128(kb2 + t * 1536); kq.x = x2.x; kq.y = x2.y;
#pragma unroll
        for (int s2 = 0; s2 < 2; ++s2) { const u32 va = vb + (32 * t + 16 * s2) * 48; vf[t & 1][s2] = ldstr2(va, va + 384); }
      };
      auto splat = [&](float v) LAM -> f32x16 {
        f32x16 c;
#pragma unroll
        for (int e = 0; e < 16; ++e) c[e] = v;
        return c;
      };
      auto pre = [&](auto m_) LAM {
        constexpr int m = decltype(m_)::v;
        if constexpr (m == 2) gld32_sc1(rcnt, ws, lane < 32 ? C_RG + j * 256 : C_RD + n8 * 256);
        if constexpr (m == 4) {
          if (duty) WAITV(6); else WAITV(0);
          PIN(rcnt);
          if (__builtin_amdgcn_ballot_w64(rcnt < (lane < 32 ? 16u : 32u)) != 0ull) { poll_ge(ws, C_RG + j * 256, 16u); poll_ge(ws, C_RD + n8 * 256, 32u); }
        }
        if constexpr (m >= 4) {
          gld_sc1<((2 * m - 8) & 3) * 1024>(st[2 * m - 8], sg0 + ((2 * m - 8) >> 2) * 4096, l16);
          gld_sc1<((2 * m - 7) & 3) * 1024>(st[2 * m - 7], sg0 + ((2 * m - 7) >> 2) * 4096, l16);
        }
        if constexpr (m == 1 && OWARM) {
#pragma unroll
          for (int g = 0; g < 3; ++g)
            if (g < 2 || h == 0) __builtin_amdgcn_raw_buffer_store_b64(u32x2{0u, 0u}, rs, ob + g * 512, 0, 16);
          asm volatile("" ::: "memory");
        }
        if (duty) {
          rwo.go<2 * m>(l16); rwo.go<2 * m + 1>(l16);
          const u32x4 f = {~0u, ~0u, ~0u, ~0u};
          if constexpr (m < 6) __builtin_amdgcn_raw_buffer_store_b128(f, rs, fa + m * 1024, 0, 16);
          if constexpr (m >= 4) { if (dw < 2 || (dw == 2 && m == 4)) __builtin_amdgcn_raw_buffer_store_b128(f, rs, fx + (m - 4) * 1024, 0, 16); }
          asm volatile("" ::: "memory");
        }
        SB();
      };
      auto sync = [&](int t) LAM {
        if (t >= 4) { if (qb == 7 - t) lds_wait(L_FLAG + 8, 2u); } else { if (qb == 3 - t) lds_wait(L_FLAG + 4, 2u); }
      };
      f32x16 sb[2], cm;
      float pe[16];
      ldkv(8);
      sb[0] = MFMA32(ka, q0f, splat(0.f));
      sb[0] = MFMA32(kq, q1f, sb[0]);
      SB();
      pre(IC<0>{});
#pragma unroll
      for (int e = 0; e < 16; ++e) pe[e] = ((e & 3) + 8 * (e >> 2) > qlim) ? -1.0e30f : sb[0][e];
      float mx = __builtin_fmaxf(pe[0], pe[1]);
#pragma unroll
      for (int e = 2; e < 16; e += 2) mx = max3(mx, pe[e], pe[e + 1]);
      mx = -max32(mx);
      cm = splat(mx);
      auto cin = [&](int t) LAM -> f32x16 { if constexpr (FULL) return cm; else return splat(t >= tmin ? mx : -1.0e30f); };
#pragma unroll
      for (int s2 = 0; s2 < 2; ++s2) {
        u32x4 pf;
#pragma unroll
        for (int e = 0; e < 4; ++e) pf[e] = pk2(__builtin_amdgcn_exp2f(pe[8 * s2 + 2 * e] + mx), __builtin_amdgcn_exp2f(pe[8 * s2 + 2 * e + 1] + mx));
        if (s2 == 0) oacc = MFMA32(vf[0][0], pf, splat(0.f)); else oacc = MFMA32(vf[0][1], pf, oacc);
      }
      SB();
      halo();
      pre(IC<1>{});
      sync(7);
      ldkv(7);
      sb[1] = MFMA32(ka, q0f, cin(7));
      sb[1] = MFMA32(kq, q1f, sb[1]);
      SB();
      sfor<0, 8>([&](auto u_) LAM {
        constexpr int t = 7 - decltype(u_)::v, c = t & 1, n = c ^ 1;
        u32x4 pf0, pf1;
        if constexpr (t > 0) { if constexpr (t - 1 != 7) sync(t - 1); ldkv(t - 1); }
        if constexpr (t == 0) {
#pragma unroll
          for (int e = 0; e < 16; ++e) sb[c][e] = ((e & 3) + 8 * (e >> 2) > qlim) ? sb[c][e] : -1.0e30f;
        }
#pragma unroll
        for (int e = 0; e < 8; ++e) pe[e] = __builtin_amdgcn_exp2f(sb[c][e]);
        SB();
        if constexpr (t > 0) { sb[n] = MFMA32(ka, q0f, cin(t - 1)); SB(); }
#pragma unroll
        for (int e = 8; e < 12; ++e) pe[e] = __builtin_amdgcn_exp2f(sb[c][e]);
#pragma unroll
        for (int e = 0; e < 4; ++e) pf0[e] = pk2(pe[2 * e], pe[2 * e + 1]);
        SB();
        if constexpr (t > 0) { sb[n] = MFMA32(kq, q1f, sb[n]); SB(); }
#pragma unroll
        for (int e = 12; e < 16; ++e) pe[e] = __builtin_amdgcn_exp2f(sb[c][e]);
        SB();
        oacc = MFMA32(vf[c][0], pf0, oacc);
        SB();
#pragma unroll
        for (int e = 0; e < 4; ++e) pf1[e] = pk2(pe[8 + 2 * e], pe[8 + 2 * e + 1]);
        SB();
        if constexpr (t > 0) pre(IC<9 - t>{});
        oacc = MFMA32(vf[c][1], pf1, oacc);
        SB();
      });
    };
    auto exact = [&]() LAM {
      auto sc = [&](int t, float c0, f32x16& s) LAM {
        const u32x2 x2 = lds64(kb + t * 1536 + 32);
#pragma unroll
        for (int e = 0; e < 16; ++e) s[e] = c0;
        s = MFMA32(lds128(kb2 + t * 1536), q0f, s);
        s = MFMA32((u32x4{x2.x, x2.y, 0u, 0u}), q1f, s);
#pragma unroll
        for (int e = 0; e < 16; ++e) s[e] = (u32)(32 * t + (e & 3) + 8 * (e >> 2) - qlim - 1) < 256u ? s[e] : -1.0e30f;
      };
      float mx = -1.0e30f;
      f32x16 s;
#pragma nounroll
      for (int t = tmin; t < 9; ++t) {
        sc(t, 0.f, s);
#pragma unroll
        for (int e = 0; e < 16; e += 2) mx = max3(mx, s[e], s[e + 1]);
      }
      mx = -max32(mx);
#pragma unroll
      for (int e = 0; e < 16; ++e) oacc[e] = 0.f;
#pragma nounroll
      for (int t = tmin; t < 9; ++t) {
        sc(t, mx, s);
#pragma unroll
        for (int s2 = 0; s2 < 2; ++s2) {
          u32x4 pf;
#pragma unroll
          for (int e = 0; e < 4; ++e) pf[e] = pk2(__builtin_amdgcn_exp2f(s[8 * s2 + 2 * e]), __builtin_amdgcn_exp2f(s[8 * s2 + 2 * e + 1]));
          const u32 va = vb + (32 * t + 16 * s2) * 48;
          oacc = MFMA32(ldstr2(va, va + 384), pf, oacc);
        }
      }
    };
    if (mt >= 2) attn(BoolC<true>{}); else attn(BoolC<false>{});
    if (__builtin_amdgcn_ballot_w64(h != 0 && !(oacc[8] < ATT_LIMIT)) != 0ull) exact();
    const float lx = xor32f(oacc[8]);
    const float inv = __builtin_amdgcn_rcpf(64.f * (h ? oacc[8] : lx));
#pragma unroll
    for (int g = 0; g < 3; ++g) {
      const u32x2 o = {pk2(oacc[4 * g] * inv, oacc[4 * g + 1] * inv), pk2(oacc[4 * g + 2] * inv, oacc[4 * g + 3] * inv)};
      if (g < 2 || h == 0) __builtin_amdgcn_raw_buffer_store_b64(o, rs, ob + g * 512, 0, 16);
    }
    WAITVM(0);
    BAR();
    if (tid == 0) signal(ws, C_O + mt * 256);
  }
  u32 wbase[5], avoff;
  u32x2 x1k[3];
  f32x4 c5[5];
  {
    PHASE();
    const char* sg1 = ws + WS_WGF + (size_t)j * WGB + 81920 + (kh * 40 + tt * 8) * 1024;
    auto stage1 = [&]() LAM { sfor<0, 8>([&](auto m_) LAM { constexpr int m = decltype(m_)::v; gld_sc1<(m & 3) * 1024>(st[10 + m], sg1 + (m >> 2) * 4096, l16); }); };
    if (w >= 4) { __builtin_amdgcn_s_sleep(2); Run<30, 4, false>(ws + WS_WOF + (size_t)n8 * WOB + 71680, wo_slot(7), w - 4).all(l16); }
    stage1();
#pragma unroll
    for (int m = 0; m < 5; ++m) wbase[m] = (kh * 5 + (m + 3 * kh) % 5) * 1024 + l16;
    avoff = (g4 >> 1) * 1024 + (g4 & 1) * 512 + (16 * (tt & 1) + c16) * 16;
    const int tt64 = mt * 4 + 2 * th + (tt >> 1);
    const char* ab = ws + WS_OAF + (size_t)(tt64 * 40 + 20 * kh) * 1024;
    const u32 ew = 73728 + (tt * 5) * 1024 + l16;
    u32 xb = WS_X1 + (u32)(tt64 * 40 + 5 * n8) * 1024 + (g4 >> 1) * 512 + (16 * (tt & 1) + c16) * 16 + (g4 & 1) * 8 + kh * 3072;
    u32 sta = WS_STAT + (u32)((tt64 * 8 + n8) * 32 + 16 * (tt & 1) + c16) * 16 + kh * 8;
    u32 mo = L_MODS + kh * 192 + g4 * 16;
    PIN(xb); PIN(sta); PIN(mo); PIN(avoff);
    auto iss = [&](auto s_) LAM { constexpr int s = decltype(s_)::v; gld_sc1<(s & 1) * 2048>(a[s], ab + (s >> 1) * 4096, avoff); };
    u32x4 wf[3][5];
#pragma unroll
    for (int s = 0; s < 2; ++s)
#pragma unroll
      for (int m = 0; m < 5; ++m) wf[s][m] = lds128(wbase[m] + wo_slot(s));
#pragma unroll
    for (int m = 0; m < 5; ++m) c5[m] = f32x4{0.f, 0.f, 0.f, 0.f};
    if (w == 0) poll_ge(ws, C_O + mt * 256, 16u);
    BAR();
    sfor<0, 5>(iss);
    sfor<0, 10>([&](auto s_) LAM {
      constexpr int s = decltype(s_)::v;
      WAITV(s == 0 ? 4 : s == 1 ? 6 : 9 - s);
      if (s == 4) BAR();
      PIN(a[s]);
      if constexpr (s + 2 < 10) {
#pragma unroll
        for (int m = 0; m < 5; ++m) wf[(s + 2) % 3][m] = lds128(wbase[m] + wo_slot(s + 2));
      }
#pragma unroll
      for (int m = 0; m < 5; ++m) c5[m] = MFMA16(wf[s % 3][m], a[s], c5[m]);
      SB();
      if constexpr (s == 0) sfor<5, 8>(iss);
      if constexpr (s == 1) sfor<8, 10>(iss);
      SB();
    });
    stf4(ew + ((3 + 3 * kh) % 5) * 1024, c5[3]);
    stf4(ew + ((4 + 3 * kh) % 5) * 1024, c5[4]);
    if (kh) stf4(ew, c5[2]);
    BAR();
    float xs[12];
#pragma unroll
    for (int m = 0; m < 3; ++m) PIN(xr[m]);
#pragma unroll
    for (int m = 0; m < 3; ++m) {
      if (m < 2 || kh == 0) {
        const f32x4 o = ldsf4(ew + kh * 3072 + m * 1024), ga = ldsf4(mo + m * 64), g2 = ldsf4(mo + 320 + m * 64);
        const u32 y01 = pk2(c5[m][0] + o[0], c5[m][1] + o[1]), y23 = pk2(c5[m][2] + o[2], c5[m][3] + o[3]);
        const u32 t01 = pk2(ga[0] * lo16(y01), ga[1] * hi16(y01)), t23 = pk2(ga[2] * lo16(y23), ga[3] * hi16(y23));
        x1k[m] = u32x2{pk2(lo16(xr[m].x) + lo16(t01), hi16(xr[m].x) + hi16(t01)), pk2(lo16(xr[m].y) + lo16(t23), hi16(xr[m].y) + hi16(t23))};
        xs[4 * m] = lo16(x1k[m].x); xs[4 * m + 1] = hi16(x1k[m].x); xs[4 * m + 2] = lo16(x1k[m].y); xs[4 * m + 3] = hi16(x1k[m].y);
        __builtin_amdgcn_raw_buffer_store_b64(u32x2{pk2(xs[4 * m] * g2[0], xs[4 * m + 1] * g2[1]), pk2(xs[4 * m + 2] * g2[2], xs[4 * m + 3] * g2[3])}, rs, xb + m * 1024, 0, 16);
      } else {
        x1k[m] = u32x2{0u, 0u};
        xs[8] = xs[9] = xs[10] = xs[11] = 0.f;
      }
    }
    float s = ((xs[0] + xs[1]) + (xs[2] + xs[3])) + ((xs[4] + xs[5]) + (xs[6] + xs[7])) + ((xs[8] + xs[9]) + (xs[10] + xs[11]));
    s = sum32(sum16(s));
    const float mean = s * (kh ? 1.f / 32.f : 1.f / 48.f);
    float q0 = 0.f, q1 = 0.f;
#pragma unroll
    for (int e = 0; e < 4; ++e) { const float d0 = xs[e] - mean, d1 = xs[4 + e] - mean; q0 = fma1(d0, d0, q0); q1 = fma1(d1, d1, q1); }
    if (kh == 0) {
#pragma unroll
      for (int e = 8; e < 12; ++e) { const float d = xs[e] - mean; q0 = fma1(d, d, q0); }
    }
    const float q = sum32(sum16(q0 + q1));
    if (g4 == 0) __builtin_amdgcn_raw_buffer_store_b64(u32x2{f2u(mean), f2u(q)}, rs, sta, 0, 16);
    BAR();
  }
  {
    PHASE();
    const bool old4 = w < 4;
    const char* wgb = ws + WS_WGF + (size_t)j * WGB;
    const char* wdb = ws + WS_WDF + (size_t)n8 * WDB;
    u32x4 stv[4], csv[2];
    const char* ab = ws + WS_X1 + (size_t)((mt * 4 + tt) * 40 + 20 * kh) * 1024;
    const char* sb = ws + WS_STAT + (size_t)(mt * 4 + tt) * 4096;
    auto iss = [&](auto s_) LAM { constexpr int s = decltype(s_)::v; gld_sc1<(s & 3) * 1024>(a[s], ab + (s >> 2) * 4096, l16); };
    {
      const char* cb = ws + WS_CSP + (size_t)(j * 16 + 4 * (w >> 1) + (w & 1)) * 768;
      const u32 co = lane < 48 ? l16 : 0u;
      gld_sc1<0>(csv[0], cb, co); gld_sc1<1536>(csv[1], cb, co);
    }
#pragma unroll
    for (int m = 0; m < 18; ++m) { PIN(st[m]); st128((m < 10 ? (kh * 40 + tt * 10 + m) : (80 + kh * 32 + tt * 8 + m - 10)) * 1024 + l16, st[m]); }
    float c1 = 0.f, d2 = 0.f;
    u32x4 bcor = {0u, 0u, 0u, 0u};
    u32 sw = kh * 40960 + tt * 4096 + l16, sr = (kh ^ 1) * 40960 + tt * 4096 + l16;
    u32 actb = WS_ACT + (u32)((mt * 4 + tt) * 96 + 6 * j + kh) * 1024 + l16;
    const u32 so = h * 2048 + c32 * 16;
    PIN(actb);
    const auto rC = gu_run<1, 16, 20, 4>(wgb, w & 3);
    const auto rA = gu_run<2, 0, 8, 4>(wgb, w & 3);
    const auto rB = gu_run<2, 8, 20, 4>(wgb, w & 3);
    const Run<60, 8, true> rD(wdb, 0, w);
    const Run<90, 8, true> rS(wdb + 61440, 61440, w);
    gate(sb, (c32 >> 2) * 512 + ((c32 >> 1) & 1) * 256 + (c32 & 1) * 8);
    sfor<0, 2>(iss);
    gld_sc1<0>(stv[0], sb, so); gld_sc1<512>(stv[1], sb, so); gld_sc1<1024>(stv[2], sb, so); gld_sc1<1536>(stv[3], sb, so);
    sfor<2, 6>(iss);
    f32x16 ag[2], au[2];
    u32x4 fg[3], fu[3], outv;
    u32 ub = 0;
    auto ldw = [&](auto g_) LAM {
      constexpr int g = decltype(g_)::v, pp = g / 20, i = g % 20;
      constexpr bool first = i == 0 || gu_unit(pp, 0, i) != gu_unit(pp, 0, i - 1) + 1;
      constexpr int i0 = [] { int x = i; while (x > 0 && gu_unit(pp, 0, x) == gu_unit(pp, 0, x - 1) + 1) --x; return x; }();
      if constexpr (first) ub = frow + (kh ? gu_unit(pp, 1, i) : gu_unit(pp, 0, i)) * 2048;
      fg[g % 3] = lds128(ub + (i - i0) * 2048); fu[g % 3] = lds128(ub + (i - i0) * 2048 + 1024);
    };
    auto xwrite = [&](int pp) LAM {
      const f32x16 &G = ag[pp & 1], &U = au[pp & 1];
      stf4(sw, f32x4{G[8], G[9], G[10], G[11]}); stf4(sw + 1024, f32x4{G[12], G[13], G[14], G[15]});
      stf4(sw + 2048, f32x4{U[8], U[9], U[10], U[11]}); stf4(sw + 3072, f32x4{U[12], U[13], U[14], U[15]});
    };
    f32x4 xo[4] = {};
    auto xread = [&]() LAM {
#pragma unroll
      for (int u = 0; u < 4; ++u) xo[u] = ldsf4(sr + u * 1024);
    };
    auto xadd = [&](int pp, int q) LAM {
#pragma unroll
      for (int i = 0; i < 4; ++i) { ag[pp & 1][4 * q + i] += xo[q][i]; au[pp & 1][4 * q + i] += xo[2 + q][i]; }
    };
    auto corr = [&](int pp) LAM {
      ag[pp & 1] = MFMA32(lds128(L_T2 + pp * 1024 + rsw * 16), bcor, ag[pp & 1]);
      au[pp & 1] = MFMA32(lds128(L_T2 + pp * 1024 + 512 + rsw * 16), bcor, au[pp & 1]);
    };
    float eu[2], ee[2];
    auto epi = [&](int pp, int o, int ph) LAM {
#pragma unroll
      for (int i = 0; i < 2; ++i) {
        if (ph == 0) { ee[i] = __builtin_amdgcn_exp2f(ag[pp & 1][2 * o + i] * c1); eu[i] = ag[pp & 1][2 * o + i] * au[pp & 1][2 * o + i]; }
        if (ph == 1) ee[i] = __builtin_amdgcn_rcpf(fma1(ee[i], d2, d2));
      }
      if (ph == 2) outv[o] = pk2(eu[0] * ee[0], eu[1] * ee[1]);
    };
    auto ostore = [&](int pp) LAM { __builtin_amdgcn_raw_buffer_store_b128(outv, rs, actb + pp * 2048, 0, 16); asm volatile("" ::: "memory"); };
    BAR();
    ldw(IC<0>{}); ldw(IC<1>{});
    auto nextA = [&](auto g_) LAM {
      constexpr int g = decltype(g_)::v;
      WAITV(g == 0 ? 9 : g == 1 ? 12 : g == 2 ? 11 : g == 3 ? 14 : 19 - g);
      PIN(a[g]);
      ensure<(g & 3) * 1024>(a[g], ab + (g >> 2) * 4096, l16);
    };
    nextA(IC<0>{});
    sfor<0, 60>([&](auto g_) LAM {
      constexpr int g = decltype(g_)::v, pp = g / 20, i = g % 20;
      if (g == 32) WAITV(0);
      if (g == 43) WAITV(NRS >= 3 ? 4 : 1 + NRS);
      if (g == 12 || g == 22 || g == 32 || g == 43 || g == 50) BAR();
      if constexpr (g + 2 < 60) ldw(IC<g + 2>{});
      if (g == 22 || g == 43) xread();
      if (i == 0) {
        f32x16 z;
#pragma unroll
        for (int e = 0; e < 16; ++e) z[e] = 0.f;
        ag[pp & 1] = MFMA32(fg[g % 3], a[i], z); au[pp & 1] = MFMA32(fu[g % 3], a[i], z);
      } else if (g == 45) {
        ag[0] = MFMA32(fg[g % 3], a[i], ag[0]); au[0] = MFMA32(fu[g % 3], a[i], au[0]);
        if (!kh) corr(2);
      } else {
        ag[pp & 1] = MFMA32(fg[g % 3], a[i], ag[pp & 1]); au[pp & 1] = MFMA32(fu[g % 3], a[i], au[pp & 1]);
      }
      if (g == 4) {
        PIN(csv[0]); PIN(csv[1]);
        f32x4 sm;
#pragma unroll
        for (int e = 0; e < 4; ++e) sm[e] = u2f(csv[0][e]) + u2f(csv[1][e]);
        if (lane < 48) stf4(L_GS + (w >> 1) * 1536 + (w & 1) * 768 + l16, sm);
      }
      if (g == 6) {
        float s = 0.f;
#pragma unroll
        for (int u = 0; u < 4; ++u) s += 48.f * u2f(stv[u][0]) + 32.f * u2f(stv[u][2]);
        c1 = sum32(s) * (1.f / 640.f);
      }
      if (g == 7) {
        float q = 0.f;
#pragma unroll
        for (int u = 0; u < 4; ++u) {
          const float d0 = u2f(stv[u][0]) - c1, d1 = u2f(stv[u][2]) - c1;
          q += u2f(stv[u][1]) + u2f(stv[u][3]) + 48.f * d0 * d0 + 32.f * d1 * d1;
        }
        d2 = sum32(q);
      }
      if (g == 8) {
        const float mean = c1, den = rbf(__builtin_amdgcn_sqrtf(rbf(rbf(d2 * (1.f / 640.f)) + 1e-5f)));
        c1 = -1.4426950408889634f * __builtin_amdgcn_rcpf(den); d2 = den * den;
        const u32 nm = pk2(-mean, -mean), dd = pk2(den, den);
        bcor = u32x4{h ? 0u : nm, h ? 0u : ((nm & 0xffffu) | (dd & 0xffff0000u)), h ? 0u : dd, 0u};
      }
      if (g == 12) {
        const u32 t5 = tid - 256 < 192 ? tid - 256 : 0, t4 = L_GS + (t5 >> 3) * 64 + (t5 & 7) * 4;
        if (kh) {
#pragma unroll
          for (int u = 0; u < 4; ++u) { xo[0][u] = ldsf(t4 + u * 1536); xo[1][u] = ldsf(t4 + u * 1536 + 32); }
        }
      }
      if (g == 14 && kh) {
        const float cs = (xo[0][0] + xo[0][1]) + (xo[0][2] + xo[0][3]), bi = (xo[1][0] + xo[1][1]) + (xo[1][2] + xo[1][3]);
        const u32 h0 = pk2(cs, bi);
        const float cr = cs - lo16(h0), br = bi - hi16(h0);
        const u32 h1 = pk2(cr, br), h2 = pk2(cr - lo16(h1), br - hi16(h1));
        const int t5 = tid - 256, gu = t5 >= 96, nn = t5 - 96 * gu;
        if (t5 < 192)
          st128(L_T2 + (nn >> 5) * 1024 + gu * 512 + (nn & 31) * 16,
                u32x4{__builtin_amdgcn_perm(h1, h0, 0x05040100u), __builtin_amdgcn_perm(h0, h2, 0x07060100u), __builtin_amdgcn_perm(h2, h1, 0x07060302u), 0u});
      }
      if (g == 21 || g == 41) xwrite(pp - 1);
      if (g == 22 || g == 42) corr(pp - 1);
      if (g == 24 || g == 25) xadd(0, g - 24);
      if (g >= 28 && g <= 37 && (g - 28) % 3 == 0) epi(0, (g - 28) / 3, 2);
      if (g >= 26 && g < 37 && (g - 26) % 3 < 2) epi(0, (g - 26) / 3, (g - 26) % 3);
      if (g == 44 || g == 45) xadd(1, g - 44);
      if (g >= 48 && g <= 57 && (g - 48) % 3 == 0) epi(1, (g - 48) / 3, 2);
      if (g >= 46 && g < 57 && (g - 46) % 3 < 2) epi(1, (g - 46) / 3, (g - 46) % 3);
      SGB(0x100, 6, 0); SGB(0x008, 1, 0); SGB(0x002, 4, 0); SGB(0x008, 1, 0);
      SB();
      if constexpr (g < 3) sfor<6 + 4 * g, 10 + 4 * g>(iss);
      if constexpr (g == 3) sfor<18, 20>(iss);
      if constexpr (g + 1 < 20) nextA(IC<g + 1>{});
      if constexpr (g == 4 || g == 5) {
        PIN(stv[2 * (g - 4)]); PIN(stv[2 * (g - 4) + 1]);
        ensure<1024 * (g - 4)>(stv[2 * (g - 4)], sb, so); ensure<1024 * (g - 4) + 512>(stv[2 * (g - 4) + 1], sb, so);
      }
      if (g == 40) ostore(0);
      if constexpr (g >= 40 && g < 40 + NRS) gld_sc1<((g - 40) & 3) * 1024>(a[g - 40], rS.src + ((g - 40) >> 2) * 4096, l16);
      if constexpr (g >= 50 && g < 58) rD.template go<g - 50>(l16);
      if (g == 57) ostore(1);
      if constexpr ((g >= 19 && g < 21) || (g >= 22 && g < 26) || (g >= 32 && g < 38)) {
        if (old4) {
          if constexpr (g < 21) { rC.template go<2 * (g - 19)>(l16); rC.template go<2 * (g - 19) + 1>(l16); }
          else if constexpr (g < 26) { rA.template go<2 * (g - 22)>(l16); rA.template go<2 * (g - 22) + 1>(l16); }
          else { rB.template go<2 * (g - 32)>(l16); rB.template go<2 * (g - 32) + 1>(l16); }
        }
      }
      SB();
    });
    { const u32 lo_ = 65536 + tt * 4096, hi_ = (tt < 2 ? 106496 : 131072) + tt * 4096; sw = (kh ? hi_ : lo_) + l16; sr = (kh ? lo_ : hi_) + l16; }
    xwrite(2);
    BAR();
    xread(); xadd(2, 0); xadd(2, 1);
#pragma unroll
    for (int o = 0; o < 4; ++o)
#pragma unroll
      for (int ph = 0; ph < 3; ++ph) epi(2, o, ph);
    ostore(2);
    WAITV(8);
    BAR();
#pragma unroll
    for (int m = 0; m < NRS; ++m) { PIN(a[m]); st128(rS.lds + m * 1024 + l16, a[m]); }
    sfor<NRS, 12>([&](auto m_) LAM { rS.template go<decltype(m_)::v>(l16); });
  }
  {
    PHASE();
    const bool old4 = w < 4;
    const char* wdb = ws + WS_WDF + (size_t)n8 * WDB;
    const int tt64 = mt * 4 + 2 * th + (tt >> 1);
    const char* ab = ws + WS_ACT + (size_t)(tt64 * 96 + 48 * kh) * 1024;
    auto iss = [&](auto s_) LAM { constexpr int s = decltype(s_)::v; gld_sc1<(s & 1) * 2048>(a[s], ab + (s >> 1) * 4096, avoff); };
#pragma unroll
    for (int m = 0; m < 5; ++m) c5[m] = f32x4{0.f, 0.f, 0.f, 0.f};
    const Run<50, 4, true> rE(wdb + 153600, 0, w & 3);
    const Run<40, 4, true> rF(wdb + 204800, 51200, w & 3);
    const u32 ew = 92160 + (tt * 5) * 1024 + l16;
    u32 oo = (u32)(128 * mt + 64 * th + 16 * tt + c16) * 1280 + (80 * n8 + 48 * kh + 4 * g4) * 2;
    u32 mo = L_MODS + 640 + kh * 192 + g4 * 16;
    PIN(oo); PIN(mo);
    gate(ab, ((c16 >> 1) * 6 + 4 + (c16 & 1)) * 1024 + (tt & 1) * 256);
    sfor<0, 6>(iss);
    u32 wb2[5], wb3[5];
#pragma unroll
    for (int m = 0; m < 5; ++m) { wb2[m] = wbase[m] + 61440; wb3[m] = wbase[m] + 122880; PIN(wb2[m]); PIN(wb3[m]); }
    auto nextA = [&](auto s_) LAM {
      constexpr int s = decltype(s_)::v;
      if constexpr (dn_wait(s, true) == dn_wait(s, false)) WAITV(dn_wait(s, true)); else { if (old4) WAITV(dn_wait(s, true)); else WAITV(dn_wait(s, false)); }
      PIN(a[s]);
      ensure<(s & 1) * 2048>(a[s], ab + (s >> 1) * 4096, avoff);
    };
    BAR();
    u32x4 wf[3][5];
#pragma unroll
    for (int u = 0; u < 2; ++u)
#pragma unroll
      for (int m = 0; m < 5; ++m) wf[u][m] = lds128(wbase[m] + wd_slot(u));
    nextA(IC<0>{});
    sfor<0, 24>([&](auto s_) LAM {
      constexpr int s = decltype(s_)::v;
      if (s == 5 || s == 9 || s == 13 || s == 18) BAR();
      if constexpr (s + 2 < 24) {
        constexpr u32 so = wd_slot(s + 2);
#pragma unroll
        for (int m = 0; m < 5; ++m) wf[(s + 2) % 3][m] = lds128((so < 61440 ? wbase[m] : so < 122880 ? wb2[m] : wb3[m]) + so % 61440);
      }
#pragma unroll
      for (int m = 0; m < 5; ++m) c5[m] = MFMA16(wf[s % 3][m], a[s], c5[m]);
      SB();
      if constexpr (s >= 5 && s < 14) {
        if (old4) {
          if constexpr (s < 9) { sfor<0, 4>([&](auto u_) LAM { rE.template go<4 * (s - 5) + decltype(u_)::v>(l16); }); }
          else { rF.template go<2 * (s - 9)>(l16); rF.template go<2 * (s - 9) + 1>(l16); }
        }
      }
      sfor<dn_cumA(s), dn_cumA(s + 1)>(iss);
      if constexpr (s + 1 < 24) nextA(IC<s + 1>{});
      SB();
    });
    stf4(ew + ((3 + 3 * kh) % 5) * 1024, c5[3]);
    stf4(ew + ((4 + 3 * kh) % 5) * 1024, c5[4]);
    if (kh) stf4(ew, c5[2]);
    BAR();
#pragma unroll
    for (int m = 0; m < 3; ++m)
      if (m < 2 || kh == 0) {
        const f32x4 o = ldsf4(ew + kh * 3072 + m * 1024), gb = ldsf4(mo + m * 64);
        const u32 y01 = pk2(c5[m][0] + o[0], c5[m][1] + o[1]), y23 = pk2(c5[m][2] + o[2], c5[m][3] + o[3]);
        const u32 t01 = pk2(gb[0] * lo16(y01), gb[1] * hi16(y01)), t23 = pk2(gb[2] * lo16(y23), gb[3] * hi16(y23));
        const u32x2 r = {pk2(lo16(x1k[m].x) + lo16(t01), hi16(x1k[m].x) + hi16(t01)), pk2(lo16(x1k[m].y) + lo16(t23), hi16(x1k[m].y) + hi16(t23))};
        *(GAS u32x2*)((GAS char*)p.out + oo + m * 32) = r;
      }
  }
}
}

extern "C" void kernel_launch(void* const* d_in, const int* in_sizes, int n_in, void* d_out, int out_size, void* d_ws, size_t ws_size,
                              hipStream_t stream) {
  (void)in_sizes; (void)n_in; (void)out_size; (void)ws_size;
  (void)hipFuncSetAttribute((const void*)k2, hipFuncAttributeMaxDynamicSharedMemorySize, LDS_BYTES);
  P1 q;
  q.x = (const u16*)d_in[0]; q.wqkv = (const u16*)d_in[1]; q.wo = (const u16*)d_in[2];
  q.cvec = (const u16*)d_in[7]; q.wmod = (const u16*)d_in[8]; q.ws = (char*)d_ws;
  k1<<<K1_BLOCKS, 256, 0, stream>>>(q);
  P2 p;
  p.x = (const u16*)d_in[0]; p.wg = (const u16*)d_in[4]; p.wu = (const u16*)d_in[5]; p.wd = (const u16*)d_in[6];
  p.out = (u16*)d_out; p.ws = (char*)d_ws;
  k2<<<256, 512, LDS_BYTES, stream>>>(p);
}
```

```cpp
#include <hip/hip_runtime.h>
#include <hip/hip_bf16.h>
#include <stdint.h>

#define GAS __attribute__((address_space(1)))
#define LAS __attribute__((address_space(3)))
#if defined(__HIP_DEVICE_COMPILE__)
#define NOPK __attribute__((target("no-packed-fp32-ops")))
#else
#define NOPK
#endif
#define DI __device__ __forceinline__ NOPK
#define LAM __attribute__((always_inline)) NOPK
typedef unsigned int u32;
typedef unsigned short u16;
typedef u32 u32x4 __attribute__((ext_vector_type(4)));
typedef u32 u32x2 __attribute__((ext_vector_type(2)));
typedef float f32x2 __attribute__((ext_vector_type(2)));
typedef float f32x4 __attribute__((ext_vector_type(4)));
typedef float f32x16 __attribute__((ext_vector_type(16)));
typedef __bf16 bf16x2 __attribute__((ext_vector_type(2)));
typedef __bf16 bf16x4 __attribute__((ext_vector_type(4)));
typedef __bf16 bf16x8 __attribute__((ext_vector_type(8)));
typedef GAS u32 gu32;
typedef _Float16 f16x8 __attribute__((ext_vector_type(8)));
typedef _Float16 f16x2 __attribute__((ext_vector_type(2)));

#define STAMPS 0

namespace {
constexpr int S = 2048, D = 640, F = 1536;
constexpr u32 WS_KVF = 0;
constexpr u32 WS_CNT = 32768;
constexpr u32 WS_MODP = 65536;
constexpr u32 WS_CSP = WS_MODP + 61440;
constexpr u32 WS_STAT = WS_CSP + 16 * 16 * 1536;
constexpr u32 WS_N1 = WS_STAT + 64 * 8 * 32 * 16;
constexpr u32 WS_OAF = WS_N1 + 64 * 40 * 1024;
constexpr u32 WS_X1 = WS_OAF + 64 * 40 * 1024;
constexpr u32 WS_ACT = WS_X1 + 64 * 40 * 1024;
constexpr u32 KVB = 24576;
constexpr u32 WS_KVX = WS_ACT + 64 * 96 * 1024;
constexpr u32 WQB = 153600;
constexpr u32 WS_WQF = WS_KVX + 256 * KVB;
constexpr u32 WOB = 102400, WGB = 245760, WDB = 245760;
constexpr u32 WS_WOF = WS_WQF + 16 * WQB;
constexpr u32 WS_WGF = WS_WOF + 8 * WOB;
constexpr u32 WS_WDF = WS_WGF + 16 * WGB;
constexpr u32 WS_DBG = WS_WDF + 8 * WDB;
constexpr u32 WS_DBG1 = WS_DBG + 256 * 32 * 8;
constexpr u32 WS_END = WS_DBG1 + 1024 * 16;
constexpr u32 L_MISC = 153600;
constexpr u32 L_GS = L_MISC;
constexpr u32 L_MODS = L_MISC + 6144;
constexpr u32 L_T2 = L_MISC + 7168;
constexpr u32 L_FLAG = L_MISC + 9728;
constexpr u32 LDS_BYTES = 163840;
constexpr u32 L_K = 0, L_V = 30720;
constexpr float QSCALE = 0.22360679774997896f * 1.4426950408889634f;
constexpr int K1_BLOCKS = 488;
#define ATT_LIMIT 1.0e18f
constexpr int K2_LINES = 364;
constexpr u32 SPIN_CAP = 1u << 20;

DI float u2f(u32 v) { return __builtin_bit_cast(float, v); }
DI u32 f2u(float v) { return __builtin_bit_cast(u32, v); }
DI int tidx() { return (int)__builtin_amdgcn_workitem_id_x(); }
DI int bidx() { return (int)__builtin_amdgcn_workgroup_id_x(); }
DI float bf2f(u32 v) { return u2f(v << 16); }
DI float lo16(u32 v) { return u2f(v << 16); }
DI float hi16(u32 v) { return u2f(v & 0xffff0000u); }
DI u32 pk2(float a, float b) { f32x2 v = {a, b}; return __builtin_bit_cast(u32, __builtin_convertvector(v, bf16x2)); }
DI u32 pkh2(float a, float b) { f32x2 v = {a, b}; return __builtin_bit_cast(u32, __builtin_convertvector(v, f16x2)); }
DI float hlo(u32 v) { return (float)__builtin_bit_cast(f16x2, v).x; }
DI float hhi(u32 v) { return (float)__builtin_bit_cast(f16x2, v).y; }
DI float rbf(float a) { return lo16(pk2(a, a)); }
DI float fma1(float a, float b, float c) { return __builtin_fmaf(a, b, c); }
DI float mul1(float a, float b) { return a * b; }
DI float add1(float a, float b) { return a + b; }
DI float max3(float a, float b, float c) { return __builtin_fmaxf(__builtin_fmaxf(a, b), c); }
DI float xor32f(float v) {
  auto r = __builtin_amdgcn_permlane32_swap(f2u(v), f2u(v), false, false);
  return u2f((tidx() & 32) ? r[0] : r[1]);
}
DI float sum32(float v) { auto r = __builtin_amdgcn_permlane32_swap(f2u(v), f2u(v), false, false); return u2f(r[0]) + u2f(r[1]); }
DI float sum16(float v) { auto r = __builtin_amdgcn_permlane16_swap(f2u(v), f2u(v), false, false); return u2f(r[0]) + u2f(r[1]); }
DI float max32(float v) { auto r = __builtin_amdgcn_permlane32_swap(f2u(v), f2u(v), false, false); return __builtin_fmaxf(u2f(r[0]), u2f(r[1])); }
template <int CTRL> DI float dppf(float v) { return u2f(__builtin_amdgcn_update_dpp(0u, f2u(v), CTRL, 0xf, 0xf, true)); }

#define MFMA32(a, b, c) __builtin_amdgcn_mfma_f32_32x32x16_bf16(__builtin_bit_cast(bf16x8, a), __builtin_bit_cast(bf16x8, b), c, 0, 0, 0)
#define MFMA32H(a, b, c) __builtin_amdgcn_mfma_f32_32x32x16_f16(__builtin_bit_cast(f16x8, a), __builtin_bit_cast(f16x8, b), c, 0, 0, 0)
#define MFMA16(a, b, c) __builtin_amdgcn_mfma_f32_16x16x32_bf16(__builtin_bit_cast(bf16x8, a), __builtin_bit_cast(bf16x8, b), c, 0, 0, 0)
#define SGB(m, n, id) __builtin_amdgcn_sched_group_barrier(m, n, id)
#define SB() __builtin_amdgcn_sched_barrier(0)
template <bool B> struct BoolC { static constexpr bool v = B; };
template <int I> struct IC { static constexpr int v = I; };
template <int I, int N, class Fn> DI void sfor(Fn&& f) { if constexpr (I < N) { f(IC<I>{}); sfor<I + 1, N>(f); } }
#define PIN(x) asm volatile("" : "+v"(x))
#define WAITV(n) asm volatile("s_waitcnt vmcnt(%0)" ::"i"(n))
#define WAITVM(n) asm volatile("s_waitcnt vmcnt(%0)" ::"i"(n) : "memory")
#define BAR() asm volatile("s_waitcnt lgkmcnt(0)\n\ts_barrier" ::: "memory")
#define RLX_AGENT __ATOMIC_RELAXED, __HIP_MEMORY_SCOPE_AGENT

DI u32x4 lds128(u32 a) { return *(const LAS u32x4*)(size_t)a; }
DI f32x4 ldsf4(u32 a) { return *(const LAS f32x4*)(size_t)a; }
DI u32x2 lds64(u32 a) { return *(const LAS u32x2*)(size_t)a; }
DI float ldsf(u32 a) { return *(const LAS float*)(size_t)a; }
DI void st128(u32 a, u32x4 v) { *(LAS u32x4*)(size_t)a = v; }
DI void stf4(u32 a, f32x4 v) { *(LAS f32x4*)(size_t)a = v; }
DI void st64(u32 a, u32x2 v) { *(LAS u32x2*)(size_t)a = v; }
DI u32x2 ldstr(u32 a) { return __builtin_bit_cast(u32x2, __builtin_amdgcn_ds_read_tr16_b64_v4bf16((LAS bf16x4*)(size_t)a)); }
DI u32x4 ldstr2(u32 a, u32 b) { u32x2 lo = ldstr(a), hi = ldstr(b); return u32x4{lo.x, lo.y, hi.x, hi.y}; }

template <int IMM, bool SC1> DI void dmai(const char* sbase, u32 voff, u32 lds) {
  if constexpr (SC1) asm volatile("s_mov_b32 m0, %2\n\ts_nop 0\n\tglobal_load_lds_dwordx4 %0, %1 offset:%3 sc1" ::"v"(voff), "s"(sbase), "s"(lds), "i"(IMM));
  else asm volatile("s_mov_b32 m0, %2\n\ts_nop 0\n\tglobal_load_lds_dwordx4 %0, %1 offset:%3" ::"v"(voff), "s"(sbase), "s"(lds), "i"(IMM));
}
DI void dma(const char* sbase, u32 voff, u32 lds) { dmai<0, false>(sbase, voff, lds); }
DI void dma_nt(const char* sbase, u32 voff, u32 lds) { asm volatile("s_mov_b32 m0, %2\n\ts_nop 0\n\tglobal_load_lds_dwordx4 %0, %1 nt" ::"v"(voff), "s"(sbase), "s"(lds)); }
DI void gld32(u32& r, const char* sbase, u32 voff) { asm volatile("global_load_dword %0, %1, %2" : "=v"(r) : "v"(voff), "s"(sbase)); }
DI void gld32_sc1(u32& r, const char* sbase, u32 voff) { asm volatile("global_load_dword %0, %1, %2 sc1" : "=v"(r) : "v"(voff), "s"(sbase)); }
template <int IMM> DI void gld_sc1(u32x4& r, const char* sbase, u32 voff) {
  asm volatile("global_load_dwordx4 %0, %1, %2 offset:%3 sc1" : "=v"(r) : "v"(voff), "s"(sbase), "i"(IMM));
}
template <int IMM> DI void gld(u32x4& r, const char* sbase, u32 voff) {
  asm volatile("global_load_dwordx4 %0, %1, %2 offset:%3" : "=v"(r) : "v"(voff), "s"(sbase), "i"(IMM));
}
DI void gld64(u32x2& r, const char* sbase, u32 voff) {
  asm volatile("global_load_dwordx2 %0, %1, %2" : "=v"(r) : "v"(voff), "s"(sbase));
}
DI void poll_ge(const char* ws, u32 off, u32 want) {
  gu32* p = (gu32*)(ws + off);
  for (u32 spins = 0; spins < SPIN_CAP; ++spins) {
    if ((u32)__builtin_amdgcn_readfirstlane(__hip_atomic_load(p, RLX_AGENT)) >= want) break;
    __builtin_amdgcn_s_sleep(4);
  }
  __builtin_amdgcn_fence(__ATOMIC_ACQUIRE, "wavefront");
}
DI void signal(const char* ws, u32 off) { __hip_atomic_fetch_add((gu32*)(ws + off), 1u, RLX_AGENT); }

struct P1 {
  const u16 *x, *wqkv, *wo, *cvec, *wmod;
  char* ws;
};
#define K1AUX 0
__global__ void __launch_bounds__(256) NOPK k1(P1 p) {
  __shared__ float red[32 * 64 + 4 * 64 + 320];
  const int b = bidx(), t = tidx();
  char* ws = p.ws;
  const __amdgpu_buffer_rsrc_t rs = __builtin_amdgcn_make_buffer_rsrc(p.ws, 0, WS_END, 0x00020000);
  if (b >= 408 && b < 424) {
    unsigned long long pc;
    asm volatile("s_getpc_b64 s[40:41]\n\ts_add_u32 s40, s40, _ZN12_GLOBAL__N_12k2ENS_2P2E@rel32@lo+4\n\ts_addc_u32 s41, s41, _ZN12_GLOBAL__N_12k2ENS_2P2E@rel32@hi+12\n\ts_mov_b64 %0, s[40:41]"
                 : "=s"(pc) : : "scc", "s40", "s41");
    const int o = ((b - 408) >> 3) * 256 + t;
    if (o < K2_LINES) { u32 v_ = *(const GAS u32*)(pc + (size_t)o * 128); asm volatile("" ::"v"(v_)); }
  }
  if (b < 120) {
    const int ch = t & 7, r = t >> 3, col0 = 64 * (b >> 1), row0 = 320 * (b & 1);
    const u32x4 cv = *(const u32x4*)(p.cvec + row0 + 8 * (t < 40 ? t : 39));
    u32x4 wv[10];
#pragma unroll
    for (int i = 0; i < 10; ++i) wv[i] = __builtin_nontemporal_load((const u32x4*)(p.wmod + (size_t)(row0 + r + 32 * i) * 3840 + col0 + 8 * ch));
    if (t < 40) {
#pragma unroll
      for (int e = 0; e < 8; ++e) {
        const float c = (e & 1) ? hi16(cv[e >> 1]) : lo16(cv[e >> 1]);
        red[2304 + 8 * t + e] = rbf(c * __builtin_amdgcn_rcpf(1.f + __builtin_amdgcn_exp2f(-1.4426950408889634f * c)));
      }
    }
    BAR();
    float acc[8] = {0, 0, 0, 0, 0, 0, 0, 0};
#pragma unroll
    for (int i = 0; i < 10; ++i) {
      const float s = red[2304 + r + 32 * i];
#pragma unroll
      for (int e = 0; e < 4; ++e) { acc[2 * e] = fma1(s, lo16(wv[i][e]), acc[2 * e]); acc[2 * e + 1] = fma1(s, hi16(wv[i][e]), acc[2 * e + 1]); }
    }
    *(f32x4*)&red[r * 64 + 8 * ch] = f32x4{acc[0], acc[1], acc[2], acc[3]};
    *(f32x4*)&red[r * 64 + 8 * ch + 4] = f32x4{acc[4], acc[5], acc[6], acc[7]};
    BAR();
    { const int c = t & 63, part = t >> 6; float s = 0;
#pragma unroll
      for (int i = 0; i < 8; ++i) s += red[(part * 8 + i) * 64 + c];
      red[2048 + part * 64 + c] = s; }
    BAR();
    if (t < 64) {
      const float s = (red[2048 + t] + red[2112 + t]) + (red[2176 + t] + red[2240 + t]);
      __builtin_amdgcn_raw_buffer_store_b32(f2u(s), rs, WS_MODP + ((b & 1) * 3840 + col0 + t) * 4, 0, K1AUX);
    }
    if (b == 0) {
      *(u32*)(ws + WS_KVF + t * 128) = 0u;
      if (t < 72) *(u32*)(ws + WS_CNT + t * 256) = 0u;
    }
  } else if (b < 248) {
    const int hb = b - 120, c = t & 15, tok = 16 * (hb & 1) + (t >> 4);
    const u16* row = p.x + (size_t)(32 * (hb >> 1) + tok) * D;
    u32x4 v[5];
#pragma unroll
    for (int i = 0; i < 5; ++i) v[i] = *(const u32x4*)(row + 8 * (c + 16 * i));
    float s = 0;
#pragma unroll
    for (int i = 0; i < 5; ++i)
#pragma unroll
      for (int e = 0; e < 4; ++e) s += lo16(v[i][e]) + hi16(v[i][e]);
    s += dppf<0xB1>(s); s += dppf<0x4E>(s); s += dppf<0x141>(s); s += dppf<0x140>(s);
    const float mean = s * (1.f / 640.f);
    float q = 0;
#pragma unroll
    for (int i = 0; i < 5; ++i)
#pragma unroll
      for (int e = 0; e < 4; ++e) { const float a = lo16(v[i][e]) - mean, bb = hi16(v[i][e]) - mean; q = fma1(a, a, q); q = fma1(bb, bb, q); }
    q += dppf<0xB1>(q); q += dppf<0x4E>(q); q += dppf<0x141>(q); q += dppf<0x140>(q);
    const float mb = rbf(mean), den = rbf(__builtin_amdgcn_sqrtf(rbf(rbf(q * (1.f / 640.f)) + 1e-5f))), rr = __builtin_amdgcn_rcpf(den);
    const u32 dst = WS_N1 + (u32)(hb >> 1) * 40960 + tok * 16;
#pragma unroll
    for (int i = 0; i < 5; ++i) {
      u32x4 o;
#pragma unroll
      for (int e = 0; e < 4; ++e) {
        const u32 d = pk2(lo16(v[i][e]) - mb, hi16(v[i][e]) - mb);
        const u32 n = pk2(lo16(d) * rr, hi16(d) * rr);
        o[e] = pkh2(lo16(n), hi16(n));
      }
      __builtin_amdgcn_raw_buffer_store_b128(o, rs, dst + (c + 16 * i) * 512, 0, K1AUX);
    }
  } else {
    const u16* src; int stride; u32 off0, off1; bool act = true;
    if (b < 408) {
      const int idx = b - 248, ks = idx >> 2, jq = idx & 3, tc = t < 240 ? t : 239;
      act = t < 240;
      const int seg = tc / 80, cp = tc - 80 * seg;
      src = p.wqkv + (size_t)(16 * ks) * 1920 + 640 * seg + 160 * jq + 2 * cp; stride = 1920;
      const int jj = (2 * cp) / 40, within = 2 * cp - 40 * jj, j = 4 * jq + jj;
      const int n = (seg == 1 ? 0 : seg == 2 ? 40 : 80) + within, nb = n >> 5, r = n & 31;
      off0 = WS_WQF + j * WQB + (nb < 3 ? ((nb * 40 + ks) * 64 + r) * 16 : 122880 + ks * 768 + r * 16);
      off1 = off0 + (nb < 3 ? 512 : 384);
    } else {
      const int idx = b - 408, k16 = idx >> 1, tc = t < 160 ? t : 159, n = (idx & 1) * 320 + 2 * tc;
      act = t < 160;
      src = p.wo + (size_t)(16 * k16) * 640 + n; stride = 640;
      const int kh = k16 >= 20, kk = k16 - 20 * kh, st = kk >> 1, H = kk & 1;
      const int ntg = n >> 4, n8 = ntg / 5, nt = ntg - 5 * n8;
      off0 = WS_WOF + n8 * WOB + (st * 10 + kh * 5 + nt) * 1024 + (2 * H * 16 + (n & 15)) * 16;
      off1 = off0 + 256;
    }
    u32 v[16];
#pragma unroll
    for (int i = 0; i < 16; ++i) v[i] = __builtin_nontemporal_load((const u32*)(src + (size_t)i * stride));
    if (b < 408) {
#pragma unroll
      for (int i = 0; i < 16; ++i) v[i] = pkh2(lo16(v[i]) * 64.f, hi16(v[i]) * 64.f);
    }
    if (act) {
#pragma unroll
      for (int h = 0; h < 2; ++h) {
        u32x4 a, bq;
#pragma unroll
        for (int e = 0; e < 4; ++e) {
          a[e] = __builtin_amdgcn_perm(v[8 * h + 2 * e + 1], v[8 * h + 2 * e], 0x05040100u);
          bq[e] = __builtin_amdgcn_perm(v[8 * h + 2 * e + 1], v[8 * h + 2 * e], 0x07060302u);
        }
        __builtin_amdgcn_raw_buffer_store_b128(a, rs, h ? off1 : off0, 0, K1AUX);
        __builtin_amdgcn_raw_buffer_store_b128(bq, rs, (h ? off1 : off0) + 16, 0, K1AUX);
      }
    }
  }
}

struct P2 {
  const u16 *x, *wg, *wu, *wd;
  u16* out;
  char* ws;
};
#define MAP 0
constexpr u32 C_O = WS_CNT, C_RG = WS_CNT + 48 * 256, C_RD = WS_CNT + 64 * 256;
#define OPQ(x) asm volatile("" : "+s"(x))

DI constexpr int gu_unit(int p, int kh, int i) {
  return p == 0 ? 20 * kh + i
       : p == 1 ? (i < 16 ? 40 + 16 * kh + i : 8 + 20 * kh + (i - 16))
                : (i < 8 ? 12 + 20 * kh + i : 40 + 16 * kh + (i - 8));
}
DI constexpr u32 wo_slot(int s) { return s < 7 ? (72 + 10 * s) * 1024 : (s - 7) * 10240; }
DI constexpr u32 wd_slot(int s) { return (s < 15 ? s : s - 15) * 10240; }
template <int N, int NW, bool SC1> struct Run {
  static constexpr int C = (N + NW - 1) / NW;
  const char* src; u32 lds;
  DI Run(const char* s, u32 l, int x) { int o = x * C; o = (o < N - C ? o : N - C) * 1024; src = s + o; lds = l + o; }
  template <int M> DI void go(u32 l16) const { if constexpr (M < C) dmai<(M & 3) * 1024, SC1>(src + (M >> 2) * 4096, l16, lds + (M >> 2) * 4096); }
  DI void all(u32 l16) const { sfor<0, C>([&](auto m_) LAM { go<decltype(m_)::v>(l16); }); }
};
template <int P, int I0, int I1, int NW> DI auto gu_run(const char* blob, int x) {
  const int k = x >= NW / 2;
  return Run<2 * (I1 - I0), NW / 2, true>(blob + ((P * 2 + k) * 20 + I0) * 2048, (k ? gu_unit(P, 1, I0) : gu_unit(P, 0, I0)) * 2048, x - k * (NW / 2));
}
DI u32 pkfma16(u32 x, u32 y, u32 z) { return __builtin_bit_cast(u32, __builtin_elementwise_fma(__builtin_bit_cast(f16x2, x), __builtin_bit_cast(f16x2, y), __builtin_bit_cast(f16x2, z))); }
DI void gld4_if(int cond, u32x4& r0, u32x4& r1, u32x4& r2, u32x4& r3, const char* sbase, u32 voff) {
  asm volatile("s_cmp_eq_u32 %6, 0\n\ts_cbranch_scc1 .Lskip%=\n\tglobal_load_dwordx4 %0, %4, %5\n\tglobal_load_dwordx4 %1, %4, %5 offset:1024\n\t"
               "global_load_dwordx4 %2, %4, %5 offset:2048\n\tglobal_load_dwordx4 %3, %4, %5 offset:3072\n.Lskip%=:"
               : "+v"(r0), "+v"(r1), "+v"(r2), "+v"(r3) : "v"(voff), "s"(sbase), "s"(cond) : "scc");
}
#define NRS 12
DI constexpr int dn_nA(int s) { return s < 3 ? 3 : s == 8 || s == 10 || s == 13 ? 3 : 0; }
DI constexpr int dn_nD(int s) { return s >= 5 && s < 8 ? 4 : s == 8 ? 1 : s >= 9 && s < 14 ? 2 : 0; }
DI constexpr int dn_cumA(int s) { int c = 6; for (int u = 0; u < s; ++u) c += dn_nA(u); return c; }
DI constexpr int dn_young(int s, bool old, int a_idx, int d_step) {
  int pos = 0, tgt = -1, na = 6;
  if (a_idx >= 0 && a_idx < 6) tgt = a_idx;
  pos = 6;
  for (int u = 0; u < s; ++u) {
    if (old) { pos += dn_nD(u); if (u == d_step) tgt = pos - 1; }
    for (int q = 0; q < dn_nA(u); ++q) { if (na == a_idx) tgt = pos; ++na; ++pos; }
  }
  return tgt < 0 ? 63 : pos - 1 - tgt;
}
DI constexpr int dn_wait(int s, bool old) {
  const int x = dn_young(s, old, s, -1), y = s == 13 ? dn_young(s, old, -1, 8) : s == 18 ? dn_young(s, old, -1, 13) : 63;
  return x < y ? x : y;
}
#define OWARM 0
#define RPOS 0
#define SENT 0
DI bool has_sent(const u32x4& v) {
  const u32 m = __builtin_elementwise_max(__builtin_elementwise_max(v.x, v.y), __builtin_elementwise_max(v.z, v.w));
  return __builtin_amdgcn_ballot_w64(m == 0xffffffffu) != 0ull;
}
template <int IMM> DI void ensure(u32x4& v, const char* sbase, u32 voff) {
  if (SENT && __builtin_expect(has_sent(v), 0)) {
    for (u32 spins = 0; spins < SPIN_CAP; ++spins) {
      gld_sc1<IMM>(v, sbase, voff); WAITV(0); PIN(v);
      if (!has_sent(v)) break;
    }
  }
}
DI void gate(const char* sbase, u32 voff) {
  for (u32 spins = 0; spins < SPIN_CAP; ++spins) {
    u32 v;
    gld32_sc1(v, sbase, voff); WAITV(0); PIN(v);
    if (__builtin_amdgcn_ballot_w64(v == 0xffffffffu) == 0ull) break;
    __builtin_amdgcn_s_sleep(3);
  }
}

__global__ void __launch_bounds__(512, 2) NOPK k2(P2 p) {
  const int tid = tidx(), lane = tid & 63;
  int w = __builtin_amdgcn_readfirstlane(tid >> 6);
  const int b = bidx();
  int mt = MAP == 0 ? 4 * ((b & 7) >> 1) + (b >> 6) : (b & 7) * 2 + (b >> 7), j = MAP == 0 ? 8 * (b & 1) + ((b >> 3) & 7) : (b >> 3) & 15;
  const int c32 = lane & 31, h = lane >> 5;
  const char* ws = p.ws;
  const __amdgpu_buffer_rsrc_t rs = __builtin_amdgcn_make_buffer_rsrc(p.ws, 0, WS_END, 0x00020000);
  const u32 l16 = lane * 16;
#define PHASE() OPQ(w); OPQ(mt); OPQ(j); OPQ(ws); const int tt = w & 3, kh = w >> 2, th = j & 1, n8 = j >> 1; (void)tt; (void)kh; (void)th; (void)n8
#define FINE 0
#define CSEL 0
#define FST(k, i) do {} while (0)
#define STAMP(i) do {} while (0)
#define CST(k, i) do {} while (0)
#define PRIO 0
  if (PRIO == 1 && w >= 4) __builtin_amdgcn_s_setprio(1);
  STAMP(0); CST(3, 18);
  u32x4 a[24];
  u32x4 st[18];
  const int rsw = c32 ^ (16 * (w >> 2));
  const u32 frow = (h * 32 + rsw) * 16;
  u32 imga[8];
  {
    PHASE();
    const char* wq = ws + WS_WQF + (size_t)j * WQB;
    const char* wqw = wq + (kh * 20 + tt) * 1024;
    const u32 wql = (kh * 20 + tt) * 1024;
    const char* n1 = ws + WS_N1 + (size_t)((mt * 4 + tt) * 40 + kh * 20) * 1024;
    auto issA = [&](auto k_) LAM { constexpr int k = decltype(k_)::v; gld<(k & 3) * 1024>(a[k], n1 + (k >> 2) * 4096, l16); };
    auto issW = [&](int s, int nb) LAM { dma(wqw + nb * 40960 + s * 4096, l16, wql + nb * 40960 + s * 4096); };
    const int rk16 = w < 5 ? 5 * (mt >> 1) + w : (mt * 2 + th) * 3 + (w - 5);
    const char* rsrc = w < 5 ? (const char*)((mt & 1) ? p.wu : p.wg) + (size_t)rk16 * 49152 + 192 * j : (const char*)p.wd + (size_t)rk16 * 20480 + 160 * n8;
    const u32 rimg = 122880 + w * 3072;
    auto issR = [&]() LAM {
#pragma unroll
      for (int m = 0; m < 3; ++m) {
        const u32 x = 64 * m + lane, i5 = x < 160 ? x : 159, r12 = (x * 171) >> 11, r10 = (i5 * 205) >> 11;
        dma_nt(rsrc, w < 5 ? r12 * 2880 + x * 16 : r10 * 1120 + i5 * 16, rimg + m * 1024);
      }
    };
    if (RPOS == 1) issR();
    u32x4 mp[2];
    u32 mdst; bool plus1;
    {
      const int m1 = tid >= 160, k1 = 4 * (tid - 160 * m1);
      const int u2 = tid - 320, m2 = (u2 * 205) >> 12, e2 = u2 - 20 * m2;
      const int col2 = (m2 == 0 ? 1280 : m2 == 2 ? 3200 : m2 == 4 ? 1920 : 2560) + 80 * (m2 < 3 ? n8 : (mt >> 1)) + 4 * (m2 < 5 ? e2 : 0);
      const int col = tid < 320 ? 640 * m1 + k1 : col2;
#pragma unroll
      for (int q = 0; q < 2; ++q) gld<0>(mp[q], ws + WS_MODP + q * 15360, (u32)col * 4);
      sfor<0, 4>(issA);
      issW(0, 0); issW(0, 1); issW(0, 2);
      if (RPOS == 2) issR();
      mdst = tid < 320 ? L_GS + (k1 >> 3) * 32 + (m1 ? 0 : 16) + (k1 & 4) * 2 : L_MODS + u2 * 16;
      plus1 = tid < 320 ? m1 != 0 : (m2 & 1) != 0;
    }
    if (tid == 0) st128(L_FLAG, u32x4{0u, 0u, 0u, 0u});
    f32x16 acc[3];
    const u32 wb = kh * 20480 + frow, gb_ = L_GS + kh * 1280 + h * 32;
    u32x4 gs[3][2], wf[2][3], ro[3];
    f32x4 csum = {0.f, 0.f, 0.f, 0.f};
    auto ldgs = [&](int k) LAM { gs[k % 3][0] = lds128(gb_ + k * 64); gs[k % 3][1] = lds128(gb_ + k * 64 + 16); };
    auto modulate = [&](int k) LAM {
      PIN(a[k]);
#pragma unroll
      for (int e = 0; e < 4; ++e)
        a[k][e] = pkfma16(a[k][e], gs[k % 3][0][e], gs[k % 3][1][e]);
    };
    FST(3, 1);
    WAITV(RPOS == 2 ? 10 : 7);
    FST(3, 2);
    f32x4 mv;
    {
#pragma unroll
      for (int q = 0; q < 2; ++q) PIN(mp[q]);
      float mxv = 0.f;
#pragma unroll
      for (int e = 0; e < 4; ++e) {
        const float s = rbf(u2f(mp[0][e]) + u2f(mp[1][e]));
        mv[e] = plus1 ? rbf(1.f + s) : s;
        mxv = __builtin_fmaxf(mxv, __builtin_fabsf(mv[e]));
      }
      if (tid < 320) st64(mdst, u32x2{pkh2(mv[0], mv[1]), pkh2(mv[2], mv[3])}); else if (tid < 420) stf4(mdst, mv);
      const bool big = __builtin_amdgcn_ballot_w64(tid < 320 && !(mxv < 2000.f)) != 0ull;
      if (lane == 0) *(LAS u32*)(size_t)(L_FLAG + 16 + w * 4) = big;
    }
    WAITV(RPOS == 2 ? 3 : 0);
    BAR();
    FST(3, 3);
    auto grpA = [&](int cond, int s) LAM { gld4_if(cond, a[4 * s], a[4 * s + 1], a[4 * s + 2], a[4 * s + 3], n1 + s * 4096, l16); };
    const int kl = kh ^ 1;
    auto grpW = [&](int s) LAM { issW(s, 0); issW(s, 1); issW(s, 2); };
    grpA(kh, 1); if (kh) { grpW(1); if (RPOS == 0) issR(); }
    bool scaled;
    {
      const u32x4 f0 = lds128(L_FLAG + 16);
      const u32 f1 = *(const LAS u32*)(size_t)(L_FLAG + 32);
      ldgs(0); ldgs(1);
      scaled = __builtin_amdgcn_readfirstlane(f0.x | f0.y | f0.z | f0.w | f1) != 0;
      if (scaled) {
        BAR();
        const float sc = 9.765625e-4f;
        if (tid < 320) st64(mdst, u32x2{pkh2(mv[0] * sc, mv[1] * sc), pkh2(mv[2] * sc, mv[3] * sc)});
        BAR();
        ldgs(0); ldgs(1);
      }
    }
    modulate(0);
    SB();
    const Run<30, 8, false> rb3(wq + 122880, 122880, w);
    sfor<0, 21>([&](auto k_) LAM {
      constexpr int k = decltype(k_)::v, s = k >> 2, i = k & 3;
      if constexpr (i == 3 && k < 16) WAITV(k == 3 && RPOS == 0 ? 13 : 10);
      if constexpr (i == 0 && k > 0 && k < 20) {
        FST(3, 3 + 3 * s); WAITV(k == 4 && RPOS == 0 ? 10 : k == 16 ? 4 : 7); FST(3, 4 + 3 * s); BAR(); FST(3, 5 + 3 * s);
        if (s == 2 && CSEL == 4) { CST(4, 18); STAMP(16); } if (s == 3 && CSEL == 4) { CST(4, 19); STAMP(17); }
      }
      if constexpr (k < 20) {
#pragma unroll
        for (int nb = 0; nb < 3; ++nb) wf[k & 1][nb] = lds128(wb + nb * 40960 + k * 1024);
      }
      if constexpr (k == 1) {
        f32x16 z;
#pragma unroll
        for (int e = 0; e < 16; ++e) z[e] = 0.f;
#pragma unroll
        for (int nb = 0; nb < 3; ++nb) acc[nb] = MFMA32H(wf[0][nb], a[0], z);
      } else if constexpr (k > 1) {
#pragma unroll
        for (int nb = 0; nb < 3; ++nb) acc[nb] = MFMA32H(wf[(k - 1) & 1][nb], a[k - 1], acc[nb]);
      }
      if constexpr (k + 1 < 20) modulate(k + 1);
      if constexpr (k + 2 < 20) ldgs(k + 2);
      if constexpr (k == 8) {
        const u32 q4 = (lane >> 2) & 3, nh = (lane >> 4) & 1;
        const u32 r0 = rimg + (w < 5 ? (8 * h + q4) * 192 + 32 * nh : (4 * nh + q4) * 160 + 32 * h) + (lane & 3) * 8, r1 = r0 + (w < 5 ? 768 : 1280);
#pragma unroll
        for (int u = 0; u < 3; ++u) ro[u] = ldstr2(r0 + 64 * u, r1 + 64 * u);
      }
      if constexpr (k == 9 || k == 10) {
        if (w < 5) {
          const u32 ga_ = L_MODS + 960 + w * 64 + h * 32;
          const f32x4 g0 = ldsf4(ga_), g1 = ldsf4(ga_ + 16), s0 = ldsf4(ga_ + 320), s1 = ldsf4(ga_ + 336);
#pragma unroll
          for (int u = (k == 9 ? 0 : 2); u < (k == 9 ? 2 : 3); ++u) {
            float c = 0.f, bi = 0.f;
#pragma unroll
            for (int e = 0; e < 4; ++e) {
              const u32 v = ro[u][e];
              const float x0 = lo16(v), x1 = hi16(v), ge = e < 2 ? g0[2 * e] : g1[2 * e - 4], go = e < 2 ? g0[2 * e + 1] : g1[2 * e - 3];
              const float se = e < 2 ? s0[2 * e] : s1[2 * e - 4], so_ = e < 2 ? s0[2 * e + 1] : s1[2 * e - 3];
              c = fma1(ge, x0, c); c = fma1(go, x1, c); bi = fma1(se, x0, bi); bi = fma1(so_, x1, bi);
            }
            auto r = __builtin_amdgcn_permlane32_swap(f2u(c), f2u(bi), false, false);
            *(LAS float*)(size_t)(w * 768 + u * 256 + (c32 >> 3) * 64 + h * 32 + (c32 & 7) * 4) = u2f(r[0]) + u2f(r[1]);
          }
        }
      }
      if constexpr (k == 17) {
        if (w == 7) {
#pragma unroll
          for (int u = 0; u < 5; ++u) csum += ldsf4(u * 768 + (lane < 48 ? l16 : 0u));
        }
      }
      if constexpr (k == 16) {
        const u32 x2 = 16 * kh + 4 * h, tb = 12288 + (32 * tt + c32) * 48 + 2 * x2;
#pragma unroll
        for (int u = 0; u < 8; ++u) {
          const u32 part = ((x2 + 32 * (u >> 1) + 8 * (u & 1)) * 205) >> 12;
          imga[u] = part * 18392 + tb + 64 * (u >> 1) + 16 * (u & 1); PIN(imga[u]);
        }
      }
      SB();
      if constexpr (k == 0) { grpA(kl, 1); if (!kh) { grpW(1); if (RPOS == 0) issR(); } }
      if constexpr (k == 1) grpA(kl, 2);
      if constexpr (k == 2) { if (!kh) grpW(2); grpA(kh, 2); }
      if constexpr (k == 3) { if (kh) grpW(2); }
      if constexpr (s == 1 || s == 2) {
        if constexpr (i == 0) grpA(kl, s + 2);
        if constexpr (i == 1) { if (!kh) grpW(s + 2); }
        if constexpr (i == 2) grpA(kh, s + 2);
        if constexpr (i == 3) { if (kh) grpW(s + 2); }
      }
      if constexpr (k == 12) { if (!kh) rb3.all(l16); }
      if constexpr (k == 14) { if (kh) rb3.all(l16); }
      if constexpr (k == 16) {
        u32 off0, du;
        if (w < 5) { const int k2 = rk16 >= 20, i2 = rk16 - 20 * k2; off0 = WS_WGF + j * WGB + (k2 * 20 + i2) * 2048 + (mt & 1) * 1024 + l16; du = 81920; }
        else { const int k2 = rk16 >= 48, kk = rk16 - 48 * k2; off0 = WS_WDF + n8 * WDB + ((kk >> 1) * 10 + k2 * 5 + h) * 1024 + (kk & 1) * 512 + c32 * 16; du = 2048; }
        asm volatile("" ::: "memory");
        __builtin_amdgcn_raw_buffer_store_b128(ro[0], rs, off0, 0, 16);
        __builtin_amdgcn_raw_buffer_store_b128(ro[1], rs, off0 + du, 0, 16);
        __builtin_amdgcn_raw_buffer_store_b128(ro[2], rs, (w < 5 || h == 0) ? off0 + 2 * du : 0xfffffff0u, 0, 16);
        asm volatile("" ::: "memory");
      }
      SB();
    });
    STAMP(1); CST(3, 19); FST(6, 1);
    WAITV(3);
    BAR();
    FST(6, 2);
    if (w == 7 && lane < 48) __builtin_amdgcn_raw_buffer_store_b128(__builtin_bit_cast(u32x4, csum), rs, WS_CSP + (u32)(j * 16 + mt) * 768 + l16, 0, 16);
    const u32 exw = (w >> 1) * 18432 + (w & 1) * 6144 + l16, exr = ((w ^ 4) >> 1) * 18432 + (w & 1) * 6144 + l16;
#pragma unroll
    for (int nb = 0; nb < 3; ++nb) {
      stf4(exw + nb * 2048, f32x4{acc[nb][8], acc[nb][9], acc[nb][10], acc[nb][11]});
      stf4(exw + nb * 2048 + 1024, f32x4{acc[nb][12], acc[nb][13], acc[nb][14], acc[nb][15]});
    }
    if (kh) st64((tid >> 7) * 18432 + 12288 + (tid & 127) * 48 + 40, u32x2{0x3f803f80u, 0x3f803f80u});
    BAR();
    FST(6, 3);
    const float osc = scaled ? 1024.f : 1.f, qs = QSCALE * (1.f / 4096.f) * osc, sc2 = kh ? qs : osc;
#pragma unroll
    for (int nb = 0; nb < 3; ++nb)
#pragma unroll
      for (int q = 0; q < 2; ++q) {
        const f32x4 o = ldsf4(exr + nb * 2048 + q * 1024);
        float v0 = acc[nb][4 * q] + o[0], v1 = acc[nb][4 * q + 1] + o[1], v2 = acc[nb][4 * q + 2] + o[2], v3 = acc[nb][4 * q + 3] + o[3];
        if (nb == 2) { v0 *= sc2; v1 *= sc2; v2 *= sc2; v3 *= sc2; }
        else if (scaled) { v0 *= osc; v1 *= osc; v2 *= osc; v3 *= osc; }
        st64(imga[2 * nb + q], u32x2{pk2(v0, v1), pk2(v2, v3)});
      }
    BAR();
    FST(6, 4);
    {
      const u32 dst = WS_KVX + (u32)(mt * 16 + j) * KVB + w * 3072 + l16, src = (w >> 1) * 18432 + 12288 + (w & 1) * 3072 + l16;
#pragma unroll
      for (int i = 0; i < 3; ++i) __builtin_amdgcn_raw_buffer_store_b128(lds128(src + i * 1024), rs, dst + i * 1024, 0, 16);
    }
    f32x16 acc3;
#pragma unroll
    for (int e = 0; e < 16; ++e) acc3[e] = 0.f;
    {
      const int r3 = rsw < 24 ? rsw : (rsw < 28 ? rsw - 4 : rsw - 12);
      const u32 wb3 = 122880 + kh * 15360 + (h * 24 + r3) * 16;
      u32x4 w3[4];
#pragma unroll
      for (int k = 0; k < 3; ++k) w3[k] = lds128(wb3 + k * 768);
#pragma unroll
      for (int k = 0; k < 20; ++k) {
        if (k + 3 < 20) w3[(k + 3) & 3] = lds128(wb3 + (k + 3) * 768);
        acc3 = MFMA32H(w3[k & 3], a[k], acc3);
        if (k == 9) {
          FST(6, 5);
          WAITVM(0);
          FST(6, 6);
          if (lane == 0) {
            const u32 old = __hip_atomic_fetch_add((LAS u32*)(size_t)L_FLAG, 1u, __ATOMIC_RELAXED, __HIP_MEMORY_SCOPE_WORKGROUP);
            if (old == 7u) {
              __hip_atomic_store((gu32*)(ws + WS_KVF + (mt * 16 + j) * 128), 1u, RLX_AGENT);
              signal(ws, C_RG + j * 256); signal(ws, C_RD + n8 * 256);
            }
          }
        }
        SB();
      }
    }
    const u32 ex3w = (w < 4 ? 73728 : 84992) + w * 2048 + l16, ex3r = (w < 4 ? 93184 : 65536) + w * 2048 + l16;
    stf4(ex3w, f32x4{acc3[8], acc3[9], acc3[10], acc3[11]});
    stf4(ex3w + 1024, f32x4{acc3[12], acc3[13], acc3[14], acc3[15]});
    FST(6, 7);
    BAR();
    FST(6, 8);
    STAMP(2);
#pragma unroll
    for (int q = 0; q < 2; ++q) {
      const f32x4 o = ldsf4(ex3r + q * 1024);
      st64(imga[6 + q], u32x2{pk2((acc3[4 * q] + o[0]) * qs, (acc3[4 * q + 1] + o[1]) * qs), pk2((acc3[4 * q + 2] + o[2]) * qs, (acc3[4 * q + 3] + o[3]) * qs)});
    }
    BAR();
    FST(6, 9);
  }
  u32x2 xr[3];
  const int c16 = lane & 15, g4 = lane >> 4;
  {
    PHASE();
    const int hh = w >> 2, qb = w < 4 ? w : 7 - w;
    u32x4 q0f, q1f;
    {
      const u32 qa = (4 + hh) * 18432 + 12288 + (32 * qb + c32) * 48;
      const u32x2 x2 = lds64(qa + 32);
      q0f = lds128(qa + 16 * h);
      q1f = u32x4{h ? 0u : x2.x, h ? 0u : x2.y, 0u, 0u};
      const int tok64 = 128 * mt + 64 * th + 16 * tt + c16;
#pragma unroll
      for (int m = 0; m < 3; ++m) gld64(xr[m], (const char*)p.x, (u32)(tok64 * 1280 + (80 * n8 + 16 * ((m + 3 * kh) % 5) + 4 * g4) * 2));
    }
    BAR();
    STAMP(3); STAMP(4); CST(1, 18); FST(4, 12);
    const char* sg0 = ws + WS_WGF + (size_t)j * WGB + (kh * 40 + tt * 10) * 1024;
    const int tmin = mt >= 2 ? 0 : 8 - 4 * mt - qb;
    const u32 kb = hh * 18432 + (32 * qb + c32) * 48, kb2 = kb + 16 * h;
    const u32 vb = (2 + hh) * 18432 + (32 * qb + 4 * h + ((lane >> 2) & 3)) * 48 + ((lane >> 4) & 1) * 32 + (lane & 3) * 8;
    const int qlim = c32 - 4 * h;
    const int dw = (w - 2) & 3;
    const bool duty = w >= 2 && w <= 5;
    const Run<70, 4, false> rwo(ws + WS_WOF + (size_t)n8 * WOB, wo_slot(0), dw);
    const u32 fa = WS_ACT + (u32)((mt * 4 + dw) * 96 + 6 * j) * 1024 + l16;
    const u32 fx = dw < 2 ? WS_X1 + (u32)((mt * 4 + 2 * th + dw) * 40 + 5 * n8) * 1024 + l16 : WS_STAT + (u32)((mt * 4 + 2 * th + h) * 8 + n8) * 512 + c32 * 16;
    const int ko = 40 * j + 20 * hh + 4 * h;
    u32 ob = WS_OAF + (u32)((mt * 4 + qb) * 40) * 1024 + c32 * 16 + (ko >> 3) * 512 + ((ko >> 2) & 1) * 8;
    PIN(ob);
    auto lds_wait = [&](u32 addr, u32 want) LAM {
      for (u32 spins = 0; spins < SPIN_CAP * 8; ++spins) {
        if ((u32)__builtin_amdgcn_readfirstlane(__hip_atomic_load((LAS u32*)(size_t)addr, __ATOMIC_RELAXED, __HIP_MEMORY_SCOPE_WORKGROUP)) >= want) break;
        __builtin_amdgcn_s_sleep(1);
      }
      asm volatile("" ::: "memory");
    };
    {
      if (!duty) {
        const int sl = w >= 6;
        const u32 l0 = (w & 1) * 36864 + sl * 6144;
        if (mt + sl >= 2) {
          poll_ge(ws, WS_KVF + ((mt - 2 + sl) * 16 + j) * 128, 1u);
          const char* src = ws + WS_KVX + (size_t)((mt - 2 + sl) * 16 + j) * KVB + (w & 1) * 12288;
#pragma unroll
          for (int u = 0; u < 4; ++u) {
            const u32 l1 = l0 + (u >> 1) * 18432 + (u & 1) * 3072;
            dmai<0, true>(src + u * 3072, l16, l1); dmai<1024, true>(src + u * 3072, l16, l1); dmai<2048, true>(src + u * 3072, l16, l1);
          }
        } else {
#pragma unroll
          for (int u = 0; u < 12; ++u) st128(l0 + (u / 6) * 18432 + (u % 6) * 1024 + l16, u32x4{0u, 0u, 0u, 0u});
        }
      }
    }
    auto halo = [&]() LAM {
      if (!duty) {
        asm volatile("s_waitcnt vmcnt(0) lgkmcnt(0)" ::: "memory");
        if (lane == 0) __hip_atomic_fetch_add((LAS u32*)(size_t)(L_FLAG + (w >= 6 ? 8 : 4)), 1u, __ATOMIC_RELAXED, __HIP_MEMORY_SCOPE_WORKGROUP);
      }
    };
    f32x16 oacc;
    u32 rcnt;
    auto attn = [&](auto full_) LAM {
      constexpr bool FULL = decltype(full_)::v;
      u32x4 ka, kq, vf[2][2];
      kq = u32x4{0u, 0u, 0u, 0u};
      auto ldkv = [&](int t) LAM {
        const u32x2 x2 = lds64(kb + t * 1536 + 32);
        ka = lds128(kb2 + t * 1536); kq.x = x2.x; kq.y = x2.y;
#pragma unroll
        for (int s2 = 0; s2 < 2; ++s2) { const u32 va = vb + (32 * t + 16 * s2) * 48; vf[t & 1][s2] = ldstr2(va, va + 384); }
      };
      auto splat = [&](float v) LAM -> f32x16 {
        f32x16 c;
#pragma unroll
        for (int e = 0; e < 16; ++e) c[e] = v;
        return c;
      };
      auto pre = [&](auto m_) LAM {
        constexpr int m = decltype(m_)::v;
        if constexpr (m == 2) gld32_sc1(rcnt, ws, lane < 32 ? C_RG + j * 256 : C_RD + n8 * 256);
        if constexpr (m == 4) {
          if (duty) WAITV(SENT ? 6 : 4); else WAITV(0);
          PIN(rcnt);
          if (__builtin_amdgcn_ballot_w64(rcnt < (lane < 32 ? 16u : 32u)) != 0ull) { poll_ge(ws, C_RG + j * 256, 16u); poll_ge(ws, C_RD + n8 * 256, 32u); }
        }
        if constexpr (m >= 4) {
          gld_sc1<((2 * m - 8) & 3) * 1024>(st[2 * m - 8], sg0 + ((2 * m - 8) >> 2) * 4096, l16);
          gld_sc1<((2 * m - 7) & 3) * 1024>(st[2 * m - 7], sg0 + ((2 * m - 7) >> 2) * 4096, l16);
        }
        if constexpr (m == 1 && OWARM) {
#pragma unroll
          for (int g = 0; g < 3; ++g)
            if (g < 2 || h == 0) __builtin_amdgcn_raw_buffer_store_b64(u32x2{0u, 0u}, rs, ob + g * 512, 0, 16);
          asm volatile("" ::: "memory");
        }
        if (duty) {
          rwo.go<2 * m>(l16); rwo.go<2 * m + 1>(l16);
          const u32x4 f = {~0u, ~0u, ~0u, ~0u};
          if constexpr (m < 6 && SENT) __builtin_amdgcn_raw_buffer_store_b128(f, rs, fa + m * 1024, 0, 16);
          if constexpr (m >= 4 && SENT) { if (dw < 2 || (dw == 2 && m == 4)) __builtin_amdgcn_raw_buffer_store_b128(f, rs, fx + (m - 4) * 1024, 0, 16); }
          asm volatile("" ::: "memory");
        }
        SB();
      };
      auto sync = [&](int t) LAM {
        if (t >= 4) { if (qb == 7 - t) lds_wait(L_FLAG + 8, 2u); } else { if (qb == 3 - t) lds_wait(L_FLAG + 4, 2u); }
      };
      f32x16 sb[2], cm;
      float pe[16];
      FST(4, 1);
      ldkv(8);
      sb[0] = MFMA32(ka, q0f, splat(0.f));
      sb[0] = MFMA32(kq, q1f, sb[0]);
      SB();
      pre(IC<0>{});
#pragma unroll
      for (int e = 0; e < 16; ++e) pe[e] = ((e & 3) + 8 * (e >> 2) > qlim) ? -1.0e30f : sb[0][e];
      float mx = __builtin_fmaxf(pe[0], pe[1]);
#pragma unroll
      for (int e = 2; e < 16; e += 2) mx = max3(mx, pe[e], pe[e + 1]);
      mx = -max32(mx);
      cm = splat(mx);
      auto cin = [&](int t) LAM -> f32x16 { if constexpr (FULL) return cm; else return splat(t >= tmin ? mx : -1.0e30f); };
#pragma unroll
      for (int s2 = 0; s2 < 2; ++s2) {
        u32x4 pf;
#pragma unroll
        for (int e = 0; e < 4; ++e) pf[e] = pk2(__builtin_amdgcn_exp2f(pe[8 * s2 + 2 * e] + mx), __builtin_amdgcn_exp2f(pe[8 * s2 + 2 * e + 1] + mx));
        if (s2 == 0) oacc = MFMA32(vf[0][0], pf, splat(0.f)); else oacc = MFMA32(vf[0][1], pf, oacc);
      }
      SB();
      FST(4, 2);
      halo();
      pre(IC<1>{});
      sync(7);
      ldkv(7);
      sb[1] = MFMA32(ka, q0f, cin(7));
      sb[1] = MFMA32(kq, q1f, sb[1]);
      SB();
      sfor<0, 8>([&](auto u_) LAM {
        constexpr int t = 7 - decltype(u_)::v, c = t & 1, n = c ^ 1;
        u32x4 pf0, pf1;
        FST(4, 3 + decltype(u_)::v);
        if constexpr (t > 0) { if constexpr (t - 1 != 7) sync(t - 1); ldkv(t - 1); }
        if constexpr (t == 0) {
#pragma unroll
          for (int e = 0; e < 16; ++e) sb[c][e] = ((e & 3) + 8 * (e >> 2) > qlim) ? sb[c][e] : -1.0e30f;
        }
#pragma unroll
        for (int e = 0; e < 8; ++e) pe[e] = __builtin_amdgcn_exp2f(sb[c][e]);
        SB();
        if constexpr (t > 0) { sb[n] = MFMA32(ka, q0f, cin(t - 1)); SB(); }
#pragma unroll
        for (int e = 8; e < 12; ++e) pe[e] = __builtin_amdgcn_exp2f(sb[c][e]);
#pragma unroll
        for (int e = 0; e < 4; ++e) pf0[e] = pk2(pe[2 * e], pe[2 * e + 1]);
        SB();
        if constexpr (t > 0) { sb[n] = MFMA32(kq, q1f, sb[n]); SB(); }
#pragma unroll
        for (int e = 12; e < 16; ++e) pe[e] = __builtin_amdgcn_exp2f(sb[c][e]);
        SB();
        oacc = MFMA32(vf[c][0], pf0, oacc);
        SB();
#pragma unroll
        for (int e = 0; e < 4; ++e) pf1[e] = pk2(pe[8 + 2 * e], pe[8 + 2 * e + 1]);
        SB();
        if constexpr (t > 0) pre(IC<9 - t>{});
        oacc = MFMA32(vf[c][1], pf1, oacc);
        SB();
      });
    };
    auto exact = [&]() LAM {
      auto sc = [&](int t, float c0, f32x16& s) LAM {
        const u32x2 x2 = lds64(kb + t * 1536 + 32);
#pragma unroll
        for (int e = 0; e < 16; ++e) s[e] = c0;
        s = MFMA32(lds128(kb2 + t * 1536), q0f, s);
        s = MFMA32((u32x4{x2.x, x2.y, 0u, 0u}), q1f, s);
#pragma unroll
        for (int e = 0; e < 16; ++e) s[e] = (u32)(32 * t + (e & 3) + 8 * (e >> 2) - qlim - 1) < 256u ? s[e] : -1.0e30f;
      };
      float mx = -1.0e30f;
      f32x16 s;
#pragma nounroll
      for (int t = tmin; t < 9; ++t) {
        sc(t, 0.f, s);
#pragma unroll
        for (int e = 0; e < 16; e += 2) mx = max3(mx, s[e], s[e + 1]);
      }
      mx = -max32(mx);
#pragma unroll
      for (int e = 0; e < 16; ++e) oacc[e] = 0.f;
#pragma nounroll
      for (int t = tmin; t < 9; ++t) {
        sc(t, mx, s);
#pragma unroll
        for (int s2 = 0; s2 < 2; ++s2) {
          u32x4 pf;
#pragma unroll
          for (int e = 0; e < 4; ++e) pf[e] = pk2(__builtin_amdgcn_exp2f(s[8 * s2 + 2 * e]), __builtin_amdgcn_exp2f(s[8 * s2 + 2 * e + 1]));
          const u32 va = vb + (32 * t + 16 * s2) * 48;
          oacc = MFMA32(ldstr2(va, va + 384), pf, oacc);
        }
      }
    };
    if (mt >= 2) attn(BoolC<true>{}); else attn(BoolC<false>{});
    if (__builtin_amdgcn_ballot_w64(h != 0 && !(oacc[8] < ATT_LIMIT)) != 0ull) exact();
    const float lx = xor32f(oacc[8]);
    const float inv = __builtin_amdgcn_rcpf(64.f * (h ? oacc[8] : lx));
#pragma unroll
    for (int g = 0; g < 3; ++g) {
      const u32x2 o = {pk2(oacc[4 * g] * inv, oacc[4 * g + 1] * inv), pk2(oacc[4 * g + 2] * inv, oacc[4 * g + 3] * inv)};
      if (g < 2 || h == 0) __builtin_amdgcn_raw_buffer_store_b64(o, rs, ob + g * 512, 0, 16);
    }
    STAMP(5); CST(1, 19); FST(4, 11);
    WAITVM(0);
    BAR();
    if (tid == 0) signal(ws, C_O + mt * 256);
    STAMP(6); FST(4, 13);
  }
  u32 wbase[5], avoff;
  u32x2 x1k[3];
  f32x4 c5[5];
  {
    PHASE();
    const char* sg1 = ws + WS_WGF + (size_t)j * WGB + 81920 + (kh * 40 + tt * 8) * 1024;
    auto stage1 = [&]() LAM { sfor<0, 8>([&](auto m_) LAM { constexpr int m = decltype(m_)::v; gld_sc1<(m & 3) * 1024>(st[10 + m], sg1 + (m >> 2) * 4096, l16); }); };
    if (w >= 4) { __builtin_amdgcn_s_sleep(2); Run<30, 4, false>(ws + WS_WOF + (size_t)n8 * WOB + 71680, wo_slot(7), w - 4).all(l16); }
    stage1();
#pragma unroll
    for (int m = 0; m < 5; ++m) wbase[m] = (kh * 5 + (m + 3 * kh) % 5) * 1024 + l16;
    avoff = (g4 >> 1) * 1024 + (g4 & 1) * 512 + (16 * (tt & 1) + c16) * 16;
    const int tt64 = mt * 4 + 2 * th + (tt >> 1);
    const char* ab = ws + WS_OAF + (size_t)(tt64 * 40 + 20 * kh) * 1024;
    const u32 ew = 73728 + (tt * 5) * 1024 + l16;
    u32 xb = WS_X1 + (u32)(tt64 * 40 + 5 * n8) * 1024 + (g4 >> 1) * 512 + (16 * (tt & 1) + c16) * 16 + (g4 & 1) * 8 + kh * 3072;
    u32 sta = WS_STAT + (u32)((tt64 * 8 + n8) * 32 + 16 * (tt & 1) + c16) * 16 + kh * 8;
    u32 mo = L_MODS + kh * 192 + g4 * 16;
    PIN(xb); PIN(sta); PIN(mo); PIN(avoff);
    auto iss = [&](auto s_) LAM { constexpr int s = decltype(s_)::v; gld_sc1<(s & 1) * 2048>(a[s], ab + (s >> 1) * 4096, avoff); };
    u32x4 wf[3][5];
#pragma unroll
    for (int s = 0; s < 2; ++s)
#pragma unroll
      for (int m = 0; m < 5; ++m) wf[s][m] = lds128(wbase[m] + wo_slot(s));
#pragma unroll
    for (int m = 0; m < 5; ++m) c5[m] = f32x4{0.f, 0.f, 0.f, 0.f};
    if (w == 0) poll_ge(ws, C_O + mt * 256, 16u);
    BAR();
    STAMP(7); FST(5, 1);
    sfor<0, 5>(iss);
    sfor<0, 10>([&](auto s_) LAM {
      constexpr int s = decltype(s_)::v;
      WAITV(s == 0 ? 4 : s == 1 ? 6 : 9 - s);
      FST(5, 2 + s);
      if (s == 4) BAR();
      PIN(a[s]);
      if constexpr (s + 2 < 10) {
#pragma unroll
        for (int m = 0; m < 5; ++m) wf[(s + 2) % 3][m] = lds128(wbase[m] + wo_slot(s + 2));
      }
#pragma unroll
      for (int m = 0; m < 5; ++m) c5[m] = MFMA16(wf[s % 3][m], a[s], c5[m]);
      SB();
      if constexpr (s == 0) sfor<5, 8>(iss);
      if constexpr (s == 1) sfor<8, 10>(iss);
      SB();
    });
    STAMP(8); FST(5, 12);
    stf4(ew + ((3 + 3 * kh) % 5) * 1024, c5[3]);
    stf4(ew + ((4 + 3 * kh) % 5) * 1024, c5[4]);
    if (kh) stf4(ew, c5[2]);
    BAR();
    FST(5, 13);
    float xs[12];
#pragma unroll
    for (int m = 0; m < 3; ++m) PIN(xr[m]);
#pragma unroll
    for (int m = 0; m < 3; ++m) {
      if (m < 2 || kh == 0) {
        const f32x4 o = ldsf4(ew + kh * 3072 + m * 1024), ga = ldsf4(mo + m * 64), g2 = ldsf4(mo + 320 + m * 64);
        const u32 y01 = pk2(c5[m][0] + o[0], c5[m][1] + o[1]), y23 = pk2(c5[m][2] + o[2], c5[m][3] + o[3]);
        const u32 t01 = pk2(ga[0] * lo16(y01), ga[1] * hi16(y01)), t23 = pk2(ga[2] * lo16(y23), ga[3] * hi16(y23));
        x1k[m] = u32x2{pk2(lo16(xr[m].x) + lo16(t01), hi16(xr[m].x) + hi16(t01)), pk2(lo16(xr[m].y) + lo16(t23), hi16(xr[m].y) + hi16(t23))};
        xs[4 * m] = lo16(x1k[m].x); xs[4 * m + 1] = hi16(x1k[m].x); xs[4 * m + 2] = lo16(x1k[m].y); xs[4 * m + 3] = hi16(x1k[m].y);
        __builtin_amdgcn_raw_buffer_store_b64(u32x2{pk2(xs[4 * m] * g2[0], xs[4 * m + 1] * g2[1]), pk2(xs[4 * m + 2] * g2[2], xs[4 * m + 3] * g2[3])}, rs, xb + m * 1024, 0, 16);
      } else {
        x1k[m] = u32x2{0u, 0u};
        xs[8] = xs[9] = xs[10] = xs[11] = 0.f;
      }
    }
    float s = ((xs[0] + xs[1]) + (xs[2] + xs[3])) + ((xs[4] + xs[5]) + (xs[6] + xs[7])) + ((xs[8] + xs[9]) + (xs[10] + xs[11]));
    s = sum32(sum16(s));
    const float mean = s * (kh ? 1.f / 32.f : 1.f / 48.f);
    float q0 = 0.f, q1 = 0.f;
#pragma unroll
    for (int e = 0; e < 4; ++e) { const float d0 = xs[e] - mean, d1 = xs[4 + e] - mean; q0 = fma1(d0, d0, q0); q1 = fma1(d1, d1, q1); }
    if (kh == 0) {
#pragma unroll
      for (int e = 8; e < 12; ++e) { const float d = xs[e] - mean; q0 = fma1(d, d, q0); }
    }
    const float q = sum32(sum16(q0 + q1));
    if (g4 == 0) __builtin_amdgcn_raw_buffer_store_b64(u32x2{f2u(mean), f2u(q)}, rs, sta, 0, 16);
    FST(5, 14);
    if (!SENT) WAITVM(0);
    BAR();
    if (!SENT && tid == 0) signal(ws, WS_CNT + (16 + mt) * 256);
    STAMP(9); FST(5, 16);
  }
  {
    PHASE();
    const bool old4 = w < 4;
    const char* wgb = ws + WS_WGF + (size_t)j * WGB;
    const char* wdb = ws + WS_WDF + (size_t)n8 * WDB;
    u32x4 stv[4], csv[2];
    const char* ab = ws + WS_X1 + (size_t)((mt * 4 + tt) * 40 + 20 * kh) * 1024;
    const char* sb = ws + WS_STAT + (size_t)(mt * 4 + tt) * 4096;
    auto iss = [&](auto s_) LAM { constexpr int s = decltype(s_)::v; gld_sc1<(s & 3) * 1024>(a[s], ab + (s >> 2) * 4096, l16); };
    {
      const char* cb = ws + WS_CSP + (size_t)(j * 16 + 4 * (w >> 1) + (w & 1)) * 768;
      const u32 co = lane < 48 ? l16 : 0u;
      gld_sc1<0>(csv[0], cb, co); gld_sc1<1536>(csv[1], cb, co);
    }
#pragma unroll
    for (int m = 0; m < 18; ++m) { PIN(st[m]); st128((m < 10 ? (kh * 40 + tt * 10 + m) : (80 + kh * 32 + tt * 8 + m - 10)) * 1024 + l16, st[m]); }
    float c1 = 0.f, d2 = 0.f;
    u32x4 bcor = {0u, 0u, 0u, 0u};
    u32 sw = kh * 40960 + tt * 4096 + l16, sr = (kh ^ 1) * 40960 + tt * 4096 + l16;
    u32 actb = WS_ACT + (u32)((mt * 4 + tt) * 96 + 6 * j + kh) * 1024 + l16;
    const u32 so = h * 2048 + c32 * 16;
    PIN(actb);
    const auto rC = gu_run<1, 16, 20, 4>(wgb, w & 3);
    const auto rA = gu_run<2, 0, 8, 4>(wgb, w & 3);
    const auto rB = gu_run<2, 8, 20, 4>(wgb, w & 3);
    const Run<60, 8, true> rD(wdb, 0, w);
    const Run<90, 8, true> rS(wdb + 61440, 61440, w);
    if (SENT) gate(sb, (c32 >> 2) * 512 + ((c32 >> 1) & 1) * 256 + (c32 & 1) * 8);
    else { if (w == 0) poll_ge(ws, WS_CNT + (16 + mt) * 256, 16u); BAR(); }
    STAMP(10); FST(2, 1); FST(5, 17); FST(9, 1);
    sfor<0, 2>(iss);
    gld_sc1<0>(stv[0], sb, so); gld_sc1<512>(stv[1], sb, so); gld_sc1<1024>(stv[2], sb, so); gld_sc1<1536>(stv[3], sb, so);
    sfor<2, 6>(iss);
    f32x16 ag[2], au[2];
    u32x4 fg[3], fu[3], outv;
    u32 ub = 0;
    auto ldw = [&](auto g_) LAM {
      constexpr int g = decltype(g_)::v, pp = g / 20, i = g % 20;
      constexpr bool first = i == 0 || gu_unit(pp, 0, i) != gu_unit(pp, 0, i - 1) + 1;
      constexpr int i0 = [] { int x = i; while (x > 0 && gu_unit(pp, 0, x) == gu_unit(pp, 0, x - 1) + 1) --x; return x; }();
      if constexpr (first) ub = frow + (kh ? gu_unit(pp, 1, i) : gu_unit(pp, 0, i)) * 2048;
      fg[g % 3] = lds128(ub + (i - i0) * 2048); fu[g % 3] = lds128(ub + (i - i0) * 2048 + 1024);
    };
    auto xwrite = [&](int pp) LAM {
      const f32x16 &G = ag[pp & 1], &U = au[pp & 1];
      stf4(sw, f32x4{G[8], G[9], G[10], G[11]}); stf4(sw + 1024, f32x4{G[12], G[13], G[14], G[15]});
      stf4(sw + 2048, f32x4{U[8], U[9], U[10], U[11]}); stf4(sw + 3072, f32x4{U[12], U[13], U[14], U[15]});
    };
    f32x4 xo[4] = {};
    auto xread = [&]() LAM {
#pragma unroll
      for (int u = 0; u < 4; ++u) xo[u] = ldsf4(sr + u * 1024);
    };
    auto xadd = [&](int pp, int q) LAM {
#pragma unroll
      for (int i = 0; i < 4; ++i) { ag[pp & 1][4 * q + i] += xo[q][i]; au[pp & 1][4 * q + i] += xo[2 + q][i]; }
    };
    auto corr = [&](int pp) LAM {
      ag[pp & 1] = MFMA32(lds128(L_T2 + pp * 1024 + rsw * 16), bcor, ag[pp & 1]);
      au[pp & 1] = MFMA32(lds128(L_T2 + pp * 1024 + 512 + rsw * 16), bcor, au[pp & 1]);
    };
    float eu[2], ee[2];
    auto epi = [&](int pp, int o, int ph) LAM {
#pragma unroll
      for (int i = 0; i < 2; ++i) {
        if (ph == 0) { ee[i] = __builtin_amdgcn_exp2f(ag[pp & 1][2 * o + i] * c1); eu[i] = ag[pp & 1][2 * o + i] * au[pp & 1][2 * o + i]; }
        if (ph == 1) ee[i] = __builtin_amdgcn_rcpf(fma1(ee[i], d2, d2));
      }
      if (ph == 2) outv[o] = pk2(eu[0] * ee[0], eu[1] * ee[1]);
    };
    auto ostore = [&](int pp) LAM { __builtin_amdgcn_raw_buffer_store_b128(outv, rs, actb + pp * 2048, 0, 16); asm volatile("" ::: "memory"); };
    BAR();
    FST(9, 2);
    ldw(IC<0>{}); ldw(IC<1>{});
    auto nextA = [&](auto g_) LAM {
      constexpr int g = decltype(g_)::v;
      WAITV(g == 0 ? 9 : g == 1 ? 12 : g == 2 ? 11 : g == 3 ? 14 : 19 - g);
      PIN(a[g]);
      ensure<(g & 3) * 1024>(a[g], ab + (g >> 2) * 4096, l16);
    };
    nextA(IC<0>{});
    sfor<0, 60>([&](auto g_) LAM {
      constexpr int g = decltype(g_)::v, pp = g / 20, i = g % 20;
      if (g == 32) WAITV(0);
      if (g == 43) WAITV(NRS >= 3 ? 4 : 1 + NRS);
      if ((g & 3) == 0) FST(2, 2 + g / 4);
      if (g == 12 || g == 22 || g == 32 || g == 43 || g == 50) BAR();
      if (g == 22 && CSEL != 4) { STAMP(16); CST(0, 18); }
      if (g == 43 && CSEL != 4) { STAMP(17); CST(0, 19); }
      if constexpr (g + 2 < 60) ldw(IC<g + 2>{});
      if (g == 22 || g == 43) xread();
      if (i == 0) {
        f32x16 z;
#pragma unroll
        for (int e = 0; e < 16; ++e) z[e] = 0.f;
        ag[pp & 1] = MFMA32(fg[g % 3], a[i], z); au[pp & 1] = MFMA32(fu[g % 3], a[i], z);
      } else if (g == 45) {
        ag[0] = MFMA32(fg[g % 3], a[i], ag[0]); au[0] = MFMA32(fu[g % 3], a[i], au[0]);
        if (!kh) corr(2);
      } else {
        ag[pp & 1] = MFMA32(fg[g % 3], a[i], ag[pp & 1]); au[pp & 1] = MFMA32(fu[g % 3], a[i], au[pp & 1]);
      }
      if (g == 4) {
        PIN(csv[0]); PIN(csv[1]);
        f32x4 sm;
#pragma unroll
        for (int e = 0; e < 4; ++e) sm[e] = u2f(csv[0][e]) + u2f(csv[1][e]);
        if (lane < 48) stf4(L_GS + (w >> 1) * 1536 + (w & 1) * 768 + l16, sm);
      }
      if (g == 6) {
        float s = 0.f;
#pragma unroll
        for (int u = 0; u < 4; ++u) s += 48.f * u2f(stv[u][0]) + 32.f * u2f(stv[u][2]);
        c1 = sum32(s) * (1.f / 640.f);
      }
      if (g == 7) {
        float q = 0.f;
#pragma unroll
        for (int u = 0; u < 4; ++u) {
          const float d0 = u2f(stv[u][0]) - c1, d1 = u2f(stv[u][2]) - c1;
          q += u2f(stv[u][1]) + u2f(stv[u][3]) + 48.f * d0 * d0 + 32.f * d1 * d1;
        }
        d2 = sum32(q);
      }
      if (g == 8) {
        const float mean = c1, den = rbf(__builtin_amdgcn_sqrtf(rbf(rbf(d2 * (1.f / 640.f)) + 1e-5f)));
        c1 = -1.4426950408889634f * __builtin_amdgcn_rcpf(den); d2 = den * den;
        const u32 nm = pk2(-mean, -mean), dd = pk2(den, den);
        bcor = u32x4{h ? 0u : nm, h ? 0u : ((nm & 0xffffu) | (dd & 0xffff0000u)), h ? 0u : dd, 0u};
      }
      if (g == 12) {
        const u32 t5 = tid - 256 < 192 ? tid - 256 : 0, t4 = L_GS + (t5 >> 3) * 64 + (t5 & 7) * 4;
        if (kh) {
#pragma unroll
          for (int u = 0; u < 4; ++u) { xo[0][u] = ldsf(t4 + u * 1536); xo[1][u] = ldsf(t4 + u * 1536 + 32); }
        }
      }
      if (g == 14 && kh) {
        const float cs = (xo[0][0] + xo[0][1]) + (xo[0][2] + xo[0][3]), bi = (xo[1][0] + xo[1][1]) + (xo[1][2] + xo[1][3]);
        const u32 h0 = pk2(cs, bi);
        const float cr = cs - lo16(h0), br = bi - hi16(h0);
        const u32 h1 = pk2(cr, br), h2 = pk2(cr - lo16(h1), br - hi16(h1));
        const int t5 = tid - 256, gu = t5 >= 96, nn = t5 - 96 * gu;
        if (t5 < 192)
          st128(L_T2 + (nn >> 5) * 1024 + gu * 512 + (nn & 31) * 16,
                u32x4{__builtin_amdgcn_perm(h1, h0, 0x05040100u), __builtin_amdgcn_perm(h0, h2, 0x07060100u), __builtin_amdgcn_perm(h2, h1, 0x07060302u), 0u});
      }
      if (g == 21 || g == 41) xwrite(pp - 1);
      if (g == 22 || g == 42) corr(pp - 1);
      if (g == 24 || g == 25) xadd(0, g - 24);
      if (g >= 28 && g <= 37 && (g - 28) % 3 == 0) epi(0, (g - 28) / 3, 2);
      if (g >= 26 && g < 37 && (g - 26) % 3 < 2) epi(0, (g - 26) / 3, (g - 26) % 3);
      if (g == 44 || g == 45) xadd(1, g - 44);
      if (g >= 48 && g <= 57 && (g - 48) % 3 == 0) epi(1, (g - 48) / 3, 2);
      if (g >= 46 && g < 57 && (g - 46) % 3 < 2) epi(1, (g - 46) / 3, (g - 46) % 3);
      SGB(0x100, 6, 0); SGB(0x008, 1, 0); SGB(0x002, 4, 0); SGB(0x008, 1, 0);
      SB();
      if constexpr (g < 3) sfor<6 + 4 * g, 10 + 4 * g>(iss);
      if constexpr (g == 3) sfor<18, 20>(iss);
      if constexpr (g + 1 < 20) nextA(IC<g + 1>{});
      if constexpr (g == 4 || g == 5) {
        PIN(stv[2 * (g - 4)]); PIN(stv[2 * (g - 4) + 1]);
        ensure<1024 * (g - 4)>(stv[2 * (g - 4)], sb, so); ensure<1024 * (g - 4) + 512>(stv[2 * (g - 4) + 1], sb, so);
      }
      if (g == 40) ostore(0);
      if constexpr (g >= 40 && g < 40 + NRS) gld_sc1<((g - 40) & 3) * 1024>(a[g - 40], rS.src + ((g - 40) >> 2) * 4096, l16);
      if constexpr (g >= 50 && g < 58) rD.template go<g - 50>(l16);
      if (g == 57) ostore(1);
      if constexpr ((g >= 19 && g < 21) || (g >= 22 && g < 26) || (g >= 32 && g < 38)) {
        if (old4) {
          if constexpr (g < 21) { rC.template go<2 * (g - 19)>(l16); rC.template go<2 * (g - 19) + 1>(l16); }
          else if constexpr (g < 26) { rA.template go<2 * (g - 22)>(l16); rA.template go<2 * (g - 22) + 1>(l16); }
          else { rB.template go<2 * (g - 32)>(l16); rB.template go<2 * (g - 32) + 1>(l16); }
        }
      }
      SB();
    });
    { const u32 lo_ = 65536 + tt * 4096, hi_ = (tt < 2 ? 106496 : 131072) + tt * 4096; sw = (kh ? hi_ : lo_) + l16; sr = (kh ? lo_ : hi_) + l16; }
    xwrite(2);
    BAR();
    STAMP(11); FST(2, 17);
    xread(); xadd(2, 0); xadd(2, 1);
#pragma unroll
    for (int o = 0; o < 4; ++o)
#pragma unroll
      for (int ph = 0; ph < 3; ++ph) epi(2, o, ph);
    ostore(2);
    if (SENT) WAITV(8); else WAITVM(0);
    BAR();
    if (!SENT && tid == 0) signal(ws, WS_CNT + (32 + mt) * 256);
#pragma unroll
    for (int m = 0; m < NRS; ++m) { PIN(a[m]); st128(rS.lds + m * 1024 + l16, a[m]); }
    sfor<NRS, 12>([&](auto m_) LAM { rS.template go<decltype(m_)::v>(l16); });
    STAMP(12);
  }
  {
    PHASE();
    const bool old4 = w < 4;
    const char* wdb = ws + WS_WDF + (size_t)n8 * WDB;
    const int tt64 = mt * 4 + 2 * th + (tt >> 1);
    const char* ab = ws + WS_ACT + (size_t)(tt64 * 96 + 48 * kh) * 1024;
    auto iss = [&](auto s_) LAM { constexpr int s = decltype(s_)::v; gld_sc1<(s & 1) * 2048>(a[s], ab + (s >> 1) * 4096, avoff); };
#pragma unroll
    for (int m = 0; m < 5; ++m) c5[m] = f32x4{0.f, 0.f, 0.f, 0.f};
    const Run<50, 4, true> rE(wdb + 153600, 0, w & 3);
    const Run<40, 4, true> rF(wdb + 204800, 51200, w & 3);
    const u32 ew = 92160 + (tt * 5) * 1024 + l16;
    u32 oo = (u32)(128 * mt + 64 * th + 16 * tt + c16) * 1280 + (80 * n8 + 48 * kh + 4 * g4) * 2;
    u32 mo = L_MODS + 640 + kh * 192 + g4 * 16;
    PIN(oo); PIN(mo);
    if (SENT) gate(ab, ((c16 >> 1) * 6 + 4 + (c16 & 1)) * 1024 + (tt & 1) * 256);
    else { if (w == 0) poll_ge(ws, WS_CNT + (32 + mt) * 256, 16u); BAR(); }
    STAMP(13); CST(2, 18); FST(1, 1);
    sfor<0, 6>(iss);
    u32 wb2[5], wb3[5];
#pragma unroll
    for (int m = 0; m < 5; ++m) { wb2[m] = wbase[m] + 61440; wb3[m] = wbase[m] + 122880; PIN(wb2[m]); PIN(wb3[m]); }
    auto nextA = [&](auto s_) LAM {
      constexpr int s = decltype(s_)::v;
      if constexpr (dn_wait(s, true) == dn_wait(s, false)) WAITV(dn_wait(s, true)); else { if (old4) WAITV(dn_wait(s, true)); else WAITV(dn_wait(s, false)); }
      PIN(a[s]);
      ensure<(s & 1) * 2048>(a[s], ab + (s >> 1) * 4096, avoff);
    };
    BAR();
    u32x4 wf[3][5];
#pragma unroll
    for (int u = 0; u < 2; ++u)
#pragma unroll
      for (int m = 0; m < 5; ++m) wf[u][m] = lds128(wbase[m] + wd_slot(u));
    nextA(IC<0>{});
    sfor<0, 24>([&](auto s_) LAM {
      constexpr int s = decltype(s_)::v;
      if ((s & 1) == 0) FST(1, 2 + s / 2);
      if (s == 5 || s == 9 || s == 13 || s == 18) BAR();
      if constexpr (s + 2 < 24) {
        constexpr u32 so = wd_slot(s + 2);
#pragma unroll
        for (int m = 0; m < 5; ++m) wf[(s + 2) % 3][m] = lds128((so < 61440 ? wbase[m] : so < 122880 ? wb2[m] : wb3[m]) + so % 61440);
      }
#pragma unroll
      for (int m = 0; m < 5; ++m) c5[m] = MFMA16(wf[s % 3][m], a[s], c5[m]);
      SB();
      if constexpr (s >= 5 && s < 14) {
        if (old4) {
          if constexpr (s < 9) { sfor<0, 4>([&](auto u_) LAM { rE.template go<4 * (s - 5) + decltype(u_)::v>(l16); }); }
          else { rF.template go<2 * (s - 9)>(l16); rF.template go<2 * (s - 9) + 1>(l16); }
        }
      }
      sfor<dn_cumA(s), dn_cumA(s + 1)>(iss);
      if constexpr (s + 1 < 24) nextA(IC<s + 1>{});
      SB();
    });
    STAMP(14); CST(2, 19); FST(1, 14);
    stf4(ew + ((3 + 3 * kh) % 5) * 1024, c5[3]);
    stf4(ew + ((4 + 3 * kh) % 5) * 1024, c5[4]);
    if (kh) stf4(ew, c5[2]);
    BAR();
#pragma unroll
    for (int m = 0; m < 3; ++m)
      if (m < 2 || kh == 0) {
        const f32x4 o = ldsf4(ew + kh * 3072 + m * 1024), gb = ldsf4(mo + m * 64);
        const u32 y01 = pk2(c5[m][0] + o[0], c5[m][1] + o[1]), y23 = pk2(c5[m][2] + o[2], c5[m][3] + o[3]);
        const u32 t01 = pk2(gb[0] * lo16(y01), gb[1] * hi16(y01)), t23 = pk2(gb[2] * lo16(y23), gb[3] * hi16(y23));
        const u32x2 r = {pk2(lo16(x1k[m].x) + lo16(t01), hi16(x1k[m].x) + hi16(t01)), pk2(lo16(x1k[m].y) + lo16(t23), hi16(x1k[m].y) + hi16(t23))};
        *(GAS u32x2*)((GAS char*)p.out + oo + m * 32) = r;
      }
  }
}
}

extern "C" void kernel_launch(void* const* d_in, const int* in_sizes, int n_in, void* d_out, int out_size, void* d_ws, size_t ws_size,
                              hipStream_t stream) {
  (void)in_sizes; (void)n_in; (void)out_size; (void)ws_size;
  (void)hipFuncSetAttribute((const void*)k2, hipFuncAttributeMaxDynamicSharedMemorySize, LDS_BYTES);
  P1 q;
  q.x = (const u16*)d_in[0]; q.wqkv = (const u16*)d_in[1]; q.wo = (const u16*)d_in[2];
  q.cvec = (const u16*)d_in[7]; q.wmod = (const u16*)d_in[8]; q.ws = (char*)d_ws;
  k1<<<K1_BLOCKS, 256, 0, stream>>>(q);
  P2 p;
  p.x = (const u16*)d_in[0]; p.wg = (const u16*)d_in[4]; p.wu = (const u16*)d_in[5]; p.wd = (const u16*)d_in[6];
  p.out = (u16*)d_out; p.ws = (char*)d_ws;
  k2<<<256, 512, LDS_BYTES, stream>>>(p);
}
```

```cpp
#include <hip/hip_runtime.h>
#include <hip/hip_bf16.h>
#include <stdint.h>

#define GAS __attribute__((address_space(1)))
#define LAS __attribute__((address_space(3)))
#if defined(__HIP_DEVICE_COMPILE__)
#define NOPK __attribute__((target("no-packed-fp32-ops")))
#else
#define NOPK
#endif
#define DI __device__ __forceinline__ NOPK
#define LAM __attribute__((always_inline)) NOPK
typedef unsigned int u32;
typedef unsigned short u16;
typedef u32 u32x4 __attribute__((ext_vector_type(4)));
typedef u32 u32x2 __attribute__((ext_vector_type(2)));
typedef float f32x2 __attribute__((ext_vector_type(2)));
typedef float f32x4 __attribute__((ext_vector_type(4)));
typedef float f32x16 __attribute__((ext_vector_type(16)));
typedef __bf16 bf16x2 __attribute__((ext_vector_type(2)));
typedef __bf16 bf16x4 __attribute__((ext_vector_type(4)));
typedef __bf16 bf16x8 __attribute__((ext_vector_type(8)));
typedef GAS u32 gu32;
typedef _Float16 f16x8 __attribute__((ext_vector_type(8)));
typedef _Float16 f16x2 __attribute__((ext_vector_type(2)));

#define STAMPS 0

namespace {
constexpr int S = 2048, D = 640, F = 1536;
constexpr u32 WS_KVF = 0;
constexpr u32 WS_CNT = 32768;
constexpr u32 WS_MODP = 65536;
constexpr u32 WS_CSP = WS_MODP + 61440;
constexpr u32 WS_STAT = WS_CSP + 16 * 16 * 1536;
constexpr u32 WS_N1 = WS_STAT + 64 * 8 * 32 * 16;
constexpr u32 WS_OAF = WS_N1 + 64 * 40 * 1024;
constexpr u32 WS_X1 = WS_OAF + 64 * 40 * 1024;
constexpr u32 WS_ACT = WS_X1 + 64 * 40 * 1024;
constexpr u32 KVB = 24576;
constexpr u32 WS_KVX = WS_ACT + 64 * 96 * 1024;
constexpr u32 WQB = 153600;
constexpr u32 WS_WQF = WS_KVX + 256 * KVB;
constexpr u32 WOB = 102400, WGB = 245760, WDB = 245760;
constexpr u32 WS_WOF = WS_WQF + 16 * WQB;
constexpr u32 WS_WGF = WS_WOF + 8 * WOB;
constexpr u32 WS_WDF = WS_WGF + 16 * WGB;
constexpr u32 WS_DBG = WS_WDF + 8 * WDB;
constexpr u32 WS_DBG1 = WS_DBG + 256 * 32 * 8;
constexpr u32 WS_END = WS_DBG1 + 1024 * 16;
constexpr u32 L_MISC = 153600;
constexpr u32 L_GS = L_MISC;
constexpr u32 L_MODS = L_MISC + 6144;
constexpr u32 L_T2 = L_MISC + 7168;
constexpr u32 L_FLAG = L_MISC + 9728;
constexpr u32 LDS_BYTES = 163840;
constexpr u32 L_K = 0, L_V = 30720;
constexpr float QSCALE = 0.22360679774997896f * 1.4426950408889634f;
constexpr int K1_BLOCKS = 488;
#define ATT_LIMIT 1.0e18f
constexpr int K2_LINES = 364;
constexpr u32 SPIN_CAP = 1u << 20;

DI float u2f(u32 v) { return __builtin_bit_cast(float, v); }
DI u32 f2u(float v) { return __builtin_bit_cast(u32, v); }
DI int tidx() { return (int)__builtin_amdgcn_workitem_id_x(); }
DI int bidx() { return (int)__builtin_amdgcn_workgroup_id_x(); }
DI float bf2f(u32 v) { return u2f(v << 16); }
DI float lo16(u32 v) { return u2f(v << 16); }
DI float hi16(u32 v) { return u2f(v & 0xffff0000u); }
DI u32 pk2(float a, float b) { f32x2 v = {a, b}; return __builtin_bit_cast(u32, __builtin_convertvector(v, bf16x2)); }
DI u32 pkh2(float a, float b) { f32x2 v = {a, b}; return __builtin_bit_cast(u32, __builtin_convertvector(v, f16x2)); }
DI float hlo(u32 v) { return (float)__builtin_bit_cast(f16x2, v).x; }
DI float hhi(u32 v) { return (float)__builtin_bit_cast(f16x2, v).y; }
DI float rbf(float a) { return lo16(pk2(a, a)); }
DI float fma1(float a, float b, float c) { return __builtin_fmaf(a, b, c); }
DI float mul1(float a, float b) { return a * b; }
DI float add1(float a, float b) { return a + b; }
DI float max3(float a, float b, float c) { return __builtin_fmaxf(__builtin_fmaxf(a, b), c); }
DI float xor32f(float v) {
  auto r = __builtin_amdgcn_permlane32_swap(f2u(v), f2u(v), false, false);
  return u2f((tidx() & 32) ? r[0] : r[1]);
}
DI float sum32(float v) { auto r = __builtin_amdgcn_permlane32_swap(f2u(v), f2u(v), false, false); return u2f(r[0]) + u2f(r[1]); }
DI float sum16(float v) { auto r = __builtin_amdgcn_permlane16_swap(f2u(v), f2u(v), false, false); return u2f(r[0]) + u2f(r[1]); }
DI float max32(float v) { auto r = __builtin_amdgcn_permlane32_swap(f2u(v), f2u(v), false, false); return __builtin_fmaxf(u2f(r[0]), u2f(r[1])); }
template <int CTRL> DI float dppf(float v) { return u2f(__builtin_amdgcn_update_dpp(0u, f2u(v), CTRL, 0xf, 0xf, true)); }

#define MFMA32(a, b, c) __builtin_amdgcn_mfma_f32_32x32x16_bf16(__builtin_bit_cast(bf16x8, a), __builtin_bit_cast(bf16x8, b), c, 0, 0, 0)
#define MFMA32H(a, b, c) __builtin_amdgcn_mfma_f32_32x32x16_f16(__builtin_bit_cast(f16x8, a), __builtin_bit_cast(f16x8, b), c, 0, 0, 0)
#define MFMA16(a, b, c) __builtin_amdgcn_mfma_f32_16x16x32_bf16(__builtin_bit_cast(bf16x8, a), __builtin_bit_cast(bf16x8, b), c, 0, 0, 0)
#define SGB(m, n, id) __builtin_amdgcn_sched_group_barrier(m, n, id)
#define SB() __builtin_amdgcn_sched_barrier(0)
template <bool B> struct BoolC { static constexpr bool v = B; };
template <int I> struct IC { static constexpr int v = I; };
template <int I, int N, class Fn> DI void sfor(Fn&& f) { if constexpr (I < N) { f(IC<I>{}); sfor<I + 1, N>(f); } }
#define PIN(x) asm volatile("" : "+v"(x))
#define WAITV(n) asm volatile("s_waitcnt vmcnt(%0)" ::"i"(n))
#define WAITVM(n) asm volatile("s_waitcnt vmcnt(%0)" ::"i"(n) : "memory")
#define BAR() asm volatile("s_waitcnt lgkmcnt(0)\n\ts_barrier" ::: "memory")
#define RLX_AGENT __ATOMIC_RELAXED, __HIP_MEMORY_SCOPE_AGENT

DI u32x4 lds128(u32 a) { return *(const LAS u32x4*)(size_t)a; }
DI f32x4 ldsf4(u32 a) { return *(const LAS f32x4*)(size_t)a; }
DI u32x2 lds64(u32 a) { return *(const LAS u32x2*)(size_t)a; }
DI float ldsf(u32 a) { return *(const LAS float*)(size_t)a; }
DI void st128(u32 a, u32x4 v) { *(LAS u32x4*)(size_t)a = v; }
DI void stf4(u32 a, f32x4 v) { *(LAS f32x4*)(size_t)a = v; }
DI void st64(u32 a, u32x2 v) { *(LAS u32x2*)(size_t)a = v; }
DI u32x2 ldstr(u32 a) { return __builtin_bit_cast(u32x2, __builtin_amdgcn_ds_read_tr16_b64_v4bf16((LAS bf16x4*)(size_t)a)); }
DI u32x4 ldstr2(u32 a, u32 b) { u32x2 lo = ldstr(a), hi = ldstr(b); return u32x4{lo.x, lo.y, hi.x, hi.y}; }

template <int IMM, bool SC1> DI void dmai(const char* sbase, u32 voff, u32 lds) {
  if constexpr (SC1) asm volatile("s_mov_b32 m0, %2\n\ts_nop 0\n\tglobal_load_lds_dwordx4 %0, %1 offset:%3 sc1" ::"v"(voff), "s"(sbase), "s"(lds), "i"(IMM));
  else asm volatile("s_mov_b32 m0, %2\n\ts_nop 0\n\tglobal_load_lds_dwordx4 %0, %1 offset:%3" ::"v"(voff), "s"(sbase), "s"(lds), "i"(IMM));
}
DI void dma(const char* sbase, u32 voff, u32 lds) { dmai<0, false>(sbase, voff, lds); }
DI void dma_nt(const char* sbase, u32 voff, u32 lds) { asm volatile("s_mov_b32 m0, %2\n\ts_nop 0\n\tglobal_load_lds_dwordx4 %0, %1 nt" ::"v"(voff), "s"(sbase), "s"(lds)); }
DI void gld32(u32& r, const char* sbase, u32 voff) { asm volatile("global_load_dword %0, %1, %2" : "=v"(r) : "v"(voff), "s"(sbase)); }
DI void gld32_sc1(u32& r, const char* sbase, u32 voff) { asm volatile("global_load_dword %0, %1, %2 sc1" : "=v"(r) : "v"(voff), "s"(sbase)); }
template <int IMM> DI void gld_sc1(u32x4& r, const char* sbase, u32 voff) {
  asm volatile("global_load_dwordx4 %0, %1, %2 offset:%3 sc1" : "=v"(r) : "v"(voff), "s"(sbase), "i"(IMM));
}
template <int IMM> DI void gld(u32x4& r, const char* sbase, u32 voff) {
  asm volatile("global_load_dwordx4 %0, %1, %2 offset:%3" : "=v"(r) : "v"(voff), "s"(sbase), "i"(IMM));
}
DI void gld64(u32x2& r, const char* sbase, u32 voff) {
  asm volatile("global_load_dwordx2 %0, %1, %2" : "=v"(r) : "v"(voff), "s"(sbase));
}
DI void poll_ge(const char* ws, u32 off, u32 want) {
  gu32* p = (gu32*)(ws + off);
  for (u32 spins = 0; spins < SPIN_CAP; ++spins) {
    if ((u32)__builtin_amdgcn_readfirstlane(__hip_atomic_load(p, RLX_AGENT)) >= want) break;
    __builtin_amdgcn_s_sleep(4);
  }
  __builtin_amdgcn_fence(__ATOMIC_ACQUIRE, "wavefront");
}
DI void signal(const char* ws, u32 off) { __hip_atomic_fetch_add((gu32*)(ws + off), 1u, RLX_AGENT); }

struct P1 {
  const u16 *x, *wqkv, *wo, *cvec, *wmod;
  char* ws;
};
#define K1AUX 0
__global__ void __launch_bounds__(256) NOPK k1(P1 p) {
  __shared__ float red[32 * 64 + 4 * 64 + 320];
  const int b = bidx(), t = tidx();
  char* ws = p.ws;
  const __amdgpu_buffer_rsrc_t rs = __builtin_amdgcn_make_buffer_rsrc(p.ws, 0, WS_END, 0x00020000);
  if (b >= 408 && b < 424) {
    unsigned long long pc;
    asm volatile("s_getpc_b64 s[40:41]\n\ts_add_u32 s40, s40, _ZN12_GLOBAL__N_12k2ENS_2P2E@rel32@lo+4\n\ts_addc_u32 s41, s41, _ZN12_GLOBAL__N_12k2ENS_2P2E@rel32@hi+12\n\ts_mov_b64 %0, s[40:41]"
                 : "=s"(pc) : : "scc", "s40", "s41");
    const int o = ((b - 408) >> 3) * 256 + t;
    if (o < K2_LINES) { u32 v_ = *(const GAS u32*)(pc + (size_t)o * 128); asm volatile("" ::"v"(v_)); }
  }
  if (b < 120) {
    const int ch = t & 7, r = t >> 3, col0 = 64 * (b >> 1), row0 = 320 * (b & 1);
    const u32x4 cv = *(const u32x4*)(p.cvec + row0 + 8 * (t < 40 ? t : 39));
    u32x4 wv[10];
#pragma unroll
    for (int i = 0; i < 10; ++i) wv[i] = __builtin_nontemporal_load((const u32x4*)(p.wmod + (size_t)(row0 + r + 32 * i) * 3840 + col0 + 8 * ch));
    if (t < 40) {
#pragma unroll
      for (int e = 0; e < 8; ++e) {
        const float c = (e & 1) ? hi16(cv[e >> 1]) : lo16(cv[e >> 1]);
        red[2304 + 8 * t + e] = rbf(c * __builtin_amdgcn_rcpf(1.f + __builtin_amdgcn_exp2f(-1.4426950408889634f * c)));
      }
    }
    BAR();
    float acc[8] = {0, 0, 0, 0, 0, 0, 0, 0};
#pragma unroll
    for (int i = 0; i < 10; ++i) {
      const float s = red[2304 + r + 32 * i];
#pragma unroll
      for (int e = 0; e < 4; ++e) { acc[2 * e] = fma1(s, lo16(wv[i][e]), acc[2 * e]); acc[2 * e + 1] = fma1(s, hi16(wv[i][e]), acc[2 * e + 1]); }
    }
    *(f32x4*)&red[r * 64 + 8 * ch] = f32x4{acc[0], acc[1], acc[2], acc[3]};
    *(f32x4*)&red[r * 64 + 8 * ch + 4] = f32x4{acc[4], acc[5], acc[6], acc[7]};
    BAR();
    { const int c = t & 63, part = t >> 6; float s = 0;
#pragma unroll
      for (int i = 0; i < 8; ++i) s += red[(part * 8 + i) * 64 + c];
      red[2048 + part * 64 + c] = s; }
    BAR();
    if (t < 64) {
      const float s = (red[2048 + t] + red[2112 + t]) + (red[2176 + t] + red[2240 + t]);
      __builtin_amdgcn_raw_buffer_store_b32(f2u(s), rs, WS_MODP + ((b & 1) * 3840 + col0 + t) * 4, 0, K1AUX);
    }
    if (b == 0) {
      *(u32*)(ws + WS_KVF + t * 128) = 0u;
      if (t < 72) *(u32*)(ws + WS_CNT + t * 256) = 0u;
    }
  } else if (b < 248) {
    const int hb = b - 120, c = t & 15, tok = 16 * (hb & 1) + (t >> 4);
    const u16* row = p.x + (size_t)(32 * (hb >> 1) + tok) * D;
    u32x4 v[5];
#pragma unroll
    for (int i = 0; i < 5; ++i) v[i] = *(const u32x4*)(row + 8 * (c + 16 * i));
    float s = 0;
#pragma unroll
    for (int i = 0; i < 5; ++i)
#pragma unroll
      for (int e = 0; e < 4; ++e) s += lo16(v[i][e]) + hi16(v[i][e]);
    s += dppf<0xB1>(s); s += dppf<0x4E>(s); s += dppf<0x141>(s); s += dppf<0x140>(s);
    const float mean = s * (1.f / 640.f);
    float q = 0;
#pragma unroll
    for (int i = 0; i < 5; ++i)
#pragma unroll
      for (int e = 0; e < 4; ++e) { const float a = lo16(v[i][e]) - mean, bb = hi16(v[i][e]) - mean; q = fma1(a, a, q); q = fma1(bb, bb, q); }
    q += dppf<0xB1>(q); q += dppf<0x4E>(q); q += dppf<0x141>(q); q += dppf<0x140>(q);
    const float mb = rbf(mean), den = rbf(__builtin_amdgcn_sqrtf(rbf(rbf(q * (1.f / 640.f)) + 1e-5f))), rr = __builtin_amdgcn_rcpf(den);
    const u32 dst = WS_N1 + (u32)(hb >> 1) * 40960 + tok * 16;
#pragma unroll
    for (int i = 0; i < 5; ++i) {
      u32x4 o;
#pragma unroll
      for (int e = 0; e < 4; ++e) {
        const u32 d = pk2(lo16(v[i][e]) - mb, hi16(v[i][e]) - mb);
        const u32 n = pk2(lo16(d) * rr, hi16(d) * rr);
        o[e] = pkh2(lo16(n), hi16(n));
      }
      __builtin_amdgcn_raw_buffer_store_b128(o, rs, dst + (c + 16 * i) * 512, 0, K1AUX);
    }
  } else {
    const u16* src; int stride; u32 off0, off1; bool act = true;
    if (b < 408) {
      const int idx = b - 248, ks = idx >> 2, jq = idx & 3, tc = t < 240 ? t : 239;
      act = t < 240;
      const int seg = tc / 80, cp = tc - 80 * seg;
      src = p.wqkv + (size_t)(16 * ks) * 1920 + 640 * seg + 160 * jq + 2 * cp; stride = 1920;
      const int jj = (2 * cp) / 40, within = 2 * cp - 40 * jj, j = 4 * jq + jj;
      const int n = (seg == 1 ? 0 : seg == 2 ? 40 : 80) + within, nb = n >> 5, r = n & 31;
      off0 = WS_WQF + j * WQB + (nb < 3 ? ((nb * 40 + ks) * 64 + r) * 16 : 122880 + ks * 768 + r * 16);
      off1 = off0 + (nb < 3 ? 512 : 384);
    } else {
      const int idx = b - 408, k16 = idx >> 1, tc = t < 160 ? t : 159, n = (idx & 1) * 320 + 2 * tc;
      act = t < 160;
      src = p.wo + (size_t)(16 * k16) * 640 + n; stride = 640;
      const int kh = k16 >= 20, kk = k16 - 20 * kh, st = kk >> 1, H = kk & 1;
      const int ntg = n >> 4, n8 = ntg / 5, nt = ntg - 5 * n8;
      off0 = WS_WOF + n8 * WOB + (st * 10 + kh * 5 + nt) * 1024 + (2 * H * 16 + (n & 15)) * 16;
      off1 = off0 + 256;
    }
    u32 v[16];
#pragma unroll
    for (int i = 0; i < 16; ++i) v[i] = __builtin_nontemporal_load((const u32*)(src + (size_t)i * stride));
    if (b < 408) {
#pragma unroll
      for (int i = 0; i < 16; ++i) v[i] = pkh2(lo16(v[i]) * 64.f, hi16(v[i]) * 64.f);
    }
    if (act) {
#pragma unroll
      for (int h = 0; h < 2; ++h) {
        u32x4 a, bq;
#pragma unroll
        for (int e = 0; e < 4; ++e) {
          a[e] = __builtin_amdgcn_perm(v[8 * h + 2 * e + 1], v[8 * h + 2 * e], 0x05040100u);
          bq[e] = __builtin_amdgcn_perm(v[8 * h + 2 * e + 1], v[8 * h + 2 * e], 0x07060302u);
        }
        __builtin_amdgcn_raw_buffer_store_b128(a, rs, h ? off1 : off0, 0, K1AUX);
        __builtin_amdgcn_raw_buffer_store_b128(bq, rs, (h ? off1 : off0) + 16, 0, K1AUX);
      }
    }
  }
}

struct P2 {
  const u16 *x, *wg, *wu, *wd;
  u16* out;
  char* ws;
};
#define MAP 0
constexpr u32 C_O = WS_CNT, C_RG = WS_CNT + 48 * 256, C_RD = WS_CNT + 64 * 256;
#define OPQ(x) asm volatile("" : "+s"(x))

DI constexpr int gu_unit(int p, int kh, int i) {
  return p == 0 ? 20 * kh + i
       : p == 1 ? (i < 16 ? 40 + 16 * kh + i : 8 + 20 * kh + (i - 16))
                : (i < 8 ? 12 + 20 * kh + i : 40 + 16 * kh + (i - 8));
}
DI constexpr u32 wo_slot(int s) { return s < 7 ? (72 + 10 * s) * 1024 : (s - 7) * 10240; }
DI constexpr u32 wd_slot(int s) { return (s < 15 ? s : s - 15) * 10240; }
template <int N, int NW, bool SC1> struct Run {
  static constexpr int C = (N + NW - 1) / NW;
  const char* src; u32 lds;
  DI Run(const char* s, u32 l, int x) { int o = x * C; o = (o < N - C ? o : N - C) * 1024; src = s + o; lds = l + o; }
  template <int M> DI void go(u32 l16) const { if constexpr (M < C) dmai<(M & 3) * 1024, SC1>(src + (M >> 2) * 4096, l16, lds + (M >> 2) * 4096); }
  DI void all(u32 l16) const { sfor<0, C>([&](auto m_) LAM { go<decltype(m_)::v>(l16); }); }
};
template <int P, int I0, int I1, int NW> DI auto gu_run(const char* blob, int x) {
  const int k = x >= NW / 2;
  return Run<2 * (I1 - I0), NW / 2, true>(blob + ((P * 2 + k) * 20 + I0) * 2048, (k ? gu_unit(P, 1, I0) : gu_unit(P, 0, I0)) * 2048, x - k * (NW / 2));
}
DI u32 pkfma16(u32 x, u32 y, u32 z) { return __builtin_bit_cast(u32, __builtin_elementwise_fma(__builtin_bit_cast(f16x2, x), __builtin_bit_cast(f16x2, y), __builtin_bit_cast(f16x2, z))); }
DI void gld4_if(int cond, u32x4& r0, u32x4& r1, u32x4& r2, u32x4& r3, const char* sbase, u32 voff) {
  asm volatile("s_cmp_eq_u32 %6, 0\n\ts_cbranch_scc1 .Lskip%=\n\tglobal_load_dwordx4 %0, %4, %5\n\tglobal_load_dwordx4 %1, %4, %5 offset:1024\n\t"
               "global_load_dwordx4 %2, %4, %5 offset:2048\n\tglobal_load_dwordx4 %3, %4, %5 offset:3072\n.Lskip%=:"
               : "+v"(r0), "+v"(r1), "+v"(r2), "+v"(r3) : "v"(voff), "s"(sbase), "s"(cond) : "scc");
}
#define NRS 12
DI constexpr int dn_nA(int s) { return s < 3 ? 3 : s == 8 || s == 10 || s == 13 ? 3 : 0; }
DI constexpr int dn_nD(int s) { return s >= 5 && s < 8 ? 4 : s == 8 ? 1 : s >= 9 && s < 14 ? 2 : 0; }
DI constexpr int dn_cumA(int s) { int c = 6; for (int u = 0; u < s; ++u) c += dn_nA(u); return c; }
DI constexpr int dn_young(int s, bool old, int a_idx, int d_step) {
  int pos = 0, tgt = -1, na = 6;
  if (a_idx >= 0 && a_idx < 6) tgt = a_idx;
  pos = 6;
  for (int u = 0; u < s; ++u) {
    if (old) { pos += dn_nD(u); if (u == d_step) tgt = pos - 1; }
    for (int q = 0; q < dn_nA(u); ++q) { if (na == a_idx) tgt = pos; ++na; ++pos; }
  }
  return tgt < 0 ? 63 : pos - 1 - tgt;
}
DI constexpr int dn_wait(int s, bool old) {
  const int x = dn_young(s, old, s, -1), y = s == 13 ? dn_young(s, old, -1, 8) : s == 18 ? dn_young(s, old, -1, 13) : 63;
  return x < y ? x : y;
}
#define OWARM 0
#define RPOS 0
#define SENT 0
DI bool has_sent(const u32x4& v) {
  const u32 m = __builtin_elementwise_max(__builtin_elementwise_max(v.x, v.y), __builtin_elementwise_max(v.z, v.w));
  return __builtin_amdgcn_ballot_w64(m == 0xffffffffu) != 0ull;
}
template <int IMM> DI void ensure(u32x4& v, const char* sbase, u32 voff) {
  if (SENT && __builtin_expect(has_sent(v), 0)) {
    for (u32 spins = 0; spins < SPIN_CAP; ++spins) {
      gld_sc1<IMM>(v, sbase, voff); WAITV(0); PIN(v);
      if (!has_sent(v)) break;
    }
  }
}
DI void gate(const char* sbase, u32 voff) {
  for (u32 spins = 0; spins < SPIN_CAP; ++spins) {
    u32 v;
    gld32_sc1(v, sbase, voff); WAITV(0); PIN(v);
    if (__builtin_amdgcn_ballot_w64(v == 0xffffffffu) == 0ull) break;
    __builtin_amdgcn_s_sleep(3);
  }
}

__global__ void __launch_bounds__(512, 2) NOPK k2(P2 p) {
  const int tid = tidx(), lane = tid & 63;
  int w = __builtin_amdgcn_readfirstlane(tid >> 6);
  const int b = bidx();
  int mt = MAP == 0 ? 4 * ((b & 7) >> 1) + (b >> 6) : (b & 7) * 2 + (b >> 7), j = MAP == 0 ? 8 * (b & 1) + ((b >> 3) & 7) : (b >> 3) & 15;
  const int c32 = lane & 31, h = lane >> 5;
  const char* ws = p.ws;
  const __amdgpu_buffer_rsrc_t rs = __builtin_amdgcn_make_buffer_rsrc(p.ws, 0, WS_END, 0x00020000);
  const u32 l16 = lane * 16;
#define PHASE() OPQ(w); OPQ(mt); OPQ(j); OPQ(ws); const int tt = w & 3, kh = w >> 2, th = j & 1, n8 = j >> 1; (void)tt; (void)kh; (void)th; (void)n8
#define FINE 0
#define CSEL 0
#define FST(k, i) do {} while (0)
#define STAMP(i) do {} while (0)
#define CST(k, i) do {} while (0)
#define PRIO 0
  if (PRIO == 1 && w >= 4) __builtin_amdgcn_s_setprio(1);
  STAMP(0); CST(3, 18);
  u32x4 a[24];
  u32x4 st[18];
  const int rsw = c32 ^ (16 * (w >> 2));
  const u32 frow = (h * 32 + rsw) * 16;
  u32 imga[8];
  {
    PHASE();
    const char* wq = ws + WS_WQF + (size_t)j * WQB;
    const char* wqw = wq + (kh * 20 + tt) * 1024;
    const u32 wql = (kh * 20 + tt) * 1024;
    const char* n1 = ws + WS_N1 + (size_t)((mt * 4 + tt) * 40 + kh * 20) * 1024;
    auto issA = [&](auto k_) LAM { constexpr int k = decltype(k_)::v; gld<(k & 3) * 1024>(a[k], n1 + (k >> 2) * 4096, l16); };
    auto issW = [&](int s, int nb) LAM { dma(wqw + nb * 40960 + s * 4096, l16, wql + nb * 40960 + s * 4096); };
    const int rk16 = w < 5 ? 5 * (mt >> 1) + w : (mt * 2 + th) * 3 + (w - 5);
    const char* rsrc = w < 5 ? (const char*)((mt & 1) ? p.wu : p.wg) + (size_t)rk16 * 49152 + 192 * j : (const char*)p.wd + (size_t)rk16 * 20480 + 160 * n8;
    const u32 rimg = 122880 + w * 3072;
    auto issR = [&]() LAM {
#pragma unroll
      for (int m = 0; m < 3; ++m) {
        const u32 x = 64 * m + lane, i5 = x < 160 ? x : 159, r12 = (x * 171) >> 11, r10 = (i5 * 205) >> 11;
        dma(rsrc, w < 5 ? r12 * 2880 + x * 16 : r10 * 1120 + i5 * 16, rimg + m * 1024);
      }
    };
    if (RPOS == 1) issR();
    u32x4 mp[2];
    u32 mdst; bool plus1;
    {
      const int m1 = tid >= 160, k1 = 4 * (tid - 160 * m1);
      const int u2 = tid - 320, m2 = (u2 * 205) >> 12, e2 = u2 - 20 * m2;
      const int col2 = (m2 == 0 ? 1280 : m2 == 2 ? 3200 : m2 == 4 ? 1920 : 2560) + 80 * (m2 < 3 ? n8 : (mt >> 1)) + 4 * (m2 < 5 ? e2 : 0);
      const int col = tid < 320 ? 640 * m1 + k1 : col2;
#pragma unroll
      for (int q = 0; q < 2; ++q) gld<0>(mp[q], ws + WS_MODP + q * 15360, (u32)col * 4);
      sfor<0, 4>(issA);
      issW(0, 0); issW(0, 1); issW(0, 2);
      if (RPOS == 2) issR();
      mdst = tid < 320 ? L_GS + (k1 >> 3) * 32 + (m1 ? 0 : 16) + (k1 & 4) * 2 : L_MODS + u2 * 16;
      plus1 = tid < 320 ? m1 != 0 : (m2 & 1) != 0;
    }
    if (tid == 0) st128(L_FLAG, u32x4{0u, 0u, 0u, 0u});
    f32x16 acc[3];
    const u32 wb = kh * 20480 + frow, gb_ = L_GS + kh * 1280 + h * 32;
    u32x4 gs[3][2], wf[2][3], ro[3];
    f32x4 csum = {0.f, 0.f, 0.f, 0.f};
    auto ldgs = [&](int k) LAM { gs[k % 3][0] = lds128(gb_ + k * 64); gs[k % 3][1] = lds128(gb_ + k * 64 + 16); };
    auto modulate = [&](int k) LAM {
      PIN(a[k]);
#pragma unroll
      for (int e = 0; e < 4; ++e)
        a[k][e] = pkfma16(a[k][e], gs[k % 3][0][e], gs[k % 3][1][e]);
    };
    FST(3, 1);
    WAITV(RPOS == 2 ? 10 : 7);
    FST(3, 2);
    f32x4 mv;
    {
#pragma unroll
      for (int q = 0; q < 2; ++q) PIN(mp[q]);
      float mxv = 0.f;
#pragma unroll
      for (int e = 0; e < 4; ++e) {
        const float s = rbf(u2f(mp[0][e]) + u2f(mp[1][e]));
        mv[e] = plus1 ? rbf(1.f + s) : s;
        mxv = __builtin_fmaxf(mxv, __builtin_fabsf(mv[e]));
      }
      if (tid < 320) st64(mdst, u32x2{pkh2(mv[0], mv[1]), pkh2(mv[2], mv[3])}); else if (tid < 420) stf4(mdst, mv);
      const bool big = __builtin_amdgcn_ballot_w64(tid < 320 && !(mxv < 2000.f)) != 0ull;
      if (lane == 0) *(LAS u32*)(size_t)(L_FLAG + 16 + w * 4) = big;
    }
    WAITV(RPOS == 2 ? 3 : 0);
    BAR();
    FST(3, 3);
    auto grpA = [&](int cond, int s) LAM { gld4_if(cond, a[4 * s], a[4 * s + 1], a[4 * s + 2], a[4 * s + 3], n1 + s * 4096, l16); };
    const int kl = kh ^ 1;
    auto grpW = [&](int s) LAM { issW(s, 0); issW(s, 1); issW(s, 2); };
    grpA(kh, 1); if (kh) { grpW(1); if (RPOS == 0) issR(); }
    bool scaled;
    {
      const u32x4 f0 = lds128(L_FLAG + 16);
      const u32 f1 = *(const LAS u32*)(size_t)(L_FLAG + 32);
      ldgs(0); ldgs(1);
      scaled = __builtin_amdgcn_readfirstlane(f0.x | f0.y | f0.z | f0.w | f1) != 0;
      if (scaled) {
        BAR();
        const float sc = 9.765625e-4f;
        if (tid < 320) st64(mdst, u32x2{pkh2(mv[0] * sc, mv[1] * sc), pkh2(mv[2] * sc, mv[3] * sc)});
        BAR();
        ldgs(0); ldgs(1);
      }
    }
    modulate(0);
    SB();
    const Run<30, 8, false> rb3(wq + 122880, 122880, w);
    sfor<0, 21>([&](auto k_) LAM {
      constexpr int k = decltype(k_)::v, s = k >> 2, i = k & 3;
      if constexpr (i == 3 && k < 16) WAITV(k == 3 && RPOS == 0 ? 13 : 10);
      if constexpr (i == 0 && k > 0 && k < 20) {
        FST(3, 3 + 3 * s); WAITV(k == 4 && RPOS == 0 ? 10 : k == 16 ? 4 : 7); FST(3, 4 + 3 * s); BAR(); FST(3, 5 + 3 * s);
        if (s == 2 && CSEL == 4) { CST(4, 18); STAMP(16); } if (s == 3 && CSEL == 4) { CST(4, 19); STAMP(17); }
      }
      if constexpr (k < 20) {
#pragma unroll
        for (int nb = 0; nb < 3; ++nb) wf[k & 1][nb] = lds128(wb + nb * 40960 + k * 1024);
      }
      if constexpr (k == 1) {
        f32x16 z;
#pragma unroll
        for (int e = 0; e < 16; ++e) z[e] = 0.f;
#pragma unroll
        for (int nb = 0; nb < 3; ++nb) acc[nb] = MFMA32H(wf[0][nb], a[0], z);
      } else if constexpr (k > 1) {
#pragma unroll
        for (int nb = 0; nb < 3; ++nb) acc[nb] = MFMA32H(wf[(k - 1) & 1][nb], a[k - 1], acc[nb]);
      }
      if constexpr (k + 1 < 20) modulate(k + 1);
      if constexpr (k + 2 < 20) ldgs(k + 2);
      if constexpr (k == 8) {
        const u32 q4 = (lane >> 2) & 3, nh = (lane >> 4) & 1;
        const u32 r0 = rimg + (w < 5 ? (8 * h + q4) * 192 + 32 * nh : (4 * nh + q4) * 160 + 32 * h) + (lane & 3) * 8, r1 = r0 + (w < 5 ? 768 : 1280);
#pragma unroll
        for (int u = 0; u < 3; ++u) ro[u] = ldstr2(r0 + 64 * u, r1 + 64 * u);
      }
      if constexpr (k == 9 || k == 10) {
        if (w < 5) {
          const u32 ga_ = L_MODS + 960 + w * 64 + h * 32;
          const f32x4 g0 = ldsf4(ga_), g1 = ldsf4(ga_ + 16), s0 = ldsf4(ga_ + 320), s1 = ldsf4(ga_ + 336);
#pragma unroll
          for (int u = (k == 9 ? 0 : 2); u < (k == 9 ? 2 : 3); ++u) {
            float c = 0.f, bi = 0.f;
#pragma unroll
            for (int e = 0; e < 4; ++e) {
              const u32 v = ro[u][e];
              const float x0 = lo16(v), x1 = hi16(v), ge = e < 2 ? g0[2 * e] : g1[2 * e - 4], go = e < 2 ? g0[2 * e + 1] : g1[2 * e - 3];
              const float se = e < 2 ? s0[2 * e] : s1[2 * e - 4], so_ = e < 2 ? s0[2 * e + 1] : s1[2 * e - 3];
              c = fma1(ge, x0, c); c = fma1(go, x1, c); bi = fma1(se, x0, bi); bi = fma1(so_, x1, bi);
            }
            auto r = __builtin_amdgcn_permlane32_swap(f2u(c), f2u(bi), false, false);
            *(LAS float*)(size_t)(w * 768 + u * 256 + (c32 >> 3) * 64 + h * 32 + (c32 & 7) * 4) = u2f(r[0]) + u2f(r[1]);
          }
        }
      }
      if constexpr (k == 17) {
        if (w == 7) {
#pragma unroll
          for (int u = 0; u < 5; ++u) csum += ldsf4(u * 768 + (lane < 48 ? l16 : 0u));
        }
      }
      if constexpr (k == 16) {
        const u32 x2 = 16 * kh + 4 * h, tb = 12288 + (32 * tt + c32) * 48 + 2 * x2;
#pragma unroll
        for (int u = 0; u < 8; ++u) {
          const u32 part = ((x2 + 32 * (u >> 1) + 8 * (u & 1)) * 205) >> 12;
          imga[u] = part * 18392 + tb + 64 * (u >> 1) + 16 * (u & 1); PIN(imga[u]);
        }
      }
      SB();
      if constexpr (k == 0) { grpA(kl, 1); if (!kh) { grpW(1); if (RPOS == 0) issR(); } }
      if constexpr (k == 1) grpA(kl, 2);
      if constexpr (k == 2) { if (!kh) grpW(2); grpA(kh, 2); }
      if constexpr (k == 3) { if (kh) grpW(2); }
      if constexpr (s == 1 || s == 2) {
        if constexpr (i == 0) grpA(kl, s + 2);
        if constexpr (i == 1) { if (!kh) grpW(s + 2); }
        if constexpr (i == 2) grpA(kh, s + 2);
        if constexpr (i == 3) { if (kh) grpW(s + 2); }
      }
      if constexpr (k == 12) { if (!kh) rb3.all(l16); }
      if constexpr (k == 14) { if (kh) rb3.all(l16); }
      if constexpr (k == 16) {
        u32 off0, du;
        if (w < 5) { const int k2 = rk16 >= 20, i2 = rk16 - 20 * k2; off0 = WS_WGF + j * WGB + (k2 * 20 + i2) * 2048 + (mt & 1) * 1024 + l16; du = 81920; }
        else { const int k2 = rk16 >= 48, kk = rk16 - 48 * k2; off0 = WS_WDF + n8 * WDB + ((kk >> 1) * 10 + k2 * 5 + h) * 1024 + (kk & 1) * 512 + c32 * 16; du = 2048; }
        asm volatile("" ::: "memory");
        __builtin_amdgcn_raw_buffer_store_b128(ro[0], rs, off0, 0, 16);
        __builtin_amdgcn_raw_buffer_store_b128(ro[1], rs, off0 + du, 0, 16);
        __builtin_amdgcn_raw_buffer_store_b128(ro[2], rs, (w < 5 || h == 0) ? off0 + 2 * du : 0xfffffff0u, 0, 16);
        asm volatile("" ::: "memory");
      }
      SB();
    });
    STAMP(1); CST(3, 19); FST(6, 1);
    WAITV(3);
    BAR();
    FST(6, 2);
    if (w == 7 && lane < 48) __builtin_amdgcn_raw_buffer_store_b128(__builtin_bit_cast(u32x4, csum), rs, WS_CSP + (u32)(j * 16 + mt) * 768 + l16, 0, 16);
    const u32 exw = (w >> 1) * 18432 + (w & 1) * 6144 + l16, exr = ((w ^ 4) >> 1) * 18432 + (w & 1) * 6144 + l16;
#pragma unroll
    for (int nb = 0; nb < 3; ++nb) {
      stf4(exw + nb * 2048, f32x4{acc[nb][8], acc[nb][9], acc[nb][10], acc[nb][11]});
      stf4(exw + nb * 2048 + 1024, f32x4{acc[nb][12], acc[nb][13], acc[nb][14], acc[nb][15]});
    }
    if (kh) st64((tid >> 7) * 18432 + 12288 + (tid & 127) * 48 + 40, u32x2{0x3f803f80u, 0x3f803f80u});
    BAR();
    FST(6, 3);
    const float osc = scaled ? 1024.f : 1.f, qs = QSCALE * (1.f / 4096.f) * osc, sc2 = kh ? qs : osc;
#pragma unroll
    for (int nb = 0; nb < 3; ++nb)
#pragma unroll
      for (int q = 0; q < 2; ++q) {
        const f32x4 o = ldsf4(exr + nb * 2048 + q * 1024);
        float v0 = acc[nb][4 * q] + o[0], v1 = acc[nb][4 * q + 1] + o[1], v2 = acc[nb][4 * q + 2] + o[2], v3 = acc[nb][4 * q + 3] + o[3];
        if (nb == 2) { v0 *= sc2; v1 *= sc2; v2 *= sc2; v3 *= sc2; }
        else if (scaled) { v0 *= osc; v1 *= osc; v2 *= osc; v3 *= osc; }
        st64(imga[2 * nb + q], u32x2{pk2(v0, v1), pk2(v2, v3)});
      }
    BAR();
    FST(6, 4);
    {
      const u32 dst = WS_KVX + (u32)(mt * 16 + j) * KVB + w * 3072 + l16, src = (w >> 1) * 18432 + 12288 + (w & 1) * 3072 + l16;
#pragma unroll
      for (int i = 0; i < 3; ++i) __builtin_amdgcn_raw_buffer_store_b128(lds128(src + i * 1024), rs, dst + i * 1024, 0, 16);
    }
    f32x16 acc3;
#pragma unroll
    for (int e = 0; e < 16; ++e) acc3[e] = 0.f;
    {
      const int r3 = rsw < 24 ? rsw : (rsw < 28 ? rsw - 4 : rsw - 12);
      const u32 wb3 = 122880 + kh * 15360 + (h * 24 + r3) * 16;
      u32x4 w3[4];
#pragma unroll
      for (int k = 0; k < 3; ++k) w3[k] = lds128(wb3 + k * 768);
#pragma unroll
      for (int k = 0; k < 20; ++k) {
        if (k + 3 < 20) w3[(k + 3) & 3] = lds128(wb3 + (k + 3) * 768);
        acc3 = MFMA32H(w3[k & 3], a[k], acc3);
        if (k == 9) {
          FST(6, 5);
          WAITVM(0);
          FST(6, 6);
          if (lane == 0) {
            const u32 old = __hip_atomic_fetch_add((LAS u32*)(size_t)L_FLAG, 1u, __ATOMIC_RELAXED, __HIP_MEMORY_SCOPE_WORKGROUP);
            if (old == 7u) {
              __hip_atomic_store((gu32*)(ws + WS_KVF + (mt * 16 + j) * 128), 1u, RLX_AGENT);
              signal(ws, C_RG + j * 256); signal(ws, C_RD + n8 * 256);
            }
          }
        }
        SB();
      }
    }
    const u32 ex3w = (w < 4 ? 73728 : 84992) + w * 2048 + l16, ex3r = (w < 4 ? 93184 : 65536) + w * 2048 + l16;
    stf4(ex3w, f32x4{acc3[8], acc3[9], acc3[10], acc3[11]});
    stf4(ex3w + 1024, f32x4{acc3[12], acc3[13], acc3[14], acc3[15]});
    FST(6, 7);
    BAR();
    FST(6, 8);
    STAMP(2);
#pragma unroll
    for (int q = 0; q < 2; ++q) {
      const f32x4 o = ldsf4(ex3r + q * 1024);
      st64(imga[6 + q], u32x2{pk2((acc3[4 * q] + o[0]) * qs, (acc3[4 * q + 1] + o[1]) * qs), pk2((acc3[4 * q + 2] + o[2]) * qs, (acc3[4 * q + 3] + o[3]) * qs)});
    }
    BAR();
    FST(6, 9);
  }
  u32x2 xr[3];
  const int c16 = lane & 15, g4 = lane >> 4;
  {
    PHASE();
    const int hh = w >> 2, qb = w < 4 ? w : 7 - w;
    u32x4 q0f, q1f;
    {
      const u32 qa = (4 + hh) * 18432 + 12288 + (32 * qb + c32) * 48;
      const u32x2 x2 = lds64(qa + 32);
      q0f = lds128(qa + 16 * h);
      q1f = u32x4{h ? 0u : x2.x, h ? 0u : x2.y, 0u, 0u};
      const int tok64 = 128 * mt + 64 * th + 16 * tt + c16;
#pragma unroll
      for (int m = 0; m < 3; ++m) gld64(xr[m], (const char*)p.x, (u32)(tok64 * 1280 + (80 * n8 + 16 * ((m + 3 * kh) % 5) + 4 * g4) * 2));
    }
    BAR();
    STAMP(3); STAMP(4); CST(1, 18); FST(4, 12);
    const char* sg0 = ws + WS_WGF + (size_t)j * WGB + (kh * 40 + tt * 10) * 1024;
    const int tmin = mt >= 2 ? 0 : 8 - 4 * mt - qb;
    const u32 kb = hh * 18432 + (32 * qb + c32) * 48, kb2 = kb + 16 * h;
    const u32 vb = (2 + hh) * 18432 + (32 * qb + 4 * h + ((lane >> 2) & 3)) * 48 + ((lane >> 4) & 1) * 32 + (lane & 3) * 8;
    const int qlim = c32 - 4 * h;
    const int dw = (w - 2) & 3;
    const bool duty = w >= 2 && w <= 5;
    const Run<70, 4, false> rwo(ws + WS_WOF + (size_t)n8 * WOB, wo_slot(0), dw);
    const u32 fa = WS_ACT + (u32)((mt * 4 + dw) * 96 + 6 * j) * 1024 + l16;
    const u32 fx = dw < 2 ? WS_X1 + (u32)((mt * 4 + 2 * th + dw) * 40 + 5 * n8) * 1024 + l16 : WS_STAT + (u32)((mt * 4 + 2 * th + h) * 8 + n8) * 512 + c32 * 16;
    u32 ob = WS_OAF + (u32)((mt * 4 + qb) * 40) * 1024 + c32 * 16 + 5 * j * 512;
    PIN(ob);
    auto lds_wait = [&](u32 addr, u32 want) LAM {
      for (u32 spins = 0; spins < SPIN_CAP * 8; ++spins) {
        if ((u32)__builtin_amdgcn_readfirstlane(__hip_atomic_load((LAS u32*)(size_t)addr, __ATOMIC_RELAXED, __HIP_MEMORY_SCOPE_WORKGROUP)) >= want) break;
        __builtin_amdgcn_s_sleep(1);
      }
      asm volatile("" ::: "memory");
    };
    {
      if (!duty) {
        const int sl = w >= 6;
        const u32 l0 = (w & 1) * 36864 + sl * 6144;
        if (mt + sl >= 2) {
          poll_ge(ws, WS_KVF + ((mt - 2 + sl) * 16 + j) * 128, 1u);
          const char* src = ws + WS_KVX + (size_t)((mt - 2 + sl) * 16 + j) * KVB + (w & 1) * 12288;
#pragma unroll
          for (int u = 0; u < 4; ++u) {
            const u32 l1 = l0 + (u >> 1) * 18432 + (u & 1) * 3072;
            dmai<0, true>(src + u * 3072, l16, l1); dmai<1024, true>(src + u * 3072, l16, l1); dmai<2048, true>(src + u * 3072, l16, l1);
          }
        } else {
#pragma unroll
          for (int u = 0; u < 12; ++u) st128(l0 + (u / 6) * 18432 + (u % 6) * 1024 + l16, u32x4{0u, 0u, 0u, 0u});
        }
      }
    }
    auto halo = [&]() LAM {
      if (!duty) {
        asm volatile("s_waitcnt vmcnt(0) lgkmcnt(0)" ::: "memory");
        if (lane == 0) __hip_atomic_fetch_add((LAS u32*)(size_t)(L_FLAG + (w >= 6 ? 8 : 4)), 1u, __ATOMIC_RELAXED, __HIP_MEMORY_SCOPE_WORKGROUP);
      }
    };
    f32x16 oacc;
    u32 rcnt;
    auto attn = [&](auto full_) LAM {
      constexpr bool FULL = decltype(full_)::v;
      u32x4 ka, kq, vf[2][2];
      kq = u32x4{0u, 0u, 0u, 0u};
      auto ldkv = [&](int t) LAM {
        const u32x2 x2 = lds64(kb + t * 1536 + 32);
        ka = lds128(kb2 + t * 1536); kq.x = x2.x; kq.y = x2.y;
#pragma unroll
        for (int s2 = 0; s2 < 2; ++s2) { const u32 va = vb + (32 * t + 16 * s2) * 48; vf[t & 1][s2] = ldstr2(va, va + 384); }
      };
      auto splat = [&](float v) LAM -> f32x16 {
        f32x16 c;
#pragma unroll
        for (int e = 0; e < 16; ++e) c[e] = v;
        return c;
      };
      auto pre = [&](auto m_) LAM {
        constexpr int m = decltype(m_)::v;
        if constexpr (m == 2) gld32_sc1(rcnt, ws, lane < 32 ? C_RG + j * 256 : C_RD + n8 * 256);
        if constexpr (m == 4) {
          if (duty) WAITV(SENT ? 6 : 4); else WAITV(0);
          PIN(rcnt);
          if (__builtin_amdgcn_ballot_w64(rcnt < (lane < 32 ? 16u : 32u)) != 0ull) { poll_ge(ws, C_RG + j * 256, 16u); poll_ge(ws, C_RD + n8 * 256, 32u); }
        }
        if constexpr (m >= 4) {
          gld_sc1<((2 * m - 8) & 3) * 1024>(st[2 * m - 8], sg0 + ((2 * m - 8) >> 2) * 4096, l16);
          gld_sc1<((2 * m - 7) & 3) * 1024>(st[2 * m - 7], sg0 + ((2 * m - 7) >> 2) * 4096, l16);
        }
        if (duty) {
          rwo.go<2 * m>(l16); rwo.go<2 * m + 1>(l16);
          const u32x4 f = {~0u, ~0u, ~0u, ~0u};
          if constexpr (m < 6 && SENT) __builtin_amdgcn_raw_buffer_store_b128(f, rs, fa + m * 1024, 0, 16);
          if constexpr (m >= 4 && SENT) { if (dw < 2 || (dw == 2 && m == 4)) __builtin_amdgcn_raw_buffer_store_b128(f, rs, fx + (m - 4) * 1024, 0, 16); }
          asm volatile("" ::: "memory");
        }
        SB();
      };
      auto sync = [&](int t) LAM {
        if (t >= 4) { if (qb == 7 - t) lds_wait(L_FLAG + 8, 2u); } else { if (qb == 3 - t) lds_wait(L_FLAG + 4, 2u); }
      };
      f32x16 sb[2], cm;
      float pe[16];
      FST(4, 1);
      ldkv(8);
      sb[0] = MFMA32(ka, q0f, splat(0.f));
      sb[0] = MFMA32(kq, q1f, sb[0]);
      SB();
      pre(IC<0>{});
#pragma unroll
      for (int e = 0; e < 16; ++e) pe[e] = ((e & 3) + 8 * (e >> 2) > qlim) ? -1.0e30f : sb[0][e];
      float mx = __builtin_fmaxf(pe[0], pe[1]);
#pragma unroll
      for (int e = 2; e < 16; e += 2) mx = max3(mx, pe[e], pe[e + 1]);
      mx = -max32(mx);
      cm = splat(mx);
      auto cin = [&](int t) LAM -> f32x16 { if constexpr (FULL) return cm; else return splat(t >= tmin ? mx : -1.0e30f); };
#pragma unroll
      for (int s2 = 0; s2 < 2; ++s2) {
        u32x4 pf;
#pragma unroll
        for (int e = 0; e < 4; ++e) pf[e] = pk2(__builtin_amdgcn_exp2f(pe[8 * s2 + 2 * e] + mx), __builtin_amdgcn_exp2f(pe[8 * s2 + 2 * e + 1] + mx));
        if (s2 == 0) oacc = MFMA32(vf[0][0], pf, splat(0.f)); else oacc = MFMA32(vf[0][1], pf, oacc);
      }
      SB();
      FST(4, 2);
      halo();
      pre(IC<1>{});
      sync(7);
      ldkv(7);
      sb[1] = MFMA32(ka, q0f, cin(7));
      sb[1] = MFMA32(kq, q1f, sb[1]);
      SB();
      sfor<0, 8>([&](auto u_) LAM {
        constexpr int t = 7 - decltype(u_)::v, c = t & 1, n = c ^ 1;
        u32x4 pf0, pf1;
        FST(4, 3 + decltype(u_)::v);
        if constexpr (t > 0) { if constexpr (t - 1 != 7) sync(t - 1); ldkv(t - 1); }
        if constexpr (t == 0) {
#pragma unroll
          for (int e = 0; e < 16; ++e) sb[c][e] = ((e & 3) + 8 * (e >> 2) > qlim) ? sb[c][e] : -1.0e30f;
        }
#pragma unroll
        for (int e = 0; e < 8; ++e) pe[e] = __builtin_amdgcn_exp2f(sb[c][e]);
        SB();
        if constexpr (t > 0) { sb[n] = MFMA32(ka, q0f, cin(t - 1)); SB(); }
#pragma unroll
        for (int e = 8; e < 12; ++e) pe[e] = __builtin_amdgcn_exp2f(sb[c][e]);
#pragma unroll
        for (int e = 0; e < 4; ++e) pf0[e] = pk2(pe[2 * e], pe[2 * e + 1]);
        SB();
        if constexpr (t > 0) { sb[n] = MFMA32(kq, q1f, sb[n]); SB(); }
#pragma unroll
        for (int e = 12; e < 16; ++e) pe[e] = __builtin_amdgcn_exp2f(sb[c][e]);
        SB();
        oacc = MFMA32(vf[c][0], pf0, oacc);
        SB();
#pragma unroll
        for (int e = 0; e < 4; ++e) pf1[e] = pk2(pe[8 + 2 * e], pe[8 + 2 * e + 1]);
        SB();
        if constexpr (t > 0) pre(IC<9 - t>{});
        oacc = MFMA32(vf[c][1], pf1, oacc);
        SB();
      });
    };
    auto exact = [&]() LAM {
      auto sc = [&](int t, float c0, f32x16& s) LAM {
        const u32x2 x2 = lds64(kb + t * 1536 + 32);
#pragma unroll
        for (int e = 0; e < 16; ++e) s[e] = c0;
        s = MFMA32(lds128(kb2 + t * 1536), q0f, s);
        s = MFMA32((u32x4{x2.x, x2.y, 0u, 0u}), q1f, s);
#pragma unroll
        for (int e = 0; e < 16; ++e) s[e] = (u32)(32 * t + (e & 3) + 8 * (e >> 2) - qlim - 1) < 256u ? s[e] : -1.0e30f;
      };
      float mx = -1.0e30f;
      f32x16 s;
#pragma nounroll
      for (int t = tmin; t < 9; ++t) {
        sc(t, 0.f, s);
#pragma unroll
        for (int e = 0; e < 16; e += 2) mx = max3(mx, s[e], s[e + 1]);
      }
      mx = -max32(mx);
#pragma unroll
      for (int e = 0; e < 16; ++e) oacc[e] = 0.f;
#pragma nounroll
      for (int t = tmin; t < 9; ++t) {
        sc(t, mx, s);
#pragma unroll
        for (int s2 = 0; s2 < 2; ++s2) {
          u32x4 pf;
#pragma unroll
          for (int e = 0; e < 4; ++e) pf[e] = pk2(__builtin_amdgcn_exp2f(s[8 * s2 + 2 * e]), __builtin_amdgcn_exp2f(s[8 * s2 + 2 * e + 1]));
          const u32 va = vb + (32 * t + 16 * s2) * 48;
          oacc = MFMA32(ldstr2(va, va + 384), pf, oacc);
        }
      }
    };
    if (mt >= 2) attn(BoolC<true>{}); else attn(BoolC<false>{});
    if (__builtin_amdgcn_ballot_w64(h != 0 && !(oacc[8] < ATT_LIMIT)) != 0ull) exact();
    const float lx = xor32f(oacc[8]);
    const float inv = __builtin_amdgcn_rcpf(64.f * (h ? oacc[8] : lx));
    {
      u32 ov[6];
#pragma unroll
      for (int e = 0; e < 6; ++e) ov[e] = pk2(oacc[2 * e] * inv, oacc[2 * e + 1] * inv);
      const auto x0 = __builtin_amdgcn_permlane32_swap(ov[0], ov[2], false, false), x1 = __builtin_amdgcn_permlane32_swap(ov[1], ov[3], false, false);
      if (hh == 0) {
        __builtin_amdgcn_raw_buffer_store_b128(u32x4{x0[0], x1[0], x0[1], x1[1]}, rs, ob + h * 512, 0, 16);
        if (h == 0) __builtin_amdgcn_raw_buffer_store_b64(u32x2{ov[4], ov[5]}, rs, ob + 1024, 0, 16);
      } else {
        const auto y0 = __builtin_amdgcn_permlane32_swap(ov[2], ov[4], false, false), y1 = __builtin_amdgcn_permlane32_swap(ov[3], ov[5], false, false);
        __builtin_amdgcn_raw_buffer_store_b128(u32x4{x0[1], x1[1], y0[0], y1[0]}, rs, ob + 1536 + h * 512, 0, 16);
        if (h == 0) __builtin_amdgcn_raw_buffer_store_b64(u32x2{ov[0], ov[1]}, rs, ob + 1032, 0, 16);
      }
    }
    STAMP(5); CST(1, 19); FST(4, 11);
    WAITVM(0);
    BAR();
    if (tid == 0) signal(ws, C_O + mt * 256);
    STAMP(6); FST(4, 13);
  }
  u32 wbase[5], avoff;
  u32x2 x1k[3];
  f32x4 c5[5];
  {
    PHASE();
    const char* sg1 = ws + WS_WGF + (size_t)j * WGB + 81920 + (kh * 40 + tt * 8) * 1024;
    auto stage1 = [&]() LAM { sfor<0, 8>([&](auto m_) LAM { constexpr int m = decltype(m_)::v; gld_sc1<(m & 3) * 1024>(st[10 + m], sg1 + (m >> 2) * 4096, l16); }); };
    if (w >= 4) { __builtin_amdgcn_s_sleep(2); Run<30, 4, false>(ws + WS_WOF + (size_t)n8 * WOB + 71680, wo_slot(7), w - 4).all(l16); }
    stage1();
#pragma unroll
    for (int m = 0; m < 5; ++m) wbase[m] = (kh * 5 + (m + 3 * kh) % 5) * 1024 + l16;
    avoff = (g4 >> 1) * 1024 + (g4 & 1) * 512 + (16 * (tt & 1) + c16) * 16;
    const int tt64 = mt * 4 + 2 * th + (tt >> 1);
    const char* ab = ws + WS_OAF + (size_t)(tt64 * 40 + 20 * kh) * 1024;
    const u32 ew = 73728 + (tt * 5) * 1024 + l16;
    u32 xb = WS_X1 + (u32)(tt64 * 40 + 5 * n8) * 1024 + (g4 >> 1) * 512 + (16 * (tt & 1) + c16) * 16 + (g4 & 1) * 8 + kh * 3072;
    u32 sta = WS_STAT + (u32)((tt64 * 8 + n8) * 32 + 16 * (tt & 1) + c16) * 16 + kh * 8;
    u32 mo = L_MODS + kh * 192 + g4 * 16;
    PIN(xb); PIN(sta); PIN(mo); PIN(avoff);
    auto iss = [&](auto s_) LAM { constexpr int s = decltype(s_)::v; gld_sc1<(s & 1) * 2048>(a[s], ab + (s >> 1) * 4096, avoff); };
    u32x4 wf[3][5];
#pragma unroll
    for (int s = 0; s < 2; ++s)
#pragma unroll
      for (int m = 0; m < 5; ++m) wf[s][m] = lds128(wbase[m] + wo_slot(s));
#pragma unroll
    for (int m = 0; m < 5; ++m) c5[m] = f32x4{0.f, 0.f, 0.f, 0.f};
    if (w == 0) poll_ge(ws, C_O + mt * 256, 16u);
    BAR();
    STAMP(7); FST(5, 1);
    sfor<0, 5>(iss);
    sfor<0, 10>([&](auto s_) LAM {
      constexpr int s = decltype(s_)::v;
      WAITV(s == 0 ? 4 : s == 1 ? 6 : 9 - s);
      FST(5, 2 + s);
      if (s == 4) BAR();
      PIN(a[s]);
      if constexpr (s + 2 < 10) {
#pragma unroll
        for (int m = 0; m < 5; ++m) wf[(s + 2) % 3][m] = lds128(wbase[m] + wo_slot(s + 2));
      }
#pragma unroll
      for (int m = 0; m < 5; ++m) c5[m] = MFMA16(wf[s % 3][m], a[s], c5[m]);
      SB();
      if constexpr (s == 0) sfor<5, 8>(iss);
      if constexpr (s == 1) sfor<8, 10>(iss);
      SB();
    });
    STAMP(8); FST(5, 12);
    stf4(ew + ((3 + 3 * kh) % 5) * 1024, c5[3]);
    stf4(ew + ((4 + 3 * kh) % 5) * 1024, c5[4]);
    if (kh) stf4(ew, c5[2]);
    BAR();
    FST(5, 13);
    float xs[12];
    u32x2 xg[3];
#pragma unroll
    for (int m = 0; m < 3; ++m) PIN(xr[m]);
#pragma unroll
    for (int m = 0; m < 3; ++m) {
      if (m < 2 || kh == 0) {
        const f32x4 o = ldsf4(ew + kh * 3072 + m * 1024), ga = ldsf4(mo + m * 64), g2 = ldsf4(mo + 320 + m * 64);
        const u32 y01 = pk2(c5[m][0] + o[0], c5[m][1] + o[1]), y23 = pk2(c5[m][2] + o[2], c5[m][3] + o[3]);
        const u32 t01 = pk2(ga[0] * lo16(y01), ga[1] * hi16(y01)), t23 = pk2(ga[2] * lo16(y23), ga[3] * hi16(y23));
        x1k[m] = u32x2{pk2(lo16(xr[m].x) + lo16(t01), hi16(xr[m].x) + hi16(t01)), pk2(lo16(xr[m].y) + lo16(t23), hi16(xr[m].y) + hi16(t23))};
        xs[4 * m] = lo16(x1k[m].x); xs[4 * m + 1] = hi16(x1k[m].x); xs[4 * m + 2] = lo16(x1k[m].y); xs[4 * m + 3] = hi16(x1k[m].y);
        xg[m] = u32x2{pk2(xs[4 * m] * g2[0], xs[4 * m + 1] * g2[1]), pk2(xs[4 * m + 2] * g2[2], xs[4 * m + 3] * g2[3])};
        if (m == 1) {
          const auto x0 = __builtin_amdgcn_permlane16_swap(xg[0].x, xg[1].x, false, false), x1 = __builtin_amdgcn_permlane16_swap(xg[0].y, xg[1].y, false, false);
          __builtin_amdgcn_raw_buffer_store_b128(u32x4{x0[0], x1[0], x0[1], x1[1]}, rs, xb + (g4 & 1) * 1016, 0, 16);
        }
        if (m == 2) __builtin_amdgcn_raw_buffer_store_b64(xg[2], rs, xb + 2048, 0, 16);
      } else {
        x1k[m] = u32x2{0u, 0u};
        xs[8] = xs[9] = xs[10] = xs[11] = 0.f;
      }
    }
    float s = ((xs[0] + xs[1]) + (xs[2] + xs[3])) + ((xs[4] + xs[5]) + (xs[6] + xs[7])) + ((xs[8] + xs[9]) + (xs[10] + xs[11]));
    s = sum32(sum16(s));
    const float mean = s * (kh ? 1.f / 32.f : 1.f / 48.f);
    float q0 = 0.f, q1 = 0.f;
#pragma unroll
    for (int e = 0; e < 4; ++e) { const float d0 = xs[e] - mean, d1 = xs[4 + e] - mean; q0 = fma1(d0, d0, q0); q1 = fma1(d1, d1, q1); }
    if (kh == 0) {
#pragma unroll
      for (int e = 8; e < 12; ++e) { const float d = xs[e] - mean; q0 = fma1(d, d, q0); }
    }
    const float q = sum32(sum16(q0 + q1));
    if (g4 == 0) __builtin_amdgcn_raw_buffer_store_b64(u32x2{f2u(mean), f2u(q)}, rs, sta, 0, 16);
    FST(5, 14);
    if (!SENT) WAITVM(0);
    BAR();
    if (!SENT && tid == 0) signal(ws, WS_CNT + (16 + mt) * 256);
    STAMP(9); FST(5, 16);
  }
  {
    PHASE();
    const bool old4 = w < 4;
    const char* wgb = ws + WS_WGF + (size_t)j * WGB;
    const char* wdb = ws + WS_WDF + (size_t)n8 * WDB;
    u32x4 stv[4], csv[2];
    const char* ab = ws + WS_X1 + (size_t)((mt * 4 + tt) * 40 + 20 * kh) * 1024;
    const char* sb = ws + WS_STAT + (size_t)(mt * 4 + tt) * 4096;
    auto iss = [&](auto s_) LAM { constexpr int s = decltype(s_)::v; gld_sc1<(s & 3) * 1024>(a[s], ab + (s >> 2) * 4096, l16); };
    {
      const char* cb = ws + WS_CSP + (size_t)(j * 16 + 4 * (w >> 1) + (w & 1)) * 768;
      const u32 co = lane < 48 ? l16 : 0u;
      gld_sc1<0>(csv[0], cb, co); gld_sc1<1536>(csv[1], cb, co);
    }
#pragma unroll
    for (int m = 0; m < 18; ++m) { PIN(st[m]); st128((m < 10 ? (kh * 40 + tt * 10 + m) : (80 + kh * 32 + tt * 8 + m - 10)) * 1024 + l16, st[m]); }
    float c1 = 0.f, d2 = 0.f;
    u32x4 bcor = {0u, 0u, 0u, 0u};
    u32 sw = kh * 40960 + tt * 4096 + l16, sr = (kh ^ 1) * 40960 + tt * 4096 + l16;
    u32 actb = WS_ACT + (u32)((mt * 4 + tt) * 96 + 6 * j + kh) * 1024 + l16;
    const u32 so = h * 2048 + c32 * 16;
    PIN(actb);
    const auto rC = gu_run<1, 16, 20, 4>(wgb, w & 3);
    const auto rA = gu_run<2, 0, 8, 4>(wgb, w & 3);
    const auto rB = gu_run<2, 8, 20, 4>(wgb, w & 3);
    const Run<60, 8, true> rD(wdb, 0, w);
    const Run<90, 8, true> rS(wdb + 61440, 61440, w);
    if (SENT) gate(sb, (c32 >> 2) * 512 + ((c32 >> 1) & 1) * 256 + (c32 & 1) * 8);
    else { if (w == 0) poll_ge(ws, WS_CNT + (16 + mt) * 256, 16u); BAR(); }
    STAMP(10); FST(2, 1); FST(5, 17); FST(9, 1);
    sfor<0, 2>(iss);
    gld_sc1<0>(stv[0], sb, so); gld_sc1<512>(stv[1], sb, so); gld_sc1<1024>(stv[2], sb, so); gld_sc1<1536>(stv[3], sb, so);
    sfor<2, 6>(iss);
    f32x16 ag[2], au[2];
    u32x4 fg[3], fu[3], outv;
    u32 ub = 0;
    auto ldw = [&](auto g_) LAM {
      constexpr int g = decltype(g_)::v, pp = g / 20, i = g % 20;
      constexpr bool first = i == 0 || gu_unit(pp, 0, i) != gu_unit(pp, 0, i - 1) + 1;
      constexpr int i0 = [] { int x = i; while (x > 0 && gu_unit(pp, 0, x) == gu_unit(pp, 0, x - 1) + 1) --x; return x; }();
      if constexpr (first) ub = frow + (kh ? gu_unit(pp, 1, i) : gu_unit(pp, 0, i)) * 2048;
      fg[g % 3] = lds128(ub + (i - i0) * 2048); fu[g % 3] = lds128(ub + (i - i0) * 2048 + 1024);
    };
    auto xwrite = [&](int pp) LAM {
      const f32x16 &G = ag[pp & 1], &U = au[pp & 1];
      stf4(sw, f32x4{G[8], G[9], G[10], G[11]}); stf4(sw + 1024, f32x4{G[12], G[13], G[14], G[15]});
      stf4(sw + 2048, f32x4{U[8], U[9], U[10], U[11]}); stf4(sw + 3072, f32x4{U[12], U[13], U[14], U[15]});
    };
    f32x4 xo[4] = {};
    auto xread = [&]() LAM {
#pragma unroll
      for (int u = 0; u < 4; ++u) xo[u] = ldsf4(sr + u * 1024);
    };
    auto xadd = [&](int pp, int q) LAM {
#pragma unroll
      for (int i = 0; i < 4; ++i) { ag[pp & 1][4 * q + i] += xo[q][i]; au[pp & 1][4 * q + i] += xo[2 + q][i]; }
    };
    auto corr = [&](int pp) LAM {
      ag[pp & 1] = MFMA32(lds128(L_T2 + pp * 1024 + rsw * 16), bcor, ag[pp & 1]);
      au[pp & 1] = MFMA32(lds128(L_T2 + pp * 1024 + 512 + rsw * 16), bcor, au[pp & 1]);
    };
    float eu[2], ee[2];
    auto epi = [&](int pp, int o, int ph) LAM {
#pragma unroll
      for (int i = 0; i < 2; ++i) {
        if (ph == 0) { ee[i] = __builtin_amdgcn_exp2f(ag[pp & 1][2 * o + i] * c1); eu[i] = ag[pp & 1][2 * o + i] * au[pp & 1][2 * o + i]; }
        if (ph == 1) ee[i] = __builtin_amdgcn_rcpf(fma1(ee[i], d2, d2));
      }
      if (ph == 2) outv[o] = pk2(eu[0] * ee[0], eu[1] * ee[1]);
    };
    auto ostore = [&](int pp) LAM { __builtin_amdgcn_raw_buffer_store_b128(outv, rs, actb + pp * 2048, 0, 16); asm volatile("" ::: "memory"); };
    BAR();
    FST(9, 2);
    ldw(IC<0>{}); ldw(IC<1>{});
    auto nextA = [&](auto g_) LAM {
      constexpr int g = decltype(g_)::v;
      WAITV(g == 0 ? 9 : g == 1 ? 12 : g == 2 ? 11 : g == 3 ? 14 : 19 - g);
      PIN(a[g]);
      ensure<(g & 3) * 1024>(a[g], ab + (g >> 2) * 4096, l16);
    };
    nextA(IC<0>{});
    sfor<0, 60>([&](auto g_) LAM {
      constexpr int g = decltype(g_)::v, pp = g / 20, i = g % 20;
      if (g == 32) WAITV(0);
      if (g == 43) WAITV(NRS >= 3 ? 4 : 1 + NRS);
      if ((g & 3) == 0) FST(2, 2 + g / 4);
      if (g == 12 || g == 22 || g == 32 || g == 43 || g == 50) BAR();
      if (g == 22 && CSEL != 4) { STAMP(16); CST(0, 18); }
      if (g == 43 && CSEL != 4) { STAMP(17); CST(0, 19); }
      if constexpr (g + 2 < 60) ldw(IC<g + 2>{});
      if (g == 22 || g == 43) xread();
      if (i == 0) {
        f32x16 z;
#pragma unroll
        for (int e = 0; e < 16; ++e) z[e] = 0.f;
        ag[pp & 1] = MFMA32(fg[g % 3], a[i], z); au[pp & 1] = MFMA32(fu[g % 3], a[i], z);
      } else if (g == 45) {
        ag[0] = MFMA32(fg[g % 3], a[i], ag[0]); au[0] = MFMA32(fu[g % 3], a[i], au[0]);
        if (!kh) corr(2);
      } else {
        ag[pp & 1] = MFMA32(fg[g % 3], a[i], ag[pp & 1]); au[pp & 1] = MFMA32(fu[g % 3], a[i], au[pp & 1]);
      }
      if (g == 4) {
        PIN(csv[0]); PIN(csv[1]);
        f32x4 sm;
#pragma unroll
        for (int e = 0; e < 4; ++e) sm[e] = u2f(csv[0][e]) + u2f(csv[1][e]);
        if (lane < 48) stf4(L_GS + (w >> 1) * 1536 + (w & 1) * 768 + l16, sm);
      }
      if (g == 6) {
        float s = 0.f;
#pragma unroll
        for (int u = 0; u < 4; ++u) s += 48.f * u2f(stv[u][0]) + 32.f * u2f(stv[u][2]);
        c1 = sum32(s) * (1.f / 640.f);
      }
      if (g == 7) {
        float q = 0.f;
#pragma unroll
        for (int u = 0; u < 4; ++u) {
          const float d0 = u2f(stv[u][0]) - c1, d1 = u2f(stv[u][2]) - c1;
          q += u2f(stv[u][1]) + u2f(stv[u][3]) + 48.f * d0 * d0 + 32.f * d1 * d1;
        }
        d2 = sum32(q);
      }
      if (g == 8) {
        const float mean = c1, den = rbf(__builtin_amdgcn_sqrtf(rbf(rbf(d2 * (1.f / 640.f)) + 1e-5f)));
        c1 = -1.4426950408889634f * __builtin_amdgcn_rcpf(den); d2 = den * den;
        const u32 nm = pk2(-mean, -mean), dd = pk2(den, den);
        bcor = u32x4{h ? 0u : nm, h ? 0u : ((nm & 0xffffu) | (dd & 0xffff0000u)), h ? 0u : dd, 0u};
      }
      if (g == 12) {
        const u32 t5 = tid - 256 < 192 ? tid - 256 : 0, t4 = L_GS + (t5 >> 3) * 64 + (t5 & 7) * 4;
        if (kh) {
#pragma unroll
          for (int u = 0; u < 4; ++u) { xo[0][u] = ldsf(t4 + u * 1536); xo[1][u] = ldsf(t4 + u * 1536 + 32); }
        }
      }
      if (g == 14 && kh) {
        const float cs = (xo[0][0] + xo[0][1]) + (xo[0][2] + xo[0][3]), bi = (xo[1][0] + xo[1][1]) + (xo[1][2] + xo[1][3]);
        const u32 h0 = pk2(cs, bi);
        const float cr = cs - lo16(h0), br = bi - hi16(h0);
        const u32 h1 = pk2(cr, br), h2 = pk2(cr - lo16(h1), br - hi16(h1));
        const int t5 = tid - 256, gu = t5 >= 96, nn = t5 - 96 * gu;
        if (t5 < 192)
          st128(L_T2 + (nn >> 5) * 1024 + gu * 512 + (nn & 31) * 16,
                u32x4{__builtin_amdgcn_perm(h1, h0, 0x05040100u), __builtin_amdgcn_perm(h0, h2, 0x07060100u), __builtin_amdgcn_perm(h2, h1, 0x07060302u), 0u});
      }
      if (g == 21 || g == 41) xwrite(pp - 1);
      if (g == 22 || g == 42) corr(pp - 1);
      if (g == 24 || g == 25) xadd(0, g - 24);
      if (g >= 28 && g <= 37 && (g - 28) % 3 == 0) epi(0, (g - 28) / 3, 2);
      if (g >= 26 && g < 37 && (g - 26) % 3 < 2) epi(0, (g - 26) / 3, (g - 26) % 3);
      if (g == 44 || g == 45) xadd(1, g - 44);
      if (g >= 48 && g <= 57 && (g - 48) % 3 == 0) epi(1, (g - 48) / 3, 2);
      if (g >= 46 && g < 57 && (g - 46) % 3 < 2) epi(1, (g - 46) / 3, (g - 46) % 3);
      SGB(0x100, 6, 0); SGB(0x008, 1, 0); SGB(0x002, 4, 0); SGB(0x008, 1, 0);
      SB();
      if constexpr (g < 3) sfor<6 + 4 * g, 10 + 4 * g>(iss);
      if constexpr (g == 3) sfor<18, 20>(iss);
      if constexpr (g + 1 < 20) nextA(IC<g + 1>{});
      if constexpr (g == 4 || g == 5) {
        PIN(stv[2 * (g - 4)]); PIN(stv[2 * (g - 4) + 1]);
        ensure<1024 * (g - 4)>(stv[2 * (g - 4)], sb, so); ensure<1024 * (g - 4) + 512>(stv[2 * (g - 4) + 1], sb, so);
      }
      if (g == 40) ostore(0);
      if constexpr (g >= 40 && g < 40 + NRS) gld_sc1<((g - 40) & 3) * 1024>(a[g - 40], rS.src + ((g - 40) >> 2) * 4096, l16);
      if constexpr (g >= 50 && g < 58) rD.template go<g - 50>(l16);
      if (g == 57) ostore(1);
      if constexpr ((g >= 19 && g < 21) || (g >= 22 && g < 26) || (g >= 32 && g < 38)) {
        if (old4) {
          if constexpr (g < 21) { rC.template go<2 * (g - 19)>(l16); rC.template go<2 * (g - 19) + 1>(l16); }
          else if constexpr (g < 26) { rA.template go<2 * (g - 22)>(l16); rA.template go<2 * (g - 22) + 1>(l16); }
          else { rB.template go<2 * (g - 32)>(l16); rB.template go<2 * (g - 32) + 1>(l16); }
        }
      }
      SB();
    });
    { const u32 lo_ = 65536 + tt * 4096, hi_ = (tt < 2 ? 106496 : 131072) + tt * 4096; sw = (kh ? hi_ : lo_) + l16; sr = (kh ? lo_ : hi_) + l16; }
    xwrite(2);
    BAR();
    STAMP(11); FST(2, 17);
    xread(); xadd(2, 0); xadd(2, 1);
#pragma unroll
    for (int o = 0; o < 4; ++o)
#pragma unroll
      for (int ph = 0; ph < 3; ++ph) epi(2, o, ph);
    ostore(2);
    if (SENT) WAITV(8); else WAITVM(0);
    BAR();
    if (!SENT && tid == 0) signal(ws, WS_CNT + (32 + mt) * 256);
#pragma unroll
    for (int m = 0; m < NRS; ++m) { PIN(a[m]); st128(rS.lds + m * 1024 + l16, a[m]); }
    sfor<NRS, 12>([&](auto m_) LAM { rS.template go<decltype(m_)::v>(l16); });
    STAMP(12);
  }
  {
    PHASE();
    const bool old4 = w < 4;
    const char* wdb = ws + WS_WDF + (size_t)n8 * WDB;
    const int tt64 = mt * 4 + 2 * th + (tt >> 1);
    const char* ab = ws + WS_ACT + (size_t)(tt64 * 96 + 48 * kh) * 1024;
    auto iss = [&](auto s_) LAM { constexpr int s = decltype(s_)::v; gld_sc1<(s & 1) * 2048>(a[s], ab + (s >> 1) * 4096, avoff); };
#pragma unroll
    for (int m = 0; m < 5; ++m) c5[m] = f32x4{0.f, 0.f, 0.f, 0.f};
    const Run<50, 4, true> rE(wdb + 153600, 0, w & 3);
    const Run<40, 4, true> rF(wdb + 204800, 51200, w & 3);
    const u32 ew = 92160 + (tt * 5) * 1024 + l16;
    u32 oo = (u32)(128 * mt + 64 * th + 16 * tt + c16) * 1280 + (80 * n8 + 48 * kh + 4 * g4) * 2;
    u32 mo = L_MODS + 640 + kh * 192 + g4 * 16;
    PIN(oo); PIN(mo);
    if (SENT) gate(ab, ((c16 >> 1) * 6 + 4 + (c16 & 1)) * 1024 + (tt & 1) * 256);
    else { if (w == 0) poll_ge(ws, WS_CNT + (32 + mt) * 256, 16u); BAR(); }
    STAMP(13); CST(2, 18); FST(1, 1);
    sfor<0, 6>(iss);
    u32 wb2[5], wb3[5];
#pragma unroll
    for (int m = 0; m < 5; ++m) { wb2[m] = wbase[m] + 61440; wb3[m] = wbase[m] + 122880; PIN(wb2[m]); PIN(wb3[m]); }
    auto nextA = [&](auto s_) LAM {
      constexpr int s = decltype(s_)::v;
      if constexpr (dn_wait(s, true) == dn_wait(s, false)) WAITV(dn_wait(s, true)); else { if (old4) WAITV(dn_wait(s, true)); else WAITV(dn_wait(s, false)); }
      PIN(a[s]);
      ensure<(s & 1) * 2048>(a[s], ab + (s >> 1) * 4096, avoff);
    };
    BAR();
    u32x4 wf[3][5];
#pragma unroll
    for (int u = 0; u < 2; ++u)
#pragma unroll
      for (int m = 0; m < 5; ++m) wf[u][m] = lds128(wbase[m] + wd_slot(u));
    nextA(IC<0>{});
    sfor<0, 24>([&](auto s_) LAM {
      constexpr int s = decltype(s_)::v;
      if ((s & 1) == 0) FST(1, 2 + s / 2);
      if (s == 5 || s == 9 || s == 13 || s == 18) BAR();
      if constexpr (s + 2 < 24) {
        constexpr u32 so = wd_slot(s + 2);
#pragma unroll
        for (int m = 0; m < 5; ++m) wf[(s + 2) % 3][m] = lds128((so < 61440 ? wbase[m] : so < 122880 ? wb2[m] : wb3[m]) + so % 61440);
      }
#pragma unroll
      for (int m = 0; m < 5; ++m) c5[m] = MFMA16(wf[s % 3][m], a[s], c5[m]);
      SB();
      if constexpr (s >= 5 && s < 14) {
        if (old4) {
          if constexpr (s < 9) { sfor<0, 4>([&](auto u_) LAM { rE.template go<4 * (s - 5) + decltype(u_)::v>(l16); }); }
          else { rF.template go<2 * (s - 9)>(l16); rF.template go<2 * (s - 9) + 1>(l16); }
        }
      }
      sfor<dn_cumA(s), dn_cumA(s + 1)>(iss);
      if constexpr (s + 1 < 24) nextA(IC<s + 1>{});
      SB();
    });
    STAMP(14); CST(2, 19); FST(1, 14);
    stf4(ew + ((3 + 3 * kh) % 5) * 1024, c5[3]);
    stf4(ew + ((4 + 3 * kh) % 5) * 1024, c5[4]);
    if (kh) stf4(ew, c5[2]);
    BAR();
    u32x2 rv[3];
#pragma unroll
    for (int m = 0; m < 3; ++m)
      if (m < 2 || kh == 0) {
        const f32x4 o = ldsf4(ew + kh * 3072 + m * 1024), gb = ldsf4(mo + m * 64);
        const u32 y01 = pk2(c5[m][0] + o[0], c5[m][1] + o[1]), y23 = pk2(c5[m][2] + o[2], c5[m][3] + o[3]);
        const u32 t01 = pk2(gb[0] * lo16(y01), gb[1] * hi16(y01)), t23 = pk2(gb[2] * lo16(y23), gb[3] * hi16(y23));
        rv[m] = u32x2{pk2(lo16(x1k[m].x) + lo16(t01), hi16(x1k[m].x) + hi16(t01)), pk2(lo16(x1k[m].y) + lo16(t23), hi16(x1k[m].y) + hi16(t23))};
        if (m == 1) {
          const auto x0 = __builtin_amdgcn_permlane16_swap(rv[0].x, rv[1].x, false, false), x1 = __builtin_amdgcn_permlane16_swap(rv[0].y, rv[1].y, false, false);
          *(GAS u32x4*)((GAS char*)p.out + oo + (g4 & 1) * 24) = u32x4{x0[0], x1[0], x0[1], x1[1]};
        }
        if (m == 2) *(GAS u32x2*)((GAS char*)p.out + oo + 64) = rv[2];
      }
  }
}
}

extern "C" void kernel_launch(void* const* d_in, const int* in_sizes, int n_in, void* d_out, int out_size, void* d_ws, size_t ws_size,
                              hipStream_t stream) {
  (void)in_sizes; (void)n_in; (void)out_size; (void)ws_size;
  (void)hipFuncSetAttribute((const void*)k2, hipFuncAttributeMaxDynamicSharedMemorySize, LDS_BYTES);
  P1 q;
  q.x = (const u16*)d_in[0]; q.wqkv = (const u16*)d_in[1]; q.wo = (const u16*)d_in[2];
  q.cvec = (const u16*)d_in[7]; q.wmod = (const u16*)d_in[8]; q.ws = (char*)d_ws;
  k1<<<K1_BLOCKS, 256, 0, stream>>>(q);
  P2 p;
  p.x = (const u16*)d_in[0]; p.wg = (const u16*)d_in[4]; p.wu = (const u16*)d_in[5]; p.wd = (const u16*)d_in[6];
  p.out = (u16*)d_out; p.ws = (char*)d_ws;
  k2<<<256, 512, LDS_BYTES, stream>>>(p);
}
```

```cpp
#include <hip/hip_runtime.h>
#include <hip/hip_bf16.h>
#include <stdint.h>

#define GAS __attribute__((address_space(1)))
#define LAS __attribute__((address_space(3)))
#if defined(__HIP_DEVICE_COMPILE__)
#define NOPK __attribute__((target("no-packed-fp32-ops")))
#else
#define NOPK
#endif
#define DI __device__ __forceinline__ NOPK
#define LAM __attribute__((always_inline)) NOPK
typedef unsigned int u32;
typedef unsigned short u16;
typedef u32 u32x4 __attribute__((ext_vector_type(4)));
typedef u32 u32x2 __attribute__((ext_vector_type(2)));
typedef float f32x2 __attribute__((ext_vector_type(2)));
typedef float f32x4 __attribute__((ext_vector_type(4)));
typedef float f32x16 __attribute__((ext_vector_type(16)));
typedef __bf16 bf16x2 __attribute__((ext_vector_type(2)));
typedef __bf16 bf16x4 __attribute__((ext_vector_type(4)));
typedef __bf16 bf16x8 __attribute__((ext_vector_type(8)));
typedef GAS u32 gu32;
typedef _Float16 f16x8 __attribute__((ext_vector_type(8)));
typedef _Float16 f16x2 __attribute__((ext_vector_type(2)));

namespace {
constexpr int D = 640;
constexpr u32 WS_KVF = 0;
constexpr u32 WS_CNT = 32768;
constexpr u32 WS_MODP = 65536;
constexpr u32 WS_CSP = WS_MODP + 61440;
constexpr u32 WS_STAT = WS_CSP + 16 * 16 * 1536;
constexpr u32 WS_N1 = WS_STAT + 64 * 8 * 32 * 16;
constexpr u32 WS_OAF = WS_N1 + 64 * 40 * 1024;
constexpr u32 WS_X1 = WS_OAF + 64 * 40 * 1024;
constexpr u32 WS_ACT = WS_X1 + 64 * 40 * 1024;
constexpr u32 KVB = 24576;
constexpr u32 WS_KVX = WS_ACT + 64 * 96 * 1024;
constexpr u32 WQB = 153600;
constexpr u32 WS_WQF = WS_KVX + 256 * KVB;
constexpr u32 WOB = 102400, WGB = 245760, WDB = 245760;
constexpr u32 WS_WOF = WS_WQF + 16 * WQB;
constexpr u32 WS_WGF = WS_WOF + 8 * WOB;
constexpr u32 WS_WDF = WS_WGF + 16 * WGB;
constexpr u32 WS_END = WS_WDF + 8 * WDB;
constexpr u32 L_MISC = 153600;
constexpr u32 L_GS = L_MISC;
constexpr u32 L_MODS = L_MISC + 6144;
constexpr u32 L_T2 = L_MISC + 7168;
constexpr u32 L_FLAG = L_MISC + 9728;
constexpr u32 LDS_BYTES = 163840;
constexpr float QSCALE = 0.22360679774997896f * 1.4426950408889634f;
constexpr int K1_BLOCKS = 488;
#define ATT_LIMIT 1.0e18f
constexpr int K2_LINES = 320;
constexpr u32 SPIN_CAP = 1u << 20;

DI float u2f(u32 v) { return __builtin_bit_cast(float, v); }
DI u32 f2u(float v) { return __builtin_bit_cast(u32, v); }
DI int tidx() { return (int)__builtin_amdgcn_workitem_id_x(); }
DI int bidx() { return (int)__builtin_amdgcn_workgroup_id_x(); }
DI float lo16(u32 v) { return u2f(v << 16); }
DI float hi16(u32 v) { return u2f(v & 0xffff0000u); }
DI u32 pk2(float a, float b) { f32x2 v = {a, b}; return __builtin_bit_cast(u32, __builtin_convertvector(v, bf16x2)); }
DI u32 pkh2(float a, float b) { f32x2 v = {a, b}; return __builtin_bit_cast(u32, __builtin_convertvector(v, f16x2)); }
DI float rbf(float a) { return lo16(pk2(a, a)); }
DI float fma1(float a, float b, float c) { return __builtin_fmaf(a, b, c); }
DI float max3(float a, float b, float c) { return __builtin_fmaxf(__builtin_fmaxf(a, b), c); }
DI float xor32f(float v) {
  auto r = __builtin_amdgcn_permlane32_swap(f2u(v), f2u(v), false, false);
  return u2f((tidx() & 32) ? r[0] : r[1]);
}
DI float sum32(float v) { auto r = __builtin_amdgcn_permlane32_swap(f2u(v), f2u(v), false, false); return u2f(r[0]) + u2f(r[1]); }
DI float sum16(float v) { auto r = __builtin_amdgcn_permlane16_swap(f2u(v), f2u(v), false, false); return u2f(r[0]) + u2f(r[1]); }
DI float max32(float v) { auto r = __builtin_amdgcn_permlane32_swap(f2u(v), f2u(v), false, false); return __builtin_fmaxf(u2f(r[0]), u2f(r[1])); }
template <int CTRL> DI float dppf(float v) { return u2f(__builtin_amdgcn_update_dpp(0u, f2u(v), CTRL, 0xf, 0xf, true)); }

#define MFMA32(a, b, c) __builtin_amdgcn_mfma_f32_32x32x16_bf16(__builtin_bit_cast(bf16x8, a), __builtin_bit_cast(bf16x8, b), c, 0, 0, 0)
#define MFMA32H(a, b, c) __builtin_amdgcn_mfma_f32_32x32x16_f16(__builtin_bit_cast(f16x8, a), __builtin_bit_cast(f16x8, b), c, 0, 0, 0)
#define MFMA16(a, b, c) __builtin_amdgcn_mfma_f32_16x16x32_bf16(__builtin_bit_cast(bf16x8, a), __builtin_bit_cast(bf16x8, b), c, 0, 0, 0)
#define SGB(m, n, id) __builtin_amdgcn_sched_group_barrier(m, n, id)
#define SB() __builtin_amdgcn_sched_barrier(0)
template <bool B> struct BoolC { static constexpr bool v = B; };
template <int I> struct IC { static constexpr int v = I; };
template <int I, int N, class Fn> DI void sfor(Fn&& f) { if constexpr (I < N) { f(IC<I>{}); sfor<I + 1, N>(f); } }
#define PIN(x) asm volatile("" : "+v"(x))
#define WAITV(n) asm volatile("s_waitcnt vmcnt(%0)" ::"i"(n))
#define WAITVM(n) asm volatile("s_waitcnt vmcnt(%0)" ::"i"(n) : "memory")
#define BAR() asm volatile("s_waitcnt lgkmcnt(0)\n\ts_barrier" ::: "memory")
#define RLX_AGENT __ATOMIC_RELAXED, __HIP_MEMORY_SCOPE_AGENT

DI void pin4(u32x4& v) { PIN(v); }
DI u32x4 lds128(u32 a) { return *(const LAS u32x4*)(size_t)a; }
DI f32x4 ldsf4(u32 a) { return *(const LAS f32x4*)(size_t)a; }
DI u32x2 lds64(u32 a) { return *(const LAS u32x2*)(size_t)a; }
DI float ldsf(u32 a) { return *(const LAS float*)(size_t)a; }
DI void st128(u32 a, u32x4 v) { *(LAS u32x4*)(size_t)a = v; }
DI void stf4(u32 a, f32x4 v) { *(LAS f32x4*)(size_t)a = v; }
DI void st64(u32 a, u32x2 v) { *(LAS u32x2*)(size_t)a = v; }
DI u32x2 ldstr(u32 a) { return __builtin_bit_cast(u32x2, __builtin_amdgcn_ds_read_tr16_b64_v4bf16((LAS bf16x4*)(size_t)a)); }
DI u32x4 ldstr2(u32 a, u32 b) { u32x2 lo = ldstr(a), hi = ldstr(b); return u32x4{lo.x, lo.y, hi.x, hi.y}; }

template <int IMM, bool SC1> DI void dmai(const char* sbase, u32 voff, u32 lds) {
  if constexpr (SC1) asm volatile("s_mov_b32 m0, %2\n\ts_nop 0\n\tglobal_load_lds_dwordx4 %0, %1 offset:%3 sc1" ::"v"(voff), "s"(sbase), "s"(lds), "i"(IMM));
  else asm volatile("s_mov_b32 m0, %2\n\ts_nop 0\n\tglobal_load_lds_dwordx4 %0, %1 offset:%3" ::"v"(voff), "s"(sbase), "s"(lds), "i"(IMM));
}
DI void dma(const char* sbase, u32 voff, u32 lds) { dmai<0, false>(sbase, voff, lds); }
DI void gld32_sc1(u32& r, const char* sbase, u32 voff) { asm volatile("global_load_dword %0, %1, %2 sc1" : "=v"(r) : "v"(voff), "s"(sbase)); }
template <int IMM> DI void gld_sc1(u32x4& r, const char* sbase, u32 voff) {
  asm volatile("global_load_dwordx4 %0, %1, %2 offset:%3 sc1" : "=v"(r) : "v"(voff), "s"(sbase), "i"(IMM));
}
template <int IMM> DI void gld(u32x4& r, const char* sbase, u32 voff) {
  asm volatile("global_load_dwordx4 %0, %1, %2 offset:%3" : "=v"(r) : "v"(voff), "s"(sbase), "i"(IMM));
}
DI void gld64(u32x2& r, const char* sbase, u32 voff) {
  asm volatile("global_load_dwordx2 %0, %1, %2" : "=v"(r) : "v"(voff), "s"(sbase));
}
DI void poll_ge(const char* ws, u32 off, u32 want) {
  gu32* p = (gu32*)(ws + off);
  for (u32 spins = 0; spins < SPIN_CAP; ++spins) {
    if ((u32)__builtin_amdgcn_readfirstlane(__hip_atomic_load(p, RLX_AGENT)) >= want) break;
    __builtin_amdgcn_s_sleep(4);
  }
  __builtin_amdgcn_fence(__ATOMIC_ACQUIRE, "wavefront");
}
DI void signal(const char* ws, u32 off) { __hip_atomic_fetch_add((gu32*)(ws + off), 1u, RLX_AGENT); }

struct P1 {
  const u16 *x, *wqkv, *wo, *cvec, *wmod;
  char* ws;
};
__global__ void __launch_bounds__(256) NOPK k1(P1 p) {
  __shared__ float red[32 * 64 + 4 * 64 + 320];
  const int b = bidx(), t = tidx();
  char* ws = p.ws;
  const __amdgpu_buffer_rsrc_t rs = __builtin_amdgcn_make_buffer_rsrc(p.ws, 0, WS_END, 0x00020000);
  if (b >= 408 && b < 424) {
    unsigned long long pc;
    asm volatile("s_getpc_b64 s[40:41]\n\ts_add_u32 s40, s40, _ZN12_GLOBAL__N_12k2ENS_2P2E@rel32@lo+4\n\ts_addc_u32 s41, s41, _ZN12_GLOBAL__N_12k2ENS_2P2E@rel32@hi+12\n\ts_mov_b64 %0, s[40:41]"
                 : "=s"(pc) : : "scc", "s40", "s41");
    const int o = ((b - 408) >> 3) * 256 + t;
    if (o < K2_LINES) { u32 v_ = *(const GAS u32*)(pc + (size_t)o * 128); asm volatile("" ::"v"(v_)); }
  }
  if (b < 120) {
    const int ch = t & 7, r = t >> 3, col0 = 64 * (b >> 1), row0 = 320 * (b & 1);
    const u32x4 cv = *(const u32x4*)(p.cvec + row0 + 8 * (t < 40 ? t : 39));
    u32x4 wv[10];
#pragma unroll
    for (int i = 0; i < 10; ++i) wv[i] = __builtin_nontemporal_load((const u32x4*)(p.wmod + (size_t)(row0 + r + 32 * i) * 3840 + col0 + 8 * ch));
    if (t < 40) {
#pragma unroll
      for (int e = 0; e < 8; ++e) {
        const float c = (e & 1) ? hi16(cv[e >> 1]) : lo16(cv[e >> 1]);
        red[2304 + 8 * t + e] = rbf(c * __builtin_amdgcn_rcpf(1.f + __builtin_amdgcn_exp2f(-1.4426950408889634f * c)));
      }
    }
    BAR();
    float acc[8] = {0, 0, 0, 0, 0, 0, 0, 0};
#pragma unroll
    for (int i = 0; i < 10; ++i) {
      const float s = red[2304 + r + 32 * i];
#pragma unroll
      for (int e = 0; e < 4; ++e) { acc[2 * e] = fma1(s, lo16(wv[i][e]), acc[2 * e]); acc[2 * e + 1] = fma1(s, hi16(wv[i][e]), acc[2 * e + 1]); }
    }
    *(f32x4*)&red[r * 64 + 8 * ch] = f32x4{acc[0], acc[1], acc[2], acc[3]};
    *(f32x4*)&red[r * 64 + 8 * ch + 4] = f32x4{acc[4], acc[5], acc[6], acc[7]};
    BAR();
    { const int c = t & 63, part = t >> 6; float s = 0;
#pragma unroll
      for (int i = 0; i < 8; ++i) s += red[(part * 8 + i) * 64 + c];
      red[2048 + part * 64 + c] = s; }
    BAR();
    if (t < 64) {
      const float s = (red[2048 + t] + red[2112 + t]) + (red[2176 + t] + red[2240 + t]);
      __builtin_amdgcn_raw_buffer_store_b32(f2u(s), rs, WS_MODP + ((b & 1) * 3840 + col0 + t) * 4, 0, 0);
    }
    if (b == 0) {
      *(u32*)(ws + WS_KVF + t * 128) = 0u;
      if (t < 72) *(u32*)(ws + WS_CNT + t * 256) = 0u;
    }
  } else if (b < 248) {
    const int hb = b - 120, c = t & 15, tok = 16 * (hb & 1) + (t >> 4);
    const u16* row = p.x + (size_t)(32 * (hb >> 1) + tok) * D;
    u32x4 v[5];
#pragma unroll
    for (int i = 0; i < 5; ++i) v[i] = *(const u32x4*)(row + 8 * (c + 16 * i));
    float s = 0;
#pragma unroll
    for (int i = 0; i < 5; ++i)
#pragma unroll
      for (int e = 0; e < 4; ++e) s += lo16(v[i][e]) + hi16(v[i][e]);
    s += dppf<0xB1>(s); s += dppf<0x4E>(s); s += dppf<0x141>(s); s += dppf<0x140>(s);
    const float mean = s * (1.f / 640.f);
    float q = 0;
#pragma unroll
    for (int i = 0; i < 5; ++i)
#pragma unroll
      for (int e = 0; e < 4; ++e) { const float a = lo16(v[i][e]) - mean, bb = hi16(v[i][e]) - mean; q = fma1(a, a, q); q = fma1(bb, bb, q); }
    q += dppf<0xB1>(q); q += dppf<0x4E>(q); q += dppf<0x141>(q); q += dppf<0x140>(q);
    const float mb = rbf(mean), den = rbf(__builtin_amdgcn_sqrtf(rbf(rbf(q * (1.f / 640.f)) + 1e-5f))), rr = __builtin_amdgcn_rcpf(den);
    const u32 dst = WS_N1 + (u32)(hb >> 1) * 40960 + tok * 16;
#pragma unroll
    for (int i = 0; i < 5; ++i) {
      u32x4 o;
#pragma unroll
      for (int e = 0; e < 4; ++e) {
        const u32 d = pk2(lo16(v[i][e]) - mb, hi16(v[i][e]) - mb);
        const u32 n = pk2(lo16(d) * rr, hi16(d) * rr);
        o[e] = pkh2(lo16(n), hi16(n));
      }
      __builtin_amdgcn_raw_buffer_store_b128(o, rs, dst + (c + 16 * i) * 512, 0, 0);
    }
  } else {
    const u16* src; int stride; u32 off0, off1; bool act = true;
    if (b < 408) {
      const int idx = b - 248, ks = idx >> 2, jq = idx & 3, tc = t < 240 ? t : 239;
      act = t < 240;
      const int seg = tc / 80, cp = tc - 80 * seg;
      src = p.wqkv + (size_t)(16 * ks) * 1920 + 640 * seg + 160 * jq + 2 * cp; stride = 1920;
      const int jj = (2 * cp) / 40, within = 2 * cp - 40 * jj, j = 4 * jq + jj;
      const int n = (seg == 1 ? 0 : seg == 2 ? 40 : 80) + within, nb = n >> 5, r = n & 31;
      off0 = WS_WQF + j * WQB + (nb < 3 ? ((nb * 40 + ks) * 64 + r) * 16 : 122880 + ks * 768 + r * 16);
      off1 = off0 + (nb < 3 ? 512 : 384);
    } else {
      const int idx = b - 408, k16 = idx >> 1, tc = t < 160 ? t : 159, n = (idx & 1) * 320 + 2 * tc;
      act = t < 160;
      src = p.wo + (size_t)(16 * k16) * 640 + n; stride = 640;
      const int kh = k16 >= 20, kk = k16 - 20 * kh, st = kk >> 1, H = kk & 1;
      const int ntg = n >> 4, n8 = ntg / 5, nt = ntg - 5 * n8;
      off0 = WS_WOF + n8 * WOB + (st * 10 + kh * 5 + nt) * 1024 + (2 * H * 16 + (n & 15)) * 16;
      off1 = off0 + 256;
    }
    u32 v[16];
#pragma unroll
    for (int i = 0; i < 16; ++i) v[i] = __builtin_nontemporal_load((const u32*)(src + (size_t)i * stride));
    if (b < 408) {
#pragma unroll
      for (int i = 0; i < 16; ++i) v[i] = pkh2(lo16(v[i]) * 64.f, hi16(v[i]) * 64.f);
    }
    if (act) {
#pragma unroll
      for (int h = 0; h < 2; ++h) {
        u32x4 a, bq;
#pragma unroll
        for (int e = 0; e < 4; ++e) {
          a[e] = __builtin_amdgcn_perm(v[8 * h + 2 * e + 1], v[8 * h + 2 * e], 0x05040100u);
          bq[e] = __builtin_amdgcn_perm(v[8 * h + 2 * e + 1], v[8 * h + 2 * e], 0x07060302u);
        }
        __builtin_amdgcn_raw_buffer_store_b128(a, rs, h ? off1 : off0, 0, 0);
        __builtin_amdgcn_raw_buffer_store_b128(bq, rs, (h ? off1 : off0) + 16, 0, 0);
      }
    }
  }
}

struct P2 {
  const u16 *x, *wg, *wu, *wd;
  u16* out;
  char* ws;
};
constexpr u32 C_O = WS_CNT, C_RG = WS_CNT + 48 * 256, C_RD = WS_CNT + 64 * 256;
#define OPQ(x) asm volatile("" : "+s"(x))

DI constexpr int gu_unit(int p, int kh, int i) {
  return p == 0 ? 20 * kh + i
       : p == 1 ? (i < 16 ? 40 + 16 * kh + i : 8 + 20 * kh + (i - 16))
                : (i < 8 ? 12 + 20 * kh + i : 40 + 16 * kh + (i - 8));
}
DI constexpr u32 wo_slot(int s) { return s < 7 ? (72 + 10 * s) * 1024 : (s - 7) * 10240; }
DI constexpr u32 wd_slot(int s) { return (s < 15 ? s : s - 15) * 10240; }
template <int N, int NW, bool SC1> struct Run {
  static constexpr int C = (N + NW - 1) / NW;
  const char* src; u32 lds;
  DI Run(const char* s, u32 l, int x) { int o = x * C; o = (o < N - C ? o : N - C) * 1024; src = s + o; lds = l + o; }
  template <int M> DI void go(u32 l16) const { if constexpr (M < C) dmai<(M & 3) * 1024, SC1>(src + (M >> 2) * 4096, l16, lds + (M >> 2) * 4096); }
  DI void all(u32 l16) const { sfor<0, C>([&](auto m_) LAM { go<decltype(m_)::v>(l16); }); }
};
template <int P, int I0, int I1, int NW> DI auto gu_run(const char* blob, int x) {
  const int k = x >= NW / 2;
  return Run<2 * (I1 - I0), NW / 2, true>(blob + ((P * 2 + k) * 20 + I0) * 2048, (k ? gu_unit(P, 1, I0) : gu_unit(P, 0, I0)) * 2048, x - k * (NW / 2));
}
DI u32 pkfma16(u32 x, u32 y, u32 z) { return __builtin_bit_cast(u32, __builtin_elementwise_fma(__builtin_bit_cast(f16x2, x), __builtin_bit_cast(f16x2, y), __builtin_bit_cast(f16x2, z))); }
DI void gld4_if(int cond, u32x4& r0, u32x4& r1, u32x4& r2, u32x4& r3, const char* sbase, u32 voff) {
  asm volatile("s_cmp_eq_u32 %6, 0\n\ts_cbranch_scc1 .Lskip%=\n\tglobal_load_dwordx4 %0, %4, %5\n\tglobal_load_dwordx4 %1, %4, %5 offset:1024\n\t"
               "global_load_dwordx4 %2, %4, %5 offset:2048\n\tglobal_load_dwordx4 %3, %4, %5 offset:3072\n.Lskip%=:"
               : "+v"(r0), "+v"(r1), "+v"(r2), "+v"(r3) : "v"(voff), "s"(sbase), "s"(cond) : "scc");
}
DI constexpr int dn_nA(int s) { return s < 3 ? 3 : s == 8 || s == 10 || s == 13 ? 3 : 0; }
DI constexpr int dn_nD(int s) { return s >= 5 && s < 8 ? 4 : s == 8 ? 1 : s >= 9 && s < 14 ? 2 : 0; }
DI constexpr int dn_cumA(int s) { int c = 6; for (int u = 0; u < s; ++u) c += dn_nA(u); return c; }
DI constexpr int dn_young(int s, bool old, int a_idx, int d_step) {
  int pos = 0, tgt = -1, na = 6;
  if (a_idx >= 0 && a_idx < 6) tgt = a_idx;
  pos = 6;
  for (int u = 0; u < s; ++u) {
    if (old) { pos += dn_nD(u); if (u == d_step) tgt = pos - 1; }
    for (int q = 0; q < dn_nA(u); ++q) { if (na == a_idx) tgt = pos; ++na; ++pos; }
  }
  return tgt < 0 ? 63 : pos - 1 - tgt;
}
DI constexpr int dn_wait(int s, bool old) {
  const int x = dn_young(s, old, s, -1), y = s == 13 ? dn_young(s, old, -1, 8) : s == 18 ? dn_young(s, old, -1, 13) : 63;
  return x < y ? x : y;
}
__global__ void __launch_bounds__(512, 2) NOPK k2(P2 p) {
  const int tid = tidx(), lane = tid & 63;
  int w = __builtin_amdgcn_readfirstlane(tid >> 6);
  const int b = bidx();
  int mt = 4 * ((b & 7) >> 1) + (b >> 6), j = 8 * (b & 1) + ((b >> 3) & 7);
  const int c32 = lane & 31, h = lane >> 5;
  const char* ws = p.ws;
  const __amdgpu_buffer_rsrc_t rs = __builtin_amdgcn_make_buffer_rsrc(p.ws, 0, WS_END, 0x00020000);
  const u32 l16 = lane * 16;
#define PHASE() OPQ(w); OPQ(mt); OPQ(j); OPQ(ws); const int tt = w & 3, kh = w >> 2, th = j & 1, n8 = j >> 1; (void)tt; (void)kh; (void)th; (void)n8
  u32x4 a[24];
  u32x4 st[18];
  const int rsw = c32 ^ (16 * (w >> 2));
  const u32 frow = (h * 32 + rsw) * 16;
  u32 imga[8];
  {
    PHASE();
    const char* wq = ws + WS_WQF + (size_t)j * WQB;
    const char* wqw = wq + (kh * 20 + tt) * 1024;
    const u32 wql = (kh * 20 + tt) * 1024;
    const char* n1 = ws + WS_N1 + (size_t)((mt * 4 + tt) * 40 + kh * 20) * 1024;
    auto issA = [&](auto k_) LAM { constexpr int k = decltype(k_)::v; gld<(k & 3) * 1024>(a[k], n1 + (k >> 2) * 4096, l16); };
    auto issW = [&](int s, int nb) LAM { dma(wqw + nb * 40960 + s * 4096, l16, wql + nb * 40960 + s * 4096); };
    const int rk16 = w < 5 ? 5 * (mt >> 1) + w : (mt * 2 + th) * 3 + (w - 5);
    const char* rsrc = w < 5 ? (const char*)((mt & 1) ? p.wu : p.wg) + (size_t)rk16 * 49152 + 192 * j : (const char*)p.wd + (size_t)rk16 * 20480 + 160 * n8;
    const u32 rimg = 122880 + w * 3072;
    auto issR = [&]() LAM {
#pragma unroll
      for (int m = 0; m < 3; ++m) {
        const u32 x = 64 * m + lane, i5 = x < 160 ? x : 159, r12 = (x * 171) >> 11, r10 = (i5 * 205) >> 11;
        dma(rsrc, w < 5 ? r12 * 2880 + x * 16 : r10 * 1120 + i5 * 16, rimg + m * 1024);
      }
    };
    u32x4 mp[2];
    u32 mdst; bool plus1;
    {
      const int m1 = tid >= 160, k1 = 4 * (tid - 160 * m1);
      const int u2 = tid - 320, m2 = (u2 * 205) >> 12, e2 = u2 - 20 * m2;
      const int col2 = (m2 == 0 ? 1280 : m2 == 2 ? 3200 : m2 == 4 ? 1920 : 2560) + 80 * (m2 < 3 ? n8 : (mt >> 1)) + 4 * (m2 < 5 ? e2 : 0);
      const int col = tid < 320 ? 640 * m1 + k1 : col2;
#pragma unroll
      for (int q = 0; q < 2; ++q) gld<0>(mp[q], ws + WS_MODP + q * 15360, (u32)col * 4);
      sfor<0, 4>(issA);
      issW(0, 0); issW(0, 1); issW(0, 2);
      mdst = tid < 320 ? L_GS + (k1 >> 3) * 32 + (m1 ? 0 : 16) + (k1 & 4) * 2 : L_MODS + u2 * 16;
      plus1 = tid < 320 ? m1 != 0 : (m2 & 1) != 0;
    }
    if (tid == 0) st128(L_FLAG, u32x4{0u, 0u, 0u, 0u});
    f32x16 acc[3];
    const u32 wb = kh * 20480 + frow, gb_ = L_GS + kh * 1280 + h * 32;
    u32x4 gs[3][2], wf[2][3], ro[3];
    f32x4 csum = {0.f, 0.f, 0.f, 0.f};
    auto ldgs = [&](int k) LAM { gs[k % 3][0] = lds128(gb_ + k * 64); gs[k % 3][1] = lds128(gb_ + k * 64 + 16); };
    auto modulate = [&](int k) LAM {
      PIN(a[k]);
#pragma unroll
      for (int e = 0; e < 4; ++e)
        a[k][e] = pkfma16(a[k][e], gs[k % 3][0][e], gs[k % 3][1][e]);
    };
    WAITV(7);
    f32x4 mv;
    {
#pragma unroll
      for (int q = 0; q < 2; ++q) PIN(mp[q]);
      float mxv = 0.f;
#pragma unroll
      for (int e = 0; e < 4; ++e) {
        const float s = rbf(u2f(mp[0][e]) + u2f(mp[1][e]));
        mv[e] = plus1 ? rbf(1.f + s) : s;
        mxv = __builtin_fmaxf(mxv, __builtin_fabsf(mv[e]));
      }
      if (tid < 320) st64(mdst, u32x2{pkh2(mv[0], mv[1]), pkh2(mv[2], mv[3])}); else if (tid < 420) stf4(mdst, mv);
      const bool big = __builtin_amdgcn_ballot_w64(tid < 320 && !(mxv < 2000.f)) != 0ull;
      if (lane == 0) *(LAS u32*)(size_t)(L_FLAG + 16 + w * 4) = big;
    }
    WAITV(0);
    BAR();
    auto grpA = [&](int cond, int s) LAM { gld4_if(cond, a[4 * s], a[4 * s + 1], a[4 * s + 2], a[4 * s + 3], n1 + s * 4096, l16); };
    const int kl = kh ^ 1;
    auto grpW = [&](int s) LAM { issW(s, 0); issW(s, 1); issW(s, 2); };
    grpA(kh, 1); if (kh) { grpW(1); issR(); }
    bool scaled;
    {
      const u32x4 f0 = lds128(L_FLAG + 16);
      const u32 f1 = *(const LAS u32*)(size_t)(L_FLAG + 32);
      ldgs(0); ldgs(1);
      scaled = __builtin_amdgcn_readfirstlane(f0.x | f0.y | f0.z | f0.w | f1) != 0;
      if (scaled) {
        BAR();
        const float sc = 9.765625e-4f;
        if (tid < 320) st64(mdst, u32x2{pkh2(mv[0] * sc, mv[1] * sc), pkh2(mv[2] * sc, mv[3] * sc)});
        BAR();
        ldgs(0); ldgs(1);
      }
    }
    modulate(0);
    SB();
    const Run<30, 8, false> rb3(wq + 122880, 122880, w);
    sfor<0, 21>([&](auto k_) LAM {
      constexpr int k = decltype(k_)::v, s = k >> 2, i = k & 3;
      if constexpr (i == 3 && k < 16) WAITV(k == 3 ? 13 : 10);
      if constexpr (i == 0 && k > 0 && k < 20) {
        WAITV(k == 4 ? 10 : k == 16 ? 4 : 7); BAR();
      }
      if constexpr (k < 20) {
#pragma unroll
        for (int nb = 0; nb < 3; ++nb) wf[k & 1][nb] = lds128(wb + nb * 40960 + k * 1024);
      }
      if constexpr (k == 1) {
        f32x16 z;
#pragma unroll
        for (int e = 0; e < 16; ++e) z[e] = 0.f;
#pragma unroll
        for (int nb = 0; nb < 3; ++nb) acc[nb] = MFMA32H(wf[0][nb], a[0], z);
      } else if constexpr (k > 1) {
#pragma unroll
        for (int nb = 0; nb < 3; ++nb) acc[nb] = MFMA32H(wf[(k - 1) & 1][nb], a[k - 1], acc[nb]);
      }
      if constexpr (k + 1 < 20) modulate(k + 1);
      if constexpr (k + 2 < 20) ldgs(k + 2);
      if constexpr (k == 8) {
        const u32 q4 = (lane >> 2) & 3, nh = (lane >> 4) & 1;
        const u32 r0 = rimg + (w < 5 ? (8 * h + q4) * 192 + 32 * nh : (4 * nh + q4) * 160 + 32 * h) + (lane & 3) * 8, r1 = r0 + (w < 5 ? 768 : 1280);
#pragma unroll
        for (int u = 0; u < 3; ++u) ro[u] = ldstr2(r0 + 64 * u, r1 + 64 * u);
      }
      if constexpr (k == 9 || k == 10) {
        if (w < 5) {
          const u32 ga_ = L_MODS + 960 + w * 64 + h * 32;
          const f32x4 g0 = ldsf4(ga_), g1 = ldsf4(ga_ + 16), s0 = ldsf4(ga_ + 320), s1 = ldsf4(ga_ + 336);
#pragma unroll
          for (int u = (k == 9 ? 0 : 2); u < (k == 9 ? 2 : 3); ++u) {
            float c = 0.f, bi = 0.f;
#pragma unroll
            for (int e = 0; e < 4; ++e) {
              const u32 v = ro[u][e];
              const float x0 = lo16(v), x1 = hi16(v), ge = e < 2 ? g0[2 * e] : g1[2 * e - 4], go = e < 2 ? g0[2 * e + 1] : g1[2 * e - 3];
              const float se = e < 2 ? s0[2 * e] : s1[2 * e - 4], so_ = e < 2 ? s0[2 * e + 1] : s1[2 * e - 3];
              c = fma1(ge, x0, c); c = fma1(go, x1, c); bi = fma1(se, x0, bi); bi = fma1(so_, x1, bi);
            }
            auto r = __builtin_amdgcn_permlane32_swap(f2u(c), f2u(bi), false, false);
            *(LAS float*)(size_t)(w * 768 + u * 256 + (c32 >> 3) * 64 + h * 32 + (c32 & 7) * 4) = u2f(r[0]) + u2f(r[1]);
          }
        }
      }
      if constexpr (k == 17) {
        if (w == 7) {
#pragma unroll
          for (int u = 0; u < 5; ++u) csum += ldsf4(u * 768 + (lane < 48 ? l16 : 0u));
        }
      }
      if constexpr (k == 16) {
        const u32 x2 = 16 * kh + 4 * h, tb = 12288 + (32 * tt + c32) * 48 + 2 * x2;
#pragma unroll
        for (int u = 0; u < 8; ++u) {
          const u32 part = ((x2 + 32 * (u >> 1) + 8 * (u & 1)) * 205) >> 12;
          imga[u] = part * 18392 + tb + 64 * (u >> 1) + 16 * (u & 1); PIN(imga[u]);
        }
      }
      SB();
      if constexpr (k == 0) { grpA(kl, 1); if (!kh) { grpW(1); issR(); } }
      if constexpr (k == 1) grpA(kl, 2);
      if constexpr (k == 2) { if (!kh) grpW(2); grpA(kh, 2); }
      if constexpr (k == 3) { if (kh) grpW(2); }
      if constexpr (s == 1 || s == 2) {
        if constexpr (i == 0) grpA(kl, s + 2);
        if constexpr (i == 1) { if (!kh) grpW(s + 2); }
        if constexpr (i == 2) grpA(kh, s + 2);
        if constexpr (i == 3) { if (kh) grpW(s + 2); }
      }
      if constexpr (k == 12) { if (!kh) rb3.all(l16); }
      if constexpr (k == 14) { if (kh) rb3.all(l16); }
      if constexpr (k == 16) {
        u32 off0, du;
        if (w < 5) { const int k2 = rk16 >= 20, i2 = rk16 - 20 * k2; off0 = WS_WGF + j * WGB + (k2 * 20 + i2) * 2048 + (mt & 1) * 1024 + l16; du = 81920; }
        else { const int k2 = rk16 >= 48, kk = rk16 - 48 * k2; off0 = WS_WDF + n8 * WDB + ((kk >> 1) * 10 + k2 * 5 + h) * 1024 + (kk & 1) * 512 + c32 * 16; du = 2048; }
        asm volatile("" ::: "memory");
        __builtin_amdgcn_raw_buffer_store_b128(ro[0], rs, off0, 0, 16);
        __builtin_amdgcn_raw_buffer_store_b128(ro[1], rs, off0 + du, 0, 16);
        __builtin_amdgcn_raw_buffer_store_b128(ro[2], rs, (w < 5 || h == 0) ? off0 + 2 * du : 0xfffffff0u, 0, 16);
        asm volatile("" ::: "memory");
      }
      SB();
    });
    WAITV(3);
    BAR();
    if (w == 7 && lane < 48) __builtin_amdgcn_raw_buffer_store_b128(__builtin_bit_cast(u32x4, csum), rs, WS_CSP + (u32)(j * 16 + mt) * 768 + l16, 0, 16);
    const u32 exw = (w >> 1) * 18432 + (w & 1) * 6144 + l16, exr = ((w ^ 4) >> 1) * 18432 + (w & 1) * 6144 + l16;
#pragma unroll
    for (int nb = 0; nb < 3; ++nb) {
      stf4(exw + nb * 2048, f32x4{acc[nb][8], acc[nb][9], acc[nb][10], acc[nb][11]});
      stf4(exw + nb * 2048 + 1024, f32x4{acc[nb][12], acc[nb][13], acc[nb][14], acc[nb][15]});
    }
    if (kh) st64((tid >> 7) * 18432 + 12288 + (tid & 127) * 48 + 40, u32x2{0x3f803f80u, 0x3f803f80u});
    BAR();
    const float osc = scaled ? 1024.f : 1.f, qs = QSCALE * (1.f / 4096.f) * osc, sc2 = kh ? qs : osc;
#pragma unroll
    for (int nb = 0; nb < 3; ++nb)
#pragma unroll
      for (int q = 0; q < 2; ++q) {
        const f32x4 o = ldsf4(exr + nb * 2048 + q * 1024);
        float v0 = acc[nb][4 * q] + o[0], v1 = acc[nb][4 * q + 1] + o[1], v2 = acc[nb][4 * q + 2] + o[2], v3 = acc[nb][4 * q + 3] + o[3];
        if (nb == 2) { v0 *= sc2; v1 *= sc2; v2 *= sc2; v3 *= sc2; }
        else if (scaled) { v0 *= osc; v1 *= osc; v2 *= osc; v3 *= osc; }
        st64(imga[2 * nb + q], u32x2{pk2(v0, v1), pk2(v2, v3)});
      }
    BAR();
    {
      const u32 dst = WS_KVX + (u32)(mt * 16 + j) * KVB + w * 3072 + l16, src = (w >> 1) * 18432 + 12288 + (w & 1) * 3072 + l16;
#pragma unroll
      for (int i = 0; i < 3; ++i) __builtin_amdgcn_raw_buffer_store_b128(lds128(src + i * 1024), rs, dst + i * 1024, 0, 16);
    }
    f32x16 acc3;
#pragma unroll
    for (int e = 0; e < 16; ++e) acc3[e] = 0.f;
    {
      const int r3 = rsw < 24 ? rsw : (rsw < 28 ? rsw - 4 : rsw - 12);
      const u32 wb3 = 122880 + kh * 15360 + (h * 24 + r3) * 16;
      u32x4 w3[4];
#pragma unroll
      for (int k = 0; k < 3; ++k) w3[k] = lds128(wb3 + k * 768);
#pragma unroll
      for (int k = 0; k < 20; ++k) {
        if (k + 3 < 20) w3[(k + 3) & 3] = lds128(wb3 + (k + 3) * 768);
        acc3 = MFMA32H(w3[k & 3], a[k], acc3);
        if (k == 9) {
          WAITVM(0);
          if (lane == 0) {
            const u32 old = __hip_atomic_fetch_add((LAS u32*)(size_t)L_FLAG, 1u, __ATOMIC_RELAXED, __HIP_MEMORY_SCOPE_WORKGROUP);
            if (old == 7u) {
              __hip_atomic_store((gu32*)(ws + WS_KVF + (mt * 16 + j) * 128), 1u, RLX_AGENT);
              signal(ws, C_RG + j * 256); signal(ws, C_RD + n8 * 256);
            }
          }
        }
        SB();
      }
    }
    const u32 ex3w = (w < 4 ? 73728 : 84992) + w * 2048 + l16, ex3r = (w < 4 ? 93184 : 65536) + w * 2048 + l16;
    stf4(ex3w, f32x4{acc3[8], acc3[9], acc3[10], acc3[11]});
    stf4(ex3w + 1024, f32x4{acc3[12], acc3[13], acc3[14], acc3[15]});
    BAR();

#pragma unroll
    for (int q = 0; q < 2; ++q) {
      const f32x4 o = ldsf4(ex3r + q * 1024);
      st64(imga[6 + q], u32x2{pk2((acc3[4 * q] + o[0]) * qs, (acc3[4 * q + 1] + o[1]) * qs), pk2((acc3[4 * q + 2] + o[2]) * qs, (acc3[4 * q + 3] + o[3]) * qs)});
    }
    BAR();
  }
  u32x2 xr[3];
  const int c16 = lane & 15, g4 = lane >> 4;
  {
    PHASE();
    const int hh = w >> 2, qb = w < 4 ? w : 7 - w;
    u32x4 q0f, q1f;
    {
      const u32 qa = (4 + hh) * 18432 + 12288 + (32 * qb + c32) * 48;
      const u32x2 x2 = lds64(qa + 32);
      q0f = lds128(qa + 16 * h);
      q1f = u32x4{h ? 0u : x2.x, h ? 0u : x2.y, 0u, 0u};
      const int tok64 = 128 * mt + 64 * th + 16 * tt + c16;
#pragma unroll
      for (int m = 0; m < 3; ++m) gld64(xr[m], (const char*)p.x, (u32)(tok64 * 1280 + (80 * n8 + 16 * ((m + 3 * kh) % 5) + 4 * g4) * 2));
    }
    BAR();
    const char* sg0 = ws + WS_WGF + (size_t)j * WGB + (kh * 40 + tt * 10) * 1024;
    const int tmin = mt >= 2 ? 0 : 8 - 4 * mt - qb;
    const u32 kb = hh * 18432 + (32 * qb + c32) * 48, kb2 = kb + 16 * h;
    const u32 vb = (2 + hh) * 18432 + (32 * qb + 4 * h + ((lane >> 2) & 3)) * 48 + ((lane >> 4) & 1) * 32 + (lane & 3) * 8;
    const int qlim = c32 - 4 * h;
    const int dw = (w - 2) & 3;
    const bool duty = w >= 2 && w <= 5;
    const Run<70, 4, false> rwo(ws + WS_WOF + (size_t)n8 * WOB, wo_slot(0), dw);
    u32 ob = WS_OAF + (u32)((mt * 4 + qb) * 40) * 1024 + c32 * 16 + 5 * j * 512;
    PIN(ob);
    auto lds_wait = [&](u32 addr, u32 want) LAM {
      for (u32 spins = 0; spins < SPIN_CAP * 8; ++spins) {
        if ((u32)__builtin_amdgcn_readfirstlane(__hip_atomic_load((LAS u32*)(size_t)addr, __ATOMIC_RELAXED, __HIP_MEMORY_SCOPE_WORKGROUP)) >= want) break;
        __builtin_amdgcn_s_sleep(1);
      }
      asm volatile("" ::: "memory");
    };
    {
      if (!duty) {
        const int sl = w >= 6;
        const u32 l0 = (w & 1) * 36864 + sl * 6144;
        if (mt + sl >= 2) {
          poll_ge(ws, WS_KVF + ((mt - 2 + sl) * 16 + j) * 128, 1u);
          const char* src = ws + WS_KVX + (size_t)((mt - 2 + sl) * 16 + j) * KVB + (w & 1) * 12288;
#pragma unroll
          for (int u = 0; u < 4; ++u) {
            const u32 l1 = l0 + (u >> 1) * 18432 + (u & 1) * 3072;
            dmai<0, true>(src + u * 3072, l16, l1); dmai<1024, true>(src + u * 3072, l16, l1); dmai<2048, true>(src + u * 3072, l16, l1);
          }
        } else {
#pragma unroll
          for (int u = 0; u < 12; ++u) st128(l0 + (u / 6) * 18432 + (u % 6) * 1024 + l16, u32x4{0u, 0u, 0u, 0u});
        }
      }
    }
    auto halo = [&]() LAM {
      if (!duty) {
        asm volatile("s_waitcnt vmcnt(0) lgkmcnt(0)" ::: "memory");
        if (lane == 0) __hip_atomic_fetch_add((LAS u32*)(size_t)(L_FLAG + (w >= 6 ? 8 : 4)), 1u, __ATOMIC_RELAXED, __HIP_MEMORY_SCOPE_WORKGROUP);
      }
    };
    f32x16 oacc;
    u32 rcnt;
    auto attn = [&](auto full_) LAM {
      constexpr bool FULL = decltype(full_)::v;
      u32x4 ka, kq, vf[2][2];
      kq = u32x4{0u, 0u, 0u, 0u};
      auto ldkv = [&](int t) LAM {
        const u32x2 x2 = lds64(kb + t * 1536 + 32);
        ka = lds128(kb2 + t * 1536); kq.x = x2.x; kq.y = x2.y;
#pragma unroll
        for (int s2 = 0; s2 < 2; ++s2) { const u32 va = vb + (32 * t + 16 * s2) * 48; vf[t & 1][s2] = ldstr2(va, va + 384); }
      };
      auto splat = [&](float v) LAM -> f32x16 {
        f32x16 c;
#pragma unroll
        for (int e = 0; e < 16; ++e) c[e] = v;
        return c;
      };
      auto pre = [&](auto m_) LAM {
        constexpr int m = decltype(m_)::v;
        if constexpr (m == 2) gld32_sc1(rcnt, ws, lane < 32 ? C_RG + j * 256 : C_RD + n8 * 256);
        if constexpr (m == 4) {
          if (duty) WAITV(4); else WAITV(0);
          PIN(rcnt);
          if (__builtin_amdgcn_ballot_w64(rcnt < (lane < 32 ? 16u : 32u)) != 0ull) { poll_ge(ws, C_RG + j * 256, 16u); poll_ge(ws, C_RD + n8 * 256, 32u); }
        }
        if constexpr (m >= 4) {
          gld_sc1<((2 * m - 8) & 3) * 1024>(st[2 * m - 8], sg0 + ((2 * m - 8) >> 2) * 4096, l16);
          gld_sc1<((2 * m - 7) & 3) * 1024>(st[2 * m - 7], sg0 + ((2 * m - 7) >> 2) * 4096, l16);
        }
        if (duty) {
          rwo.go<2 * m>(l16); rwo.go<2 * m + 1>(l16);
          asm volatile("" ::: "memory");
        }
        SB();
      };
      auto sync = [&](int t) LAM {
        if (t >= 4) { if (qb == 7 - t) lds_wait(L_FLAG + 8, 2u); } else { if (qb == 3 - t) lds_wait(L_FLAG + 4, 2u); }
      };
      f32x16 sb[2], cm;
      float pe[16];
      ldkv(8);
      sb[0] = MFMA32(ka, q0f, splat(0.f));
      sb[0] = MFMA32(kq, q1f, sb[0]);
      SB();
      pre(IC<0>{});
#pragma unroll
      for (int e = 0; e < 16; ++e) pe[e] = ((e & 3) + 8 * (e >> 2) > qlim) ? -1.0e30f : sb[0][e];
      float mx = __builtin_fmaxf(pe[0], pe[1]);
#pragma unroll
      for (int e = 2; e < 16; e += 2) mx = max3(mx, pe[e], pe[e + 1]);
      mx = -max32(mx);
      cm = splat(mx);
      auto cin = [&](int t) LAM -> f32x16 { if constexpr (FULL) return cm; else return splat(t >= tmin ? mx : -1.0e30f); };
#pragma unroll
      for (int s2 = 0; s2 < 2; ++s2) {
        u32x4 pf;
#pragma unroll
        for (int e = 0; e < 4; ++e) pf[e] = pk2(__builtin_amdgcn_exp2f(pe[8 * s2 + 2 * e] + mx), __builtin_amdgcn_exp2f(pe[8 * s2 + 2 * e + 1] + mx));
        if (s2 == 0) oacc = MFMA32(vf[0][0], pf, splat(0.f)); else oacc = MFMA32(vf[0][1], pf, oacc);
      }
      SB();
      halo();
      pre(IC<1>{});
      sync(7);
      ldkv(7);
      sb[1] = MFMA32(ka, q0f, cin(7));
      sb[1] = MFMA32(kq, q1f, sb[1]);
      SB();
      sfor<0, 8>([&](auto u_) LAM {
        constexpr int t = 7 - decltype(u_)::v, c = t & 1, n = c ^ 1;
        u32x4 pf0, pf1;
        if constexpr (t > 0) { if constexpr (t - 1 != 7) sync(t - 1); ldkv(t - 1); }
        if constexpr (t == 0) {
#pragma unroll
          for (int e = 0; e < 16; ++e) sb[c][e] = ((e & 3) + 8 * (e >> 2) > qlim) ? sb[c][e] : -1.0e30f;
        }
#pragma unroll
        for (int e = 0; e < 8; ++e) pe[e] = __builtin_amdgcn_exp2f(sb[c][e]);
        SB();
        if constexpr (t > 0) { sb[n] = MFMA32(ka, q0f, cin(t - 1)); SB(); }
#pragma unroll
        for (int e = 8; e < 12; ++e) pe[e] = __builtin_amdgcn_exp2f(sb[c][e]);
#pragma unroll
        for (int e = 0; e < 4; ++e) pf0[e] = pk2(pe[2 * e], pe[2 * e + 1]);
        SB();
        if constexpr (t > 0) { sb[n] = MFMA32(kq, q1f, sb[n]); SB(); }
#pragma unroll
        for (int e = 12; e < 16; ++e) pe[e] = __builtin_amdgcn_exp2f(sb[c][e]);
        SB();
        oacc = MFMA32(vf[c][0], pf0, oacc);
        SB();
#pragma unroll
        for (int e = 0; e < 4; ++e) pf1[e] = pk2(pe[8 + 2 * e], pe[8 + 2 * e + 1]);
        SB();
        if constexpr (t > 0) pre(IC<9 - t>{});
        oacc = MFMA32(vf[c][1], pf1, oacc);
        SB();
      });
    };
    auto exact = [&]() LAM {
      auto sc = [&](int t, float c0, f32x16& s) LAM {
        const u32x2 x2 = lds64(kb + t * 1536 + 32);
#pragma unroll
        for (int e = 0; e < 16; ++e) s[e] = c0;
        s = MFMA32(lds128(kb2 + t * 1536), q0f, s);
        s = MFMA32((u32x4{x2.x, x2.y, 0u, 0u}), q1f, s);
#pragma unroll
        for (int e = 0; e < 16; ++e) s[e] = (u32)(32 * t + (e & 3) + 8 * (e >> 2) - qlim - 1) < 256u ? s[e] : -1.0e30f;
      };
      float mx = -1.0e30f;
      f32x16 s;
#pragma nounroll
      for (int t = tmin; t < 9; ++t) {
        sc(t, 0.f, s);
#pragma unroll
        for (int e = 0; e < 16; e += 2) mx = max3(mx, s[e], s[e + 1]);
      }
      mx = -max32(mx);
#pragma unroll
      for (int e = 0; e < 16; ++e) oacc[e] = 0.f;
#pragma nounroll
      for (int t = tmin; t < 9; ++t) {
        sc(t, mx, s);
#pragma unroll
        for (int s2 = 0; s2 < 2; ++s2) {
          u32x4 pf;
#pragma unroll
          for (int e = 0; e < 4; ++e) pf[e] = pk2(__builtin_amdgcn_exp2f(s[8 * s2 + 2 * e]), __builtin_amdgcn_exp2f(s[8 * s2 + 2 * e + 1]));
          const u32 va = vb + (32 * t + 16 * s2) * 48;
          oacc = MFMA32(ldstr2(va, va + 384), pf, oacc);
        }
      }
    };
    if (mt >= 2) attn(BoolC<true>{}); else attn(BoolC<false>{});
    if (__builtin_amdgcn_ballot_w64(h != 0 && !(oacc[8] < ATT_LIMIT)) != 0ull) exact();
    const float lx = xor32f(oacc[8]);
    const float inv = __builtin_amdgcn_rcpf(64.f * (h ? oacc[8] : lx));
    {
      u32 ov[6];
#pragma unroll
      for (int e = 0; e < 6; ++e) ov[e] = pk2(oacc[2 * e] * inv, oacc[2 * e + 1] * inv);
      const auto x0 = __builtin_amdgcn_permlane32_swap(ov[0], ov[2], false, false), x1 = __builtin_amdgcn_permlane32_swap(ov[1], ov[3], false, false);
      if (hh == 0) {
        __builtin_amdgcn_raw_buffer_store_b128(u32x4{x0[0], x1[0], x0[1], x1[1]}, rs, ob + h * 512, 0, 16);
        if (h == 0) __builtin_amdgcn_raw_buffer_store_b64(u32x2{ov[4], ov[5]}, rs, ob + 1024, 0, 16);
      } else {
        const auto y0 = __builtin_amdgcn_permlane32_swap(ov[2], ov[4], false, false), y1 = __builtin_amdgcn_permlane32_swap(ov[3], ov[5], false, false);
        __builtin_amdgcn_raw_buffer_store_b128(u32x4{x0[1], x1[1], y0[0], y1[0]}, rs, ob + 1536 + h * 512, 0, 16);
        if (h == 0) __builtin_amdgcn_raw_buffer_store_b64(u32x2{ov[0], ov[1]}, rs, ob + 1032, 0, 16);
      }
    }
    WAITVM(0);
    BAR();
    if (tid == 0) signal(ws, C_O + mt * 256);
  }
  u32 wbase[5], avoff;
  u32x2 x1k[3];
  f32x4 c5[5];
  {
    PHASE();
    const char* sg1 = ws + WS_WGF + (size_t)j * WGB + 81920 + (kh * 40 + tt * 8) * 1024;
    auto stage1 = [&]() LAM { sfor<0, 8>([&](auto m_) LAM { constexpr int m = decltype(m_)::v; gld_sc1<(m & 3) * 1024>(st[10 + m], sg1 + (m >> 2) * 4096, l16); }); };
    if (w >= 4) { __builtin_amdgcn_s_sleep(2); Run<30, 4, false>(ws + WS_WOF + (size_t)n8 * WOB + 71680, wo_slot(7), w - 4).all(l16); }
    stage1();
#pragma unroll
    for (int m = 0; m < 5; ++m) wbase[m] = (kh * 5 + (m + 3 * kh) % 5) * 1024 + l16;
    avoff = (g4 >> 1) * 1024 + (g4 & 1) * 512 + (16 * (tt & 1) + c16) * 16;
    const int tt64 = mt * 4 + 2 * th + (tt >> 1);
    const char* ab = ws + WS_OAF + (size_t)(tt64 * 40 + 20 * kh) * 1024;
    const u32 ew = 73728 + (tt * 5) * 1024 + l16;
    u32 xb = WS_X1 + (u32)(tt64 * 40 + 5 * n8) * 1024 + (g4 >> 1) * 512 + (16 * (tt & 1) + c16) * 16 + (g4 & 1) * 8 + kh * 3072;
    u32 sta = WS_STAT + (u32)((tt64 * 8 + n8) * 32 + 16 * (tt & 1) + c16) * 16 + kh * 8;
    u32 mo = L_MODS + kh * 192 + g4 * 16;
    PIN(xb); PIN(sta); PIN(mo); PIN(avoff);
    auto iss = [&](auto s_) LAM { constexpr int s = decltype(s_)::v; gld_sc1<(s & 1) * 2048>(a[s], ab + (s >> 1) * 4096, avoff); };
    u32x4 wf[3][5];
#pragma unroll
    for (int s = 0; s < 2; ++s)
#pragma unroll
      for (int m = 0; m < 5; ++m) wf[s][m] = lds128(wbase[m] + wo_slot(s));
#pragma unroll
    for (int m = 0; m < 5; ++m) c5[m] = f32x4{0.f, 0.f, 0.f, 0.f};
    if (w == 0) poll_ge(ws, C_O + mt * 256, 16u);
    BAR();
    sfor<0, 5>(iss);
    sfor<0, 10>([&](auto s_) LAM {
      constexpr int s = decltype(s_)::v;
      WAITV(s == 0 ? 4 : s == 1 ? 6 : 9 - s);
      if (s == 4) BAR();
      PIN(a[s]);
      if constexpr (s + 2 < 10) {
#pragma unroll
        for (int m = 0; m < 5; ++m) wf[(s + 2) % 3][m] = lds128(wbase[m] + wo_slot(s + 2));
      }
#pragma unroll
      for (int m = 0; m < 5; ++m) c5[m] = MFMA16(wf[s % 3][m], a[s], c5[m]);
      SB();
      if constexpr (s == 0) sfor<5, 8>(iss);
      if constexpr (s == 1) sfor<8, 10>(iss);
      SB();
    });
    stf4(ew + ((3 + 3 * kh) % 5) * 1024, c5[3]);
    stf4(ew + ((4 + 3 * kh) % 5) * 1024, c5[4]);
    if (kh) stf4(ew, c5[2]);
    BAR();
    float xs[12];
    u32x2 xg[3];
#pragma unroll
    for (int m = 0; m < 3; ++m) PIN(xr[m]);
#pragma unroll
    for (int m = 0; m < 3; ++m) {
      if (m < 2 || kh == 0) {
        const f32x4 o = ldsf4(ew + kh * 3072 + m * 1024), ga = ldsf4(mo + m * 64), g2 = ldsf4(mo + 320 + m * 64);
        const u32 y01 = pk2(c5[m][0] + o[0], c5[m][1] + o[1]), y23 = pk2(c5[m][2] + o[2], c5[m][3] + o[3]);
        const u32 t01 = pk2(ga[0] * lo16(y01), ga[1] * hi16(y01)), t23 = pk2(ga[2] * lo16(y23), ga[3] * hi16(y23));
        x1k[m] = u32x2{pk2(lo16(xr[m].x) + lo16(t01), hi16(xr[m].x) + hi16(t01)), pk2(lo16(xr[m].y) + lo16(t23), hi16(xr[m].y) + hi16(t23))};
        xs[4 * m] = lo16(x1k[m].x); xs[4 * m + 1] = hi16(x1k[m].x); xs[4 * m + 2] = lo16(x1k[m].y); xs[4 * m + 3] = hi16(x1k[m].y);
        xg[m] = u32x2{pk2(xs[4 * m] * g2[0], xs[4 * m + 1] * g2[1]), pk2(xs[4 * m + 2] * g2[2], xs[4 * m + 3] * g2[3])};
        if (m == 1) {
          const auto x0 = __builtin_amdgcn_permlane16_swap(xg[0].x, xg[1].x, false, false), x1 = __builtin_amdgcn_permlane16_swap(xg[0].y, xg[1].y, false, false);
          __builtin_amdgcn_raw_buffer_store_b128(u32x4{x0[0], x1[0], x0[1], x1[1]}, rs, xb + (g4 & 1) * 1016, 0, 16);
        }
        if (m == 2) __builtin_amdgcn_raw_buffer_store_b64(xg[2], rs, xb + 2048, 0, 16);
      } else {
        x1k[m] = u32x2{0u, 0u};
        xs[8] = xs[9] = xs[10] = xs[11] = 0.f;
      }
    }
    float s = ((xs[0] + xs[1]) + (xs[2] + xs[3])) + ((xs[4] + xs[5]) + (xs[6] + xs[7])) + ((xs[8] + xs[9]) + (xs[10] + xs[11]));
    s = sum32(sum16(s));
    const float mean = s * (kh ? 1.f / 32.f : 1.f / 48.f);
    float q0 = 0.f, q1 = 0.f;
#pragma unroll
    for (int e = 0; e < 4; ++e) { const float d0 = xs[e] - mean, d1 = xs[4 + e] - mean; q0 = fma1(d0, d0, q0); q1 = fma1(d1, d1, q1); }
    if (kh == 0) {
#pragma unroll
      for (int e = 8; e < 12; ++e) { const float d = xs[e] - mean; q0 = fma1(d, d, q0); }
    }
    const float q = sum32(sum16(q0 + q1));
    if (g4 == 0) __builtin_amdgcn_raw_buffer_store_b64(u32x2{f2u(mean), f2u(q)}, rs, sta, 0, 16);
    WAITVM(0);
    BAR();
    if (tid == 0) signal(ws, WS_CNT + (16 + mt) * 256);
  }
  {
    PHASE();
    const bool old4 = w < 4;
    const char* wgb = ws + WS_WGF + (size_t)j * WGB;
    const char* wdb = ws + WS_WDF + (size_t)n8 * WDB;
    u32x4 stv[4], csv[2];
    const char* ab = ws + WS_X1 + (size_t)((mt * 4 + tt) * 40 + 20 * kh) * 1024;
    const char* sb = ws + WS_STAT + (size_t)(mt * 4 + tt) * 4096;
    auto iss = [&](auto s_) LAM { constexpr int s = decltype(s_)::v; gld_sc1<(s & 3) * 1024>(a[s], ab + (s >> 2) * 4096, l16); };
    {
      const char* cb = ws + WS_CSP + (size_t)(j * 16 + 4 * (w >> 1) + (w & 1)) * 768;
      const u32 co = lane < 48 ? l16 : 0u;
      gld_sc1<0>(csv[0], cb, co); gld_sc1<1536>(csv[1], cb, co);
    }
#pragma unroll
    for (int m = 0; m < 18; ++m) { PIN(st[m]); st128((m < 10 ? (kh * 40 + tt * 10 + m) : (80 + kh * 32 + tt * 8 + m - 10)) * 1024 + l16, st[m]); }
    float c1 = 0.f, d2 = 0.f;
    u32x4 bcor = {0u, 0u, 0u, 0u};
    u32 sw = kh * 40960 + tt * 4096 + l16, sr = (kh ^ 1) * 40960 + tt * 4096 + l16;
    u32 actb = WS_ACT + (u32)((mt * 4 + tt) * 96 + 6 * j + kh) * 1024 + l16;
    const u32 so = h * 2048 + c32 * 16;
    PIN(actb);
    const auto rC = gu_run<1, 16, 20, 4>(wgb, w & 3);
    const auto rA = gu_run<2, 0, 8, 4>(wgb, w & 3);
    const auto rB = gu_run<2, 8, 20, 4>(wgb, w & 3);
    const Run<60, 8, true> rD(wdb, 0, w);
    const Run<90, 8, true> rS(wdb + 61440, 61440, w);
    if (w == 0) poll_ge(ws, WS_CNT + (16 + mt) * 256, 16u);
    BAR();
    sfor<0, 2>(iss);
    gld_sc1<0>(stv[0], sb, so); gld_sc1<512>(stv[1], sb, so); gld_sc1<1024>(stv[2], sb, so); gld_sc1<1536>(stv[3], sb, so);
    sfor<2, 6>(iss);
    f32x16 ag[2], au[2];
    u32x4 fg[3], fu[3], outv;
    u32 ub = 0;
    auto ldw = [&](auto g_) LAM {
      constexpr int g = decltype(g_)::v, pp = g / 20, i = g % 20;
      constexpr bool first = i == 0 || gu_unit(pp, 0, i) != gu_unit(pp, 0, i - 1) + 1;
      constexpr int i0 = [] { int x = i; while (x > 0 && gu_unit(pp, 0, x) == gu_unit(pp, 0, x - 1) + 1) --x; return x; }();
      if constexpr (first) ub = frow + (kh ? gu_unit(pp, 1, i) : gu_unit(pp, 0, i)) * 2048;
      fg[g % 3] = lds128(ub + (i - i0) * 2048); fu[g % 3] = lds128(ub + (i - i0) * 2048 + 1024);
    };
    auto xwrite = [&](int pp) LAM {
      const f32x16 &G = ag[pp & 1], &U = au[pp & 1];
      stf4(sw, f32x4{G[8], G[9], G[10], G[11]}); stf4(sw + 1024, f32x4{G[12], G[13], G[14], G[15]});
      stf4(sw + 2048, f32x4{U[8], U[9], U[10], U[11]}); stf4(sw + 3072, f32x4{U[12], U[13], U[14], U[15]});
    };
    f32x4 xo[4] = {};
    auto xread = [&]() LAM {
#pragma unroll
      for (int u = 0; u < 4; ++u) xo[u] = ldsf4(sr + u * 1024);
    };
    auto xadd = [&](int pp, int q) LAM {
#pragma unroll
      for (int i = 0; i < 4; ++i) { ag[pp & 1][4 * q + i] += xo[q][i]; au[pp & 1][4 * q + i] += xo[2 + q][i]; }
    };
    auto corr = [&](int pp) LAM {
      ag[pp & 1] = MFMA32(lds128(L_T2 + pp * 1024 + rsw * 16), bcor, ag[pp & 1]);
      au[pp & 1] = MFMA32(lds128(L_T2 + pp * 1024 + 512 + rsw * 16), bcor, au[pp & 1]);
    };
    float eu[2], ee[2];
    auto epi = [&](int pp, int o, int ph) LAM {
#pragma unroll
      for (int i = 0; i < 2; ++i) {
        if (ph == 0) { ee[i] = __builtin_amdgcn_exp2f(ag[pp & 1][2 * o + i] * c1); eu[i] = ag[pp & 1][2 * o + i] * au[pp & 1][2 * o + i]; }
        if (ph == 1) ee[i] = __builtin_amdgcn_rcpf(fma1(ee[i], d2, d2));
      }
      if (ph == 2) outv[o] = pk2(eu[0] * ee[0], eu[1] * ee[1]);
    };
    auto ostore = [&](int pp) LAM { __builtin_amdgcn_raw_buffer_store_b128(outv, rs, actb + pp * 2048, 0, 16); asm volatile("" ::: "memory"); };
    BAR();
    ldw(IC<0>{}); ldw(IC<1>{});
    auto nextA = [&](auto g_) LAM {
      constexpr int g = decltype(g_)::v;
      WAITV(g == 0 ? 9 : g == 1 ? 12 : g == 2 ? 11 : g == 3 ? 14 : 19 - g);
      pin4(a[g]);
    };
    nextA(IC<0>{});
    sfor<0, 60>([&](auto g_) LAM {
      constexpr int g = decltype(g_)::v, pp = g / 20, i = g % 20;
      if (g == 32) WAITV(0);
      if (g == 43) WAITV(4);
      if (g == 12 || g == 22 || g == 32 || g == 43 || g == 50) BAR();
      if constexpr (g + 2 < 60) ldw(IC<g + 2>{});
      if (g == 22 || g == 43) xread();
      if (i == 0) {
        f32x16 z;
#pragma unroll
        for (int e = 0; e < 16; ++e) z[e] = 0.f;
        ag[pp & 1] = MFMA32(fg[g % 3], a[i], z); au[pp & 1] = MFMA32(fu[g % 3], a[i], z);
      } else if (g == 45) {
        ag[0] = MFMA32(fg[g % 3], a[i], ag[0]); au[0] = MFMA32(fu[g % 3], a[i], au[0]);
        if (!kh) corr(2);
      } else {
        ag[pp & 1] = MFMA32(fg[g % 3], a[i], ag[pp & 1]); au[pp & 1] = MFMA32(fu[g % 3], a[i], au[pp & 1]);
      }
      if (g == 4) {
        PIN(csv[0]); PIN(csv[1]);
        f32x4 sm;
#pragma unroll
        for (int e = 0; e < 4; ++e) sm[e] = u2f(csv[0][e]) + u2f(csv[1][e]);
        if (lane < 48) stf4(L_GS + (w >> 1) * 1536 + (w & 1) * 768 + l16, sm);
      }
      if (g == 6) {
        float s = 0.f;
#pragma unroll
        for (int u = 0; u < 4; ++u) s += 48.f * u2f(stv[u][0]) + 32.f * u2f(stv[u][2]);
        c1 = sum32(s) * (1.f / 640.f);
      }
      if (g == 7) {
        float q = 0.f;
#pragma unroll
        for (int u = 0; u < 4; ++u) {
          const float d0 = u2f(stv[u][0]) - c1, d1 = u2f(stv[u][2]) - c1;
          q += u2f(stv[u][1]) + u2f(stv[u][3]) + 48.f * d0 * d0 + 32.f * d1 * d1;
        }
        d2 = sum32(q);
      }
      if (g == 8) {
        const float mean = c1, den = rbf(__builtin_amdgcn_sqrtf(rbf(rbf(d2 * (1.f / 640.f)) + 1e-5f)));
        c1 = -1.4426950408889634f * __builtin_amdgcn_rcpf(den); d2 = den * den;
        const u32 nm = pk2(-mean, -mean), dd = pk2(den, den);
        bcor = u32x4{h ? 0u : nm, h ? 0u : ((nm & 0xffffu) | (dd & 0xffff0000u)), h ? 0u : dd, 0u};
      }
      if (g == 12) {
        const u32 t5 = tid - 256 < 192 ? tid - 256 : 0, t4 = L_GS + (t5 >> 3) * 64 + (t5 & 7) * 4;
        if (kh) {
#pragma unroll
          for (int u = 0; u < 4; ++u) { xo[0][u] = ldsf(t4 + u * 1536); xo[1][u] = ldsf(t4 + u * 1536 + 32); }
        }
      }
      if (g == 14 && kh) {
        const float cs = (xo[0][0] + xo[0][1]) + (xo[0][2] + xo[0][3]), bi = (xo[1][0] + xo[1][1]) + (xo[1][2] + xo[1][3]);
        const u32 h0 = pk2(cs, bi);
        const float cr = cs - lo16(h0), br = bi - hi16(h0);
        const u32 h1 = pk2(cr, br), h2 = pk2(cr - lo16(h1), br - hi16(h1));
        const int t5 = tid - 256, gu = t5 >= 96, nn = t5 - 96 * gu;
        if (t5 < 192)
          st128(L_T2 + (nn >> 5) * 1024 + gu * 512 + (nn & 31) * 16,
                u32x4{__builtin_amdgcn_perm(h1, h0, 0x05040100u), __builtin_amdgcn_perm(h0, h2, 0x07060100u), __builtin_amdgcn_perm(h2, h1, 0x07060302u), 0u});
      }
      if (g == 21 || g == 41) xwrite(pp - 1);
      if (g == 22 || g == 42) corr(pp - 1);
      if (g == 24 || g == 25) xadd(0, g - 24);
      if (g >= 28 && g <= 37 && (g - 28) % 3 == 0) epi(0, (g - 28) / 3, 2);
      if (g >= 26 && g < 37 && (g - 26) % 3 < 2) epi(0, (g - 26) / 3, (g - 26) % 3);
      if (g == 44 || g == 45) xadd(1, g - 44);
      if (g >= 48 && g <= 57 && (g - 48) % 3 == 0) epi(1, (g - 48) / 3, 2);
      if (g >= 46 && g < 57 && (g - 46) % 3 < 2) epi(1, (g - 46) / 3, (g - 46) % 3);
      SGB(0x100, 6, 0); SGB(0x008, 1, 0); SGB(0x002, 4, 0); SGB(0x008, 1, 0);
      SB();
      if constexpr (g < 3) sfor<6 + 4 * g, 10 + 4 * g>(iss);
      if constexpr (g == 3) sfor<18, 20>(iss);
      if constexpr (g + 1 < 20) nextA(IC<g + 1>{});
      if constexpr (g == 4) { PIN(stv[0]); PIN(stv[1]); PIN(stv[2]); PIN(stv[3]); }
      if (g == 40) ostore(0);
      if constexpr (g >= 40 && g < 52) gld_sc1<((g - 40) & 3) * 1024>(a[g - 40], rS.src + ((g - 40) >> 2) * 4096, l16);
      if constexpr (g >= 50 && g < 58) rD.template go<g - 50>(l16);
      if (g == 57) ostore(1);
      if constexpr ((g >= 19 && g < 21) || (g >= 22 && g < 26) || (g >= 32 && g < 38)) {
        if (old4) {
          if constexpr (g < 21) { rC.template go<2 * (g - 19)>(l16); rC.template go<2 * (g - 19) + 1>(l16); }
          else if constexpr (g < 26) { rA.template go<2 * (g - 22)>(l16); rA.template go<2 * (g - 22) + 1>(l16); }
          else { rB.template go<2 * (g - 32)>(l16); rB.template go<2 * (g - 32) + 1>(l16); }
        }
      }
      SB();
    });
    { const u32 lo_ = 65536 + tt * 4096, hi_ = (tt < 2 ? 106496 : 131072) + tt * 4096; sw = (kh ? hi_ : lo_) + l16; sr = (kh ? lo_ : hi_) + l16; }
    xwrite(2);
    BAR();
    xread(); xadd(2, 0); xadd(2, 1);
#pragma unroll
    for (int o = 0; o < 4; ++o)
#pragma unroll
      for (int ph = 0; ph < 3; ++ph) epi(2, o, ph);
    ostore(2);
    WAITVM(0);
    BAR();
    if (tid == 0) signal(ws, WS_CNT + (32 + mt) * 256);
#pragma unroll
    for (int m = 0; m < 12; ++m) { PIN(a[m]); st128(rS.lds + m * 1024 + l16, a[m]); }
  }
  {
    PHASE();
    const bool old4 = w < 4;
    const char* wdb = ws + WS_WDF + (size_t)n8 * WDB;
    const int tt64 = mt * 4 + 2 * th + (tt >> 1);
    const char* ab = ws + WS_ACT + (size_t)(tt64 * 96 + 48 * kh) * 1024;
    auto iss = [&](auto s_) LAM { constexpr int s = decltype(s_)::v; gld_sc1<(s & 1) * 2048>(a[s], ab + (s >> 1) * 4096, avoff); };
#pragma unroll
    for (int m = 0; m < 5; ++m) c5[m] = f32x4{0.f, 0.f, 0.f, 0.f};
    const Run<50, 4, true> rE(wdb + 153600, 0, w & 3);
    const Run<40, 4, true> rF(wdb + 204800, 51200, w & 3);
    const u32 ew = 92160 + (tt * 5) * 1024 + l16;
    u32 oo = (u32)(128 * mt + 64 * th + 16 * tt + c16) * 1280 + (80 * n8 + 48 * kh + 4 * g4) * 2;
    u32 mo = L_MODS + 640 + kh * 192 + g4 * 16;
    PIN(oo); PIN(mo);
    if (w == 0) poll_ge(ws, WS_CNT + (32 + mt) * 256, 16u);
    BAR();
    sfor<0, 6>(iss);
    u32 wb2[5], wb3[5];
#pragma unroll
    for (int m = 0; m < 5; ++m) { wb2[m] = wbase[m] + 61440; wb3[m] = wbase[m] + 122880; PIN(wb2[m]); PIN(wb3[m]); }
    auto nextA = [&](auto s_) LAM {
      constexpr int s = decltype(s_)::v;
      if constexpr (dn_wait(s, true) == dn_wait(s, false)) WAITV(dn_wait(s, true)); else { if (old4) WAITV(dn_wait(s, true)); else WAITV(dn_wait(s, false)); }
      pin4(a[s]);
    };
    BAR();
    u32x4 wf[3][5];
#pragma unroll
    for (int u = 0; u < 2; ++u)
#pragma unroll
      for (int m = 0; m < 5; ++m) wf[u][m] = lds128(wbase[m] + wd_slot(u));
    nextA(IC<0>{});
    sfor<0, 24>([&](auto s_) LAM {
      constexpr int s = decltype(s_)::v;
      if (s == 5 || s == 9 || s == 13 || s == 18) BAR();
      if constexpr (s + 2 < 24) {
        constexpr u32 so = wd_slot(s + 2);
#pragma unroll
        for (int m = 0; m < 5; ++m) wf[(s + 2) % 3][m] = lds128((so < 61440 ? wbase[m] : so < 122880 ? wb2[m] : wb3[m]) + so % 61440);
      }
#pragma unroll
      for (int m = 0; m < 5; ++m) c5[m] = MFMA16(wf[s % 3][m], a[s], c5[m]);
      SB();
      if constexpr (s >= 5 && s < 14) {
        if (old4) {
          if constexpr (s < 9) { sfor<0, 4>([&](auto u_) LAM { rE.template go<4 * (s - 5) + decltype(u_)::v>(l16); }); }
          else { rF.template go<2 * (s - 9)>(l16); rF.template go<2 * (s - 9) + 1>(l16); }
        }
      }
      sfor<dn_cumA(s), dn_cumA(s + 1)>(iss);
      if constexpr (s + 1 < 24) nextA(IC<s + 1>{});
      SB();
    });
    stf4(ew + ((3 + 3 * kh) % 5) * 1024, c5[3]);
    stf4(ew + ((4 + 3 * kh) % 5) * 1024, c5[4]);
    if (kh) stf4(ew, c5[2]);
    BAR();
    u32x2 rv[3];
#pragma unroll
    for (int m = 0; m < 3; ++m)
      if (m < 2 || kh == 0) {
        const f32x4 o = ldsf4(ew + kh * 3072 + m * 1024), gb = ldsf4(mo + m * 64);
        const u32 y01 = pk2(c5[m][0] + o[0], c5[m][1] + o[1]), y23 = pk2(c5[m][2] + o[2], c5[m][3] + o[3]);
        const u32 t01 = pk2(gb[0] * lo16(y01), gb[1] * hi16(y01)), t23 = pk2(gb[2] * lo16(y23), gb[3] * hi16(y23));
        rv[m] = u32x2{pk2(lo16(x1k[m].x) + lo16(t01), hi16(x1k[m].x) + hi16(t01)), pk2(lo16(x1k[m].y) + lo16(t23), hi16(x1k[m].y) + hi16(t23))};
        if (m == 1) {
          const auto x0 = __builtin_amdgcn_permlane16_swap(rv[0].x, rv[1].x, false, false), x1 = __builtin_amdgcn_permlane16_swap(rv[0].y, rv[1].y, false, false);
          *(GAS u32x4*)((GAS char*)p.out + oo + (g4 & 1) * 24) = u32x4{x0[0], x1[0], x0[1], x1[1]};
        }
        if (m == 2) *(GAS u32x2*)((GAS char*)p.out + oo + 64) = rv[2];
      }
  }
}
}

extern "C" void kernel_launch(void* const* d_in, const int* in_sizes, int n_in, void* d_out, int out_size, void* d_ws, size_t ws_size,
                              hipStream_t stream) {
  (void)in_sizes; (void)n_in; (void)out_size; (void)ws_size;
  (void)hipFuncSetAttribute((const void*)k2, hipFuncAttributeMaxDynamicSharedMemorySize, LDS_BYTES);
  P1 q;
  q.x = (const u16*)d_in[0]; q.wqkv = (const u16*)d_in[1]; q.wo = (const u16*)d_in[2];
  q.cvec = (const u16*)d_in[7]; q.wmod = (const u16*)d_in[8]; q.ws = (char*)d_ws;
  k1<<<K1_BLOCKS, 256, 0, stream>>>(q);
  P2 p;
  p.x = (const u16*)d_in[0]; p.wg = (const u16*)d_in[4]; p.wu = (const u16*)d_in[5]; p.wd = (const u16*)d_in[6];
  p.out = (u16*)d_out; p.ws = (char*)d_ws;
  k2<<<256, 512, LDS_BYTES, stream>>>(p);
}
```
